```python
import torch

def _softmax(x, axis=-1):
    return torch.softmax(x, dim=axis)

def _gelu(x):
    return torch.nn.functional.gelu(x, approximate='tanh')

def _silu(x):
    return torch.nn.functional.silu(x)

def _sigmoid(x):
    return torch.sigmoid(x)

def _layernorm(x, axis=-1, eps=1e-05):
    m = x.mean(dim=axis, keepdim=True)
    v = x.var(dim=axis, keepdim=True, unbiased=False)
    return (x - m) / torch.sqrt(v + eps)

def _scatter_add(tgt, idx, src, axis=0):
    return tgt.clone().index_add_(axis, idx, src)

def _segment_sum(src, seg, n):
    out = torch.zeros((n,) + src.shape[1:], dtype=src.dtype)
    return out.index_add_(0, seg, src)

def _pad(x, pw):
    flat = tuple((v for pair in reversed(pw) for v in pair))
    return torch.nn.functional.pad(x, flat)
kernel_name = 'standalone__window_attn_davit__vision'

def setup_inputs():

    _w = lambda a: (lambda t: t.to(torch.bfloat16) if t.is_floating_point() else t)(torch.as_tensor(a))
    HW = 512
    C0 = 96
    ws = 7
    nh = 4
    H = W = max(HW // ws * ws, ws)
    C = max(C0 // nh * nh, nh)
    k = 3
    return {'x': _w((lambda *s: torch.randn(*s, dtype=torch.bfloat16) * 1.6)(1, H, W, C)), 'dw_pe': _w((lambda *s: torch.randn(*s, dtype=torch.bfloat16) * 1.6)(k, k, C) * 0.1), 'w_qkv': _w((lambda *s: torch.randn(*s, dtype=torch.bfloat16) * 1.6)(C, 3 * C) * C ** (-0.5)), 'w_o': _w((lambda *s: torch.randn(*s, dtype=torch.bfloat16) * 0.4)(C, C) * C ** (-0.5))}

def reference(x, dw_pe, w_qkv, w_o):
    B, H, W, C = x.shape
    ws = 7
    nh = 4
    dh = C // nh
    kk = dw_pe.shape[0]
    pad = kk // 2
    xp = _pad(x, ((0, 0), (pad, pad), (pad, pad), (0, 0)))
    pe = torch.zeros_like(x)
    for i in range(kk):
        for j in range(kk):
            pe = pe + xp[:, i:i + H, j:j + W, :] * dw_pe[i, j][None, None, None, :]
    x0 = x + pe
    h = _layernorm(x0, axis=-1)
    hw = h.reshape(B, H // ws, ws, W // ws, ws, C)
    hw = (lambda x, *d: x.permute(*d))(hw, 0, 1, 3, 2, 4, 5)
    hw = hw.reshape(-1, ws * ws, C)
    qkv = (hw @ w_qkv).reshape(-1, ws * ws, 3, nh, dh)
    q = qkv[:, :, 0].clone()
    k = qkv[:, :, 1].clone()
    v = qkv[:, :, 2].clone()
    scale = dh ** (-0.5)
    attn = torch.einsum('wnhd,wmhd->whnm', q, k) * scale
    attn = _softmax(attn, axis=-1)
    out = torch.einsum('whnm,wmhd->wnhd', attn, v)
    out = out.reshape(-1, ws * ws, C) @ w_o
    out = out.reshape(B, H // ws, W // ws, ws, ws, C)
    out = (lambda x, *d: x.permute(*d))(out, 0, 1, 3, 2, 4, 5)
    out = out.reshape(B, H, W, C)
    return x0 + out
```

```cpp
#include <hip/hip_runtime.h>
#include <hip/hip_bf16.h>
#include <stdint.h>

typedef __bf16 bf16x8 __attribute__((ext_vector_type(8)));
typedef __bf16 bf16x2 __attribute__((ext_vector_type(2)));
typedef float f32x4 __attribute__((ext_vector_type(4)));
typedef float f32x2 __attribute__((ext_vector_type(2)));
typedef unsigned u32x4 __attribute__((ext_vector_type(4), may_alias));
typedef unsigned u32x2 __attribute__((ext_vector_type(2), may_alias));
typedef short s16x4 __attribute__((ext_vector_type(4), may_alias));
typedef unsigned short u16a __attribute__((may_alias));
typedef unsigned u32a __attribute__((may_alias));

#define DI __device__ __forceinline__
#define AS3 __attribute__((address_space(3)))

constexpr unsigned IMG = 511, NW1 = 73;
constexpr unsigned PIXB = 192, ROWB = IMG * PIXB;
constexpr unsigned XBYTES = IMG * IMG * PIXB;
constexpr unsigned XSHIFT = (IMG + 1) * PIXB;

constexpr unsigned L_WQKV = 0;
constexpr unsigned L_WO = 61440;
constexpr unsigned L_CONV = 79872;
constexpr unsigned CONVF = 544;
constexpr unsigned L_SLAB = 96192;
constexpr unsigned SLABB = 5440;
constexpr unsigned L_REC = 139712;
constexpr unsigned RECB = 960;
constexpr unsigned L_PS = 159872;
constexpr unsigned L_ZERO = 160896;
constexpr unsigned L_JUNK = 161536;
constexpr unsigned L_FLAG = 161600;
constexpr unsigned L_TOTAL = 161616;

constexpr unsigned LROW = 38 * 16;
constexpr int LEFT_I = 2, LEFT_J = 3;

constexpr float SQC = 0.54266764f;
constexpr float ISQC = 1.0f / 0.54266764f;
constexpr float GUARD = 1152921504606846976.0f;

__shared__ __attribute__((aligned(16))) char g_lds[L_TOTAL];

DI f32x4 mfma(u32x4 a, u32x4 b, f32x4 c) {
  return __builtin_amdgcn_mfma_f32_16x16x32_bf16(__builtin_bit_cast(bf16x8, a), __builtin_bit_cast(bf16x8, b), c, 0, 0, 0);
}
DI unsigned cvtpk(float lo, float hi) {
  f32x2 v = {lo, hi};
  bf16x2 b = __builtin_convertvector(v, bf16x2);
  return __builtin_bit_cast(unsigned, b);
}
DI AS3 char* ldsp(unsigned off) { return (AS3 char*)g_lds + off; }
DI u32x4 ldr128(unsigned off) { return *(AS3 u32x4*)ldsp(off); }
DI unsigned ldr16(unsigned off) { return *(AS3 u16a*)ldsp(off); }
DI void ldw128(unsigned off, u32x4 v) { *(AS3 u32x4*)ldsp(off) = v; }
DI void ldw32(unsigned off, unsigned v) { *(AS3 u32a*)ldsp(off) = v; }
DI void ldw16(unsigned off, unsigned v) { *(AS3 u16a*)ldsp(off) = (unsigned short)v; }
DI float ex2(float x) { return __builtin_amdgcn_exp2f(x); }
DI float bf_lo(unsigned d) { return __builtin_bit_cast(float, d << 16); }
DI float bf_hi(unsigned d) { return __builtin_bit_cast(float, d & 0xffff0000u); }
DI unsigned rfl(unsigned v) { return __builtin_amdgcn_readfirstlane(v); }
#define CBAR() asm volatile("" ::: "memory")
#define OPQ(x) asm volatile("" : "+v"(x))
DI void wg_barrier() {
  asm volatile("s_waitcnt vmcnt(0) lgkmcnt(0)" ::: "memory");
  __builtin_amdgcn_s_barrier();
  CBAR();
}

DI unsigned tok_code(int t, unsigned n) {
  const unsigned long long lo = t == 0 ? 0x0c05180408010b00ull : t == 1 ? 0x1d16291519121c11ull : 0x343033262a232d22ull;
  const unsigned long long hi = t == 0 ? 0x0a0309020e100d06ull : t == 1 ? 0x1b141a2028211e24ull : 0x2c252b313632352eull;
  return (unsigned)(((n < 8 ? lo : hi) >> (8 * (n & 7))) & 0xff);
}

struct Lane {
  unsigned lane, n, g;
  unsigned cA[3], cB[3];
  unsigned cW;
  unsigned l16;
  unsigned l16o;
  unsigned ost[3];
  unsigned vo[6], vflag;
  unsigned bperm;
  unsigned pw, pr;
};

DI void dma_one(unsigned m0v, unsigned voff, __amdgpu_buffer_rsrc_t srd, unsigned soff) {
  asm volatile("s_mov_b32 m0, %0\n\ts_nop 0\n\tbuffer_load_dwordx4 %1, %2, %3 offen lds" ::"s"(m0v), "v"(voff), "s"(srd), "s"(soff) : "m0", "memory");
}
DI void dma_last(unsigned m0v, unsigned voff, __amdgpu_buffer_rsrc_t srd, unsigned soff) {
  asm volatile("s_mov_b32 m0, %0\n\ts_mov_b64 exec, 0xfffff\n\tbuffer_load_dwordx4 %1, %2, %3 offen lds\n\ts_mov_b64 exec, -1" ::"s"(m0v), "v"(voff), "s"(srd), "s"(soff)
               : "m0", "memory");
}
DI void dma_slab(const Lane& c, unsigned m0base, __amdgpu_buffer_rsrc_t srd, unsigned soff, unsigned bm) {
  asm volatile("s_waitcnt lgkmcnt(0)" ::: "memory");
  if (bm == 0) {
#pragma unroll
    for (int i = 0; i < 5; ++i) dma_one(m0base + 1024u * i, c.vo[i], srd, soff);
    dma_last(m0base + 5120u, c.vo[5], srd, soff);
  } else {
#pragma unroll
    for (int i = 0; i < 6; ++i) {
      const unsigned v = ((c.vflag >> (4 * i)) & bm) ? 0x80000000u : c.vo[i];
      if (i < 5) dma_one(m0base + 1024u * i, v, srd, soff);
      else dma_last(m0base + 5120u, v, srd, soff);
    }
  }
}

template <int S>
DI void conv_slab(const Lane& c, f32x4 (&xa)[3][6]) {
#pragma unroll
  for (int hh = 0; hh < 2; ++hh) {
    const int ct = 2 * S + hh;
#pragma unroll
    for (int tp = 0; tp < 5; ++tp) {
      const u32x4 A = ldr128(c.cW + (ct * 5 + tp) * CONVF);
#pragma unroll
      for (int t = 0; t < 3; ++t) {
        const unsigned off = (tp == 4 ? c.cB[t] : c.cA[t] + (tp == 3 ? LROW + 64u : 64u * tp)) + 32u * hh;
        const u32x4 B = ldr128(off);
        const f32x4 z = {0.f, 0.f, 0.f, 0.f};
        xa[t][ct] = mfma(A, B, tp == 0 ? z : xa[t][ct]);
      }
    }
  }
}

template <int NT>
DI void layer_norm(const Lane& c, const f32x4 (&xa)[NT][6], u32x4 (&x0p)[NT][3], u32x4 (&hp)[NT][3]) {
  const u32x4 ones = {0x3f803f80u, 0x3f803f80u, 0x3f803f80u, 0x3f803f80u};
  const unsigned q = c.n & 3;
#pragma unroll
  for (int t = 0; t < NT; ++t) {
#pragma unroll
    for (int s = 0; s < 3; ++s)
      x0p[t][s] = u32x4{cvtpk(xa[t][2 * s][0], xa[t][2 * s][1]), cvtpk(xa[t][2 * s][2], xa[t][2 * s][3]), cvtpk(xa[t][2 * s + 1][0], xa[t][2 * s + 1][1]),
                        cvtpk(xa[t][2 * s + 1][2], xa[t][2 * s + 1][3])};
    f32x4 sm = {0.f, 0.f, 0.f, 0.f}, sq = {0.f, 0.f, 0.f, 0.f};
#pragma unroll
    for (int s = 0; s < 3; ++s) {
      sm = mfma(ones, x0p[t][s], sm);
      sq = mfma(x0p[t][s], x0p[t][s], sq);
    }
    float d = q == 0 ? sq[0] : q == 1 ? sq[1] : q == 2 ? sq[2] : sq[3];
    d = __builtin_bit_cast(float, __builtin_amdgcn_ds_bpermute((int)c.bperm, __builtin_bit_cast(int, d)));
    const float mu = sm[0] * (1.0f / 96.0f);
    float var = __builtin_fmaf(d, 1.0f / 96.0f, -mu * mu);
    var = __builtin_fmaxf(var, 0.f);
    const float a = __builtin_amdgcn_rsqf(var + 1e-5f) * SQC;
    const float b = -mu * a;
#pragma unroll
    for (int s = 0; s < 3; ++s) {
      float h[8];
#pragma unroll
      for (int e = 0; e < 8; ++e) h[e] = __builtin_fmaf(xa[t][2 * s + (e >> 2)][e & 3], a, b);
      hp[t][s] = u32x4{cvtpk(h[0], h[1]), cvtpk(h[2], h[3]), cvtpk(h[4], h[5]), cvtpk(h[6], h[7])};
    }
  }
}

template <int NT>
DI void qkv_head(const Lane& c, unsigned wbase, const u32x4 (&hp)[NT][3], u32x4 (&Q)[NT], u32x4 (&K)[NT], f32x4 (&vacc)[2][NT]) {
  const f32x4 z = {0.f, 0.f, 0.f, 0.f};
  f32x4 T[3][NT];
#pragma unroll
  for (int T_ = 0; T_ < 3; ++T_) {
#pragma unroll
    for (int s = 0; s < 3; ++s) {
      const u32x4 A = ldr128(wbase + (T_ * 3 + s) * 1024);
#pragma unroll
      for (int t = 0; t < NT; ++t) T[T_][t] = mfma(A, hp[t][s], s == 0 ? z : T[T_][t]);
    }
  }
#pragma unroll
  for (int t = 0; t < NT; ++t) {
    Q[t] = u32x4{cvtpk(T[0][t][0], T[0][t][1]), cvtpk(T[0][t][2], T[0][t][3]), cvtpk(T[1][t][0], T[1][t][1]), 0u};
    K[t] = u32x4{cvtpk(T[2][t][0], T[2][t][1]), cvtpk(T[2][t][2], T[2][t][3]), cvtpk(T[1][t][2], T[1][t][3]), 0u};
  }
  const float one = (c.n & 3) == 2 ? 1.0f : 0.0f;
  const f32x4 vone = {one, one, one, one};
#pragma unroll
  for (int cc = 0; cc < 2; ++cc) {
#pragma unroll
    for (int s = 0; s < 3; ++s) {
      const u32x4 B = ldr128(wbase + ((3 + cc) * 3 + s) * 1024);
#pragma unroll
      for (int t = 0; t < NT; ++t) vacc[cc][t] = mfma(hp[t][s], B, s == 0 ? (cc == 0 ? z : vone) : vacc[cc][t]);
    }
  }
}

DI float max_g(float m) {
  m = __builtin_fmaxf(m, __shfl_xor(m, 16));
  m = __builtin_fmaxf(m, __shfl_xor(m, 32));
  return m;
}

template <bool SAFE>
DI void attn_head(const Lane& c, const u32x4 (&Q)[3], const u32x4 (&K)[4], const u32x4 (&V)[2][2], const u32x4 Q3, unsigned oaddr, unsigned (&od)[3][3],
                  float& guard) {
  const f32x4 z = {0.f, 0.f, 0.f, 0.f};
#pragma unroll
  for (int jq = 0; jq < 3; ++jq) {
    f32x4 S[4];
#pragma unroll
    for (int i = 0; i < 4; ++i) S[i] = mfma(K[i], Q[jq], z);
    if (SAFE) {
      float m = S[3][0];
#pragma unroll
      for (int i = 0; i < 3; ++i)
#pragma unroll
        for (int r = 0; r < 4; ++r) m = __builtin_fmaxf(m, S[i][r]);
      m = max_g(m);
#pragma unroll
      for (int i = 0; i < 3; ++i)
#pragma unroll
        for (int r = 0; r < 4; ++r) S[i][r] -= m;
      S[3][0] -= m;
    }
#pragma unroll
    for (int i = 0; i < 3; ++i)
#pragma unroll
      for (int r = 0; r < 4; ++r) S[i][r] = ex2(S[i][r]);
    const float p3 = ex2(S[3][0]);
    const u32x4 P0 = {cvtpk(S[0][0], S[0][1]), cvtpk(S[0][2], S[0][3]), cvtpk(S[1][0], S[1][1]), cvtpk(S[1][2], S[1][3])};
    const u32x4 P1 = {cvtpk(S[2][0], S[2][1]), cvtpk(S[2][2], S[2][3]), cvtpk(p3, 0.f), 0u};
    f32x4 O0 = mfma(V[0][0], P0, z), O1 = mfma(V[0][1], P0, z);
    O0 = mfma(V[1][0], P1, O0);
    O1 = mfma(V[1][1], P1, O1);
    const float l = O1[2], r = __builtin_amdgcn_rcpf(l);
    if (!SAFE) guard = __builtin_fmaxf(guard, __builtin_fmaxf(l, r));
    const float f = r * ISQC;
    od[jq][0] = cvtpk(O0[0] * f, O0[1] * f);
    od[jq][1] = cvtpk(O0[2] * f, O0[3] * f);
    od[jq][2] = cvtpk(O1[0] * f, O1[1] * f);
  }
  {
    f32x4 D[4];
#pragma unroll
    for (int i = 0; i < 4; ++i) D[i] = mfma(Q3, K[i], z);
    float s = c.g == 0 ? D[0][0] : c.g == 1 ? D[1][0] : c.g == 2 ? D[2][0] : D[3][0];
    if (SAFE) {
      float m = s;
#pragma unroll
      for (int o = 1; o < 64; o <<= 1) m = __builtin_fmaxf(m, __shfl_xor(m, o));
      s -= m;
    }
    const float p = ex2(s);
    ldw16(c.pw, cvtpk(p, p));
    const u32x4 P0 = ldr128(c.pr), P1 = ldr128(c.pr + 64);
    f32x4 O0 = mfma(V[0][0], P0, z), O1 = mfma(V[0][1], P0, z);
    O0 = mfma(V[1][0], P1, O0);
    O1 = mfma(V[1][1], P1, O1);
    const float l = O1[2], r = __builtin_amdgcn_rcpf(l);
    if (!SAFE) guard = __builtin_fmaxf(guard, __builtin_fmaxf(l, r));
    const float f = r * ISQC;
    const unsigned d0 = cvtpk(O0[0] * f, O0[1] * f), d1 = cvtpk(O0[2] * f, O0[3] * f), d2 = cvtpk(O1[0] * f, O1[1] * f);
    const unsigned q = c.n & 3;
    ldw32(oaddr, q == 0 ? d0 : q == 1 ? d1 : d2);
  }
}

template <int CT0>
DI void proj_part(const Lane& c, const u32x4 (&x0p)[3][3], const u32x4 (&Op)[3][3], __amdgpu_buffer_rsrc_t rout, unsigned wbase) {
#pragma unroll
  for (int ct = CT0; ct < CT0 + 3; ++ct) {
    u32x4 A[3];
#pragma unroll
    for (int s = 0; s < 3; ++s) A[s] = ldr128(c.l16o + (ct * 3 + s) * 1024);
#pragma unroll
    for (int t = 0; t < 3; ++t) {
      const unsigned d0 = x0p[t][ct >> 1][2 * (ct & 1)], d1 = x0p[t][ct >> 1][2 * (ct & 1) + 1];
      f32x4 acc = {bf_lo(d0), bf_hi(d0), bf_lo(d1), bf_hi(d1)};
#pragma unroll
      for (int s = 0; s < 3; ++s) acc = mfma(A[s], Op[t][s], acc);
      const u32x2 o = {cvtpk(acc[0], acc[1]), cvtpk(acc[2], acc[3])};
      __builtin_amdgcn_raw_buffer_store_b64(o, rout, c.ost[t] + ct * 32, wbase, 0);
    }
  }
}

template <int HD>
DI void head_all(const Lane& c, const u32x4 (&hp)[3][3], unsigned recg, unsigned recv, unsigned reco, u32x4 (&Op)[3][3]) {
  u32x4 Q[3], K[4], Kt[3], V[2][2];
  f32x4 va[2][3];
  qkv_head<3>(c, c.l16 + L_WQKV + HD * 15 * 1024, hp, Q, Kt, va);
  K[0] = Kt[0]; K[1] = Kt[1]; K[2] = Kt[2];
  K[3] = ldr128(recg + HD * 192);
  const u32x4 Q3 = ldr128(recg + HD * 192 + 64);
#pragma unroll
  for (int cc = 0; cc < 2; ++cc) {
    V[0][cc] = u32x4{cvtpk(va[cc][0][0], va[cc][0][1]), cvtpk(va[cc][0][2], va[cc][0][3]), cvtpk(va[cc][1][0], va[cc][1][1]), cvtpk(va[cc][1][2], va[cc][1][3])};
    V[1][cc] = u32x4{cvtpk(va[cc][2][0], va[cc][2][1]), cvtpk(va[cc][2][2], va[cc][2][3]), ldr16(recv + HD * 192 + cc * 32), 0u};
  }
  unsigned od[3][3];
  float guard = 0.f;
  attn_head<false>(c, Q, K, V, Q3, reco + HD * 192, od, guard);
  if (__builtin_expect(__builtin_amdgcn_ballot_w64(!(guard < GUARD)) != 0, 0)) {
    attn_head<true>(c, Q, K, V, Q3, reco + HD * 192, od, guard);
  }
#pragma unroll
  for (int t = 0; t < 3; ++t)
#pragma unroll
    for (int i = 0; i < 3; ++i) Op[t][(3 * HD + i) >> 2][(3 * HD + i) & 3] = od[t][i];
}

DI unsigned win_index(unsigned base_x, unsigned j, unsigned rho, unsigned w) { return base_x + (rho < 2 ? rho * 256 + j * 8 + w : 512 + 32 * w + j); }

DI void spin_until(unsigned off, unsigned target) {
  while (rfl(*(volatile AS3 unsigned*)ldsp(off)) < target) __builtin_amdgcn_s_sleep(2);
  CBAR();
}

DI void tap_of(unsigned tp, unsigned hi, int& dy, int& dx) {
  if (tp < 3) { dy = hi ? 0 : -1; dx = (int)tp - 1; }
  else if (tp == 3) { dy = hi ? 1 : 0; dx = 0; }
  else { dy = 1; dx = hi ? 1 : -1; }
}

__global__ __launch_bounds__(512, 2) __attribute__((target("no-packed-fp32-ops")))
void wattn_kernel(const uint16_t* __restrict__ x, const uint16_t* __restrict__ dw, const uint16_t* __restrict__ wqkv, const uint16_t* __restrict__ wo,
                  uint16_t* __restrict__ out) {
  const unsigned tid = threadIdx.x;
  const unsigned wv = rfl(tid >> 6);
  Lane c;
  c.lane = tid & 63; c.n = c.lane & 15; c.g = c.lane >> 4;
  const unsigned lane = c.lane, n = c.n, g = c.g;
  const unsigned xcd = blockIdx.x & 7, jw = blockIdx.x >> 3;
  const unsigned base_x = xcd * 666 + (xcd ? 1 : 0), cnt_x = 666 + (xcd == 0 ? 1 : 0);
  const unsigned n2 = 4 + ((640 + jw < cnt_x) ? 1 : 0);
  const unsigned nW = 16 + n2;
  const unsigned nwin = wv < n2 ? 3 : 2;
  const unsigned lds0 = (unsigned)(size_t)(AS3 char*)g_lds;
  const unsigned slab = L_SLAB + wv * SLABB;

#pragma unroll
  for (int t = 0; t < 3; ++t) {
    const unsigned code = tok_code(t, n), i = code >> 3, j = code & 7;
    c.cA[t] = slab + LROW * (i + (g >> 1)) + 64 * j + 16 * (g & 1);
    c.cB[t] = slab + LROW * (i + 2) + 64 * (j + 2 * (g >> 1)) + 16 * (g & 1);
    c.ost[t] = (i * IMG + j) * PIXB + g * 8;
  }
  {
    const bool act = (g & 1) == (n >> 3);
    const unsigned G = 2 * (g >> 1) + ((n >> 2) & 1), idx = 4 * (g & 1) + (n & 3);
    const unsigned k = (G == 0 ? 0 : G == 1 ? 9 : G == 2 ? 17 : 26) + idx;
    c.cW = L_CONV + 16 * (act ? k : (g < 2 ? 8 : 25));
  }
  c.l16 = lane * 16; c.l16o = L_WO + lane * 16;
  c.vflag = 0;
#pragma unroll
  for (int i = 0; i < 6; ++i) {
    const unsigned sl = 64 * i + lane, r = sl / 38, t = sl % 38, cc = t >> 2, q = t & 3;
    const bool ok = t < 36 && sl < 340;
    c.vo[i] = ok ? (r * IMG + cc) * PIXB + q * 16 : 0x80000000u;
    c.vflag |= ((r == 0 ? 1u : 0u) | (r == 8 ? 2u : 0u) | (cc == 0 ? 4u : 0u) | (cc == 8 ? 8u : 0u)) << (4 * i);
  }
  c.bperm = 4 * ((n >> 2) * 16 + n);
  c.pw = lane < 49 ? L_PS + wv * 128 + (g >> 1) * 64 + (n >> 2) * 16 + (g & 1) * 8 + (n & 3) * 2 : L_JUNK + (lane - 49) * 2;
  c.pr = L_PS + wv * 128 + g * 16;

  const __amdgpu_buffer_rsrc_t rx = __builtin_amdgcn_make_buffer_rsrc((void*)((const char*)x - XSHIFT), (short)0, (int)(XBYTES + XSHIFT), 0x00020000);
  const __amdgpu_buffer_rsrc_t rw = __builtin_amdgcn_make_buffer_rsrc((void*)wqkv, (short)0, 96 * 288 * 2, 0x00020000);
  const __amdgpu_buffer_rsrc_t rout = __builtin_amdgcn_make_buffer_rsrc((void*)out, (short)0, (int)XBYTES, 0x00020000);
  const unsigned m0slab = rfl(lds0 + slab);

#pragma unroll
  for (int i = 0; i < 7; ++i) {
    const unsigned q = wv + 8 * i;
    if (q < 54) dma_one(rfl(lds0 + L_WQKV + q * 1024), c.l16, rw, q * 1024);
  }
  unsigned widx = win_index(base_x, jw, 0, wv);
  unsigned wr = widx / NW1, wc = widx % NW1;
  unsigned wbase = (7 * wr * IMG + 7 * wc) * PIXB;
  unsigned bm = (wr == 0 ? 1u : 0u) | (wr == NW1 - 1 ? 2u : 0u) | (wc == 0 ? 4u : 0u) | (wc == NW1 - 1 ? 8u : 0u);
  dma_slab(c, m0slab, rx, wbase, bm);

  u32x4 wof[3];
#pragma unroll
  for (int i = 0; i < 3; ++i) {
    const unsigned f = wv + 8 * i;
    if (f < 18) {
      const unsigned ct = f / 3, s = f % 3;
      unsigned v[8];
#pragma unroll
      for (int e = 0; e < 8; ++e) {
        const unsigned sq = 8 * s + e, hd = sq / 6, ix = sq % 6;
        const unsigned d = ix < 4 ? 4 * g + ix : 16 + 2 * g + ix - 4;
        v[e] = wo[(hd * 24 + d) * 96 + 16 * ct + n];
      }
      wof[i] = u32x4{v[0] | (v[1] << 16), v[2] | (v[3] << 16), v[4] | (v[5] << 16), v[6] | (v[7] << 16)};
    }
  }
  u32x4 cvf[2];
#pragma unroll
  for (int i = 0; i < 2; ++i) {
    const unsigned sl = tid + 512 * i;
    u32x4 v = {0u, 0u, 0u, 0u};
    if (sl < 1020) {
      const unsigned f = sl / 34, k = sl % 34, ct = f / 5, tp = f % 5;
      if (k != 8 && k != 25) {
        const unsigned G = k < 8 ? 0 : k < 17 ? 1 : k < 25 ? 2 : 3;
        const unsigned idx = k - (G == 0 ? 0 : G == 1 ? 9 : G == 2 ? 17 : 26);
        const unsigned hi = G >> 1, m = 8 * (idx >> 2) + 4 * (G & 1) + (idx & 3);
        int dy, dx;
        tap_of(tp, hi, dy, dx);
        unsigned w = dw[((dy + 1) * 3 + dx + 1) * 96 + 16 * ct + m];
        if (tp == 3 && hi == 0) w = 0x3f80u;
        const unsigned e = m & 7, dd = w << (16 * (e & 1));
        v = u32x4{(e >> 1) == 0 ? dd : 0u, (e >> 1) == 1 ? dd : 0u, (e >> 1) == 2 ? dd : 0u, (e >> 1) == 3 ? dd : 0u};
      }
    }
    cvf[i] = v;
  }
  const bool lw = wv >= 6;
  const unsigned lt = wv == 7 ? 0 : 1;
  const unsigned lwl = 16 * lt + n;
  const bool lvalid = lwl < nW;
  unsigned lpix = 0;
  u32x4 lb[30];
  if (lw) {
    const unsigned wl = lvalid ? lwl : 0;
    const unsigned wi = win_index(base_x, jw, wl >> 3, wl & 7);
    const unsigned r = wi / NW1, cc = wi % NW1;
    lpix = ((7 * r + LEFT_I) * IMG + 7 * cc + LEFT_J) * PIXB;
#pragma unroll
    for (int f = 0; f < 30; ++f) {
      int dy, dx;
      tap_of(f % 5, g >> 1, dy, dx);
      lb[f] = *(const u32x4*)((const char*)x + lpix + (dy * (int)IMG + dx) * (int)PIXB + (f / 5) * 32 + (g & 1) * 16);
    }
  }
  asm volatile("s_waitcnt vmcnt(0)" ::: "memory");
  wg_barrier();
  {
    u32x4 fr[8];
#pragma unroll
    for (int i = 0; i < 8; ++i) {
      const unsigned f = wv + 8 * i;
      if (f < 60) {
        const unsigned hd = f / 15, T = (f % 15) / 3, s = f % 3;
        if (T == 0 || T == 2 || T == 3) {
          const unsigned col0 = (T == 0 ? 0 : T == 2 ? 96 : 192) + hd * 24;
          const unsigned a = L_WQKV + (32 * s + 4 * g + (n >> 2)) * 576 + (col0 + 4 * (n & 3)) * 2;
          const s16x4 lo = __builtin_amdgcn_ds_read_tr16_b64_v4i16((AS3 s16x4*)ldsp(a));
          const s16x4 hi = __builtin_amdgcn_ds_read_tr16_b64_v4i16((AS3 s16x4*)ldsp(a + 16 * 576));
          const u32x2 l2 = __builtin_bit_cast(u32x2, lo), h2 = __builtin_bit_cast(u32x2, hi);
          fr[i] = u32x4{l2[0], l2[1], h2[0], h2[1]};
        } else {
          const unsigned gp = n >> 2, r = n & 3;
          const unsigned col = T == 1 ? (r < 2 ? 0 : 96) + hd * 24 + 16 + 2 * gp + (r & 1) : 192 + hd * 24 + 16 + 2 * gp + (r & 1);
          const bool zero = T == 4 && r >= 2;
          unsigned v[8];
#pragma unroll
          for (int e = 0; e < 8; ++e) {
            const unsigned k = 32 * s + (e < 4 ? 4 * g + e : 16 + 4 * g + e - 4);
            v[e] = zero ? 0u : ldr16(L_WQKV + k * 576 + col * 2);
          }
          fr[i] = u32x4{v[0] | (v[1] << 16), v[2] | (v[3] << 16), v[4] | (v[5] << 16), v[6] | (v[7] << 16)};
        }
      }
    }
    wg_barrier();
#pragma unroll
    for (int i = 0; i < 8; ++i) {
      const unsigned f = wv + 8 * i;
      if (f < 60) ldw128(L_WQKV + f * 1024 + c.l16, fr[i]);
    }
#pragma unroll
    for (int i = 0; i < 3; ++i) {
      const unsigned f = wv + 8 * i;
      if (f < 18) ldw128(c.l16o + f * 1024, wof[i]);
    }
#pragma unroll
    for (int i = 0; i < 2; ++i) {
      const unsigned sl = tid + 512 * i;
      if (sl < 1020) ldw128(L_CONV + sl * 16, cvf[i]);
    }
    if (tid < (L_TOTAL - L_PS) / 4) ldw32(L_PS + tid * 4, 0u);
    wg_barrier();
  }

  if (lw) {
    f32x4 xa[1][6];
#pragma unroll
    for (int ct = 0; ct < 6; ++ct) {
      f32x4 acc = {0.f, 0.f, 0.f, 0.f};
#pragma unroll
      for (int tp = 0; tp < 5; ++tp) acc = mfma(ldr128(c.cW + (ct * 5 + tp) * CONVF), lb[ct * 5 + tp], acc);
      xa[0][ct] = acc;
    }
    u32x4 x0p[1][3], hp[1][3];
    layer_norm<1>(c, xa, x0p, hp);
    const unsigned rec = L_REC + lwl * RECB;
    if (lvalid) {
#pragma unroll
      for (int s = 0; s < 3; ++s) ldw128(rec + 768 + g * 48 + s * 16, x0p[0][s]);
    }
#pragma unroll
    for (int hd = 0; hd < 4; ++hd) {
      u32x4 Q[1], K[1];
      f32x4 va[2][1];
      qkv_head<1>(c, c.l16 + L_WQKV + hd * 15 * 1024, hp, Q, K, va);
      if (lvalid) {
        ldw128(rec + hd * 192 + g * 16, K[0]);
        ldw128(rec + hd * 192 + 64 + g * 16, Q[0]);
      }
#pragma unroll
      for (int cc = 0; cc < 2; ++cc)
#pragma unroll
        for (int r = 0; r < 4; ++r) {
          const unsigned wl = 16 * lt + 4 * g + r;
          if (wl < nW) ldw16(L_REC + wl * RECB + hd * 192 + 128 + cc * 32 + n * 2, cvtpk(va[cc][0][r], 0.f));
        }
    }
    asm volatile("s_waitcnt lgkmcnt(0)" ::: "memory");
    if (lane == 0) *(volatile AS3 unsigned*)ldsp(L_FLAG + 4 * lt) = 1u;
    CBAR();
  }

  u32x4 x0p[3][3], Op[3][3];
  unsigned pbase = 0;
#pragma unroll 1
  for (unsigned rho = 0; rho < nwin; ++rho) {
    f32x4 xa[3][6];
#pragma unroll
    for (int t = 0; t < 3; ++t) { OPQ(c.cA[t]); OPQ(c.cB[t]); OPQ(c.ost[t]); }
    OPQ(c.cW); OPQ(c.l16); OPQ(c.l16o); OPQ(c.pw); OPQ(c.pr); OPQ(c.n); OPQ(c.g);
    asm volatile("s_waitcnt vmcnt(0)" ::: "memory");
    conv_slab<0>(c, xa);
    dma_slab(c, m0slab, rx, wbase + 64, bm);
    if (rho > 0) {
      proj_part<0>(c, x0p, Op, rout, pbase);
      asm volatile("s_waitcnt vmcnt(9)" ::: "memory");
    } else {
      asm volatile("s_waitcnt vmcnt(0)" ::: "memory");
    }
    conv_slab<1>(c, xa);
    dma_slab(c, m0slab, rx, wbase + 128, bm);
    if (rho > 0) {
      proj_part<3>(c, x0p, Op, rout, pbase);
      asm volatile("s_waitcnt vmcnt(9)" ::: "memory");
    } else {
      asm volatile("s_waitcnt vmcnt(0)" ::: "memory");
    }
    conv_slab<2>(c, xa);
    pbase = wbase;
    const unsigned wl = rho * 8 + wv;
    if (rho + 1 < nwin) {
      widx = win_index(base_x, jw, rho + 1, wv);
      wr = widx / NW1; wc = widx % NW1;
      wbase = (7 * wr * IMG + 7 * wc) * PIXB;
      bm = (wr == 0 ? 1u : 0u) | (wr == NW1 - 1 ? 2u : 0u) | (wc == 0 ? 4u : 0u) | (wc == NW1 - 1 ? 8u : 0u);
      dma_slab(c, m0slab, rx, wbase, bm);
    }
    u32x4 hp[3][3];
    layer_norm<3>(c, xa, x0p, hp);
    if (rho != 1) spin_until(L_FLAG + 4 * (rho >> 1), 1u);
    const unsigned rec = L_REC + wl * RECB;
    const unsigned recg = rec + g * 16;
    const unsigned recv = g == 0 ? rec + 128 + n * 2 : L_ZERO;
    const unsigned reco = rec + 64 + g * 16 + (n & 3) * 4;
    head_all<0>(c, hp, recg, recv, reco, Op);
    head_all<1>(c, hp, recg, recv, reco, Op);
    head_all<2>(c, hp, recg, recv, reco, Op);
    head_all<3>(c, hp, recg, recv, reco, Op);
    CBAR();
    if (lane == 0) __hip_atomic_fetch_add((AS3 unsigned*)ldsp(L_FLAG + 8 + 4 * (rho >> 1)), 1u, __ATOMIC_RELAXED, __HIP_MEMORY_SCOPE_WORKGROUP);
    CBAR();
  }
  proj_part<0>(c, x0p, Op, rout, pbase);
  proj_part<3>(c, x0p, Op, rout, pbase);

  if (lw) {
    spin_until(L_FLAG + 8 + 4 * lt, lt == 0 ? 16u : n2);
    const unsigned rec = L_REC + (lvalid ? lwl : 16 * lt) * RECB;
    u32x4 oh[4], xp[3];
#pragma unroll
    for (int hd = 0; hd < 4; ++hd) oh[hd] = ldr128(rec + hd * 192 + 64 + g * 16);
#pragma unroll
    for (int s = 0; s < 3; ++s) xp[s] = ldr128(rec + 768 + g * 48 + s * 16);
    const u32x4 O3[3] = {u32x4{oh[0][0], oh[0][1], oh[0][2], oh[1][0]}, u32x4{oh[1][1], oh[1][2], oh[2][0], oh[2][1]}, u32x4{oh[2][2], oh[3][0], oh[3][1], oh[3][2]}};
#pragma unroll
    for (int ct = 0; ct < 6; ++ct) {
      const unsigned d0 = xp[ct >> 1][2 * (ct & 1)], d1 = xp[ct >> 1][2 * (ct & 1) + 1];
      f32x4 acc = {bf_lo(d0), bf_hi(d0), bf_lo(d1), bf_hi(d1)};
#pragma unroll
      for (int s = 0; s < 3; ++s) acc = mfma(ldr128(c.l16o + (ct * 3 + s) * 1024), O3[s], acc);
      const u32x2 o = {cvtpk(acc[0], acc[1]), cvtpk(acc[2], acc[3])};
      __builtin_amdgcn_raw_buffer_store_b64(o, rout, lvalid ? lpix + g * 8 + ct * 32 : 0x80000000u, 0, 0);
    }
  }
}

extern "C" void kernel_launch(void* const* d_in, const int* in_sizes, int n_in, void* d_out, int out_size, void* d_ws, size_t ws_size, hipStream_t stream) {
  (void)in_sizes; (void)n_in; (void)out_size; (void)d_ws; (void)ws_size;
  wattn_kernel<<<256, 512, 0, stream>>>((const uint16_t*)d_in[0], (const uint16_t*)d_in[1], (const uint16_t*)d_in[2], (const uint16_t*)d_in[3], (uint16_t*)d_out);
}
```

```cpp
#include <hip/hip_runtime.h>
#include <hip/hip_bf16.h>
#include <stdint.h>

typedef __bf16 bf16x8 __attribute__((ext_vector_type(8)));
typedef __bf16 bf16x2 __attribute__((ext_vector_type(2)));
typedef float f32x4 __attribute__((ext_vector_type(4)));
typedef float f32x2 __attribute__((ext_vector_type(2)));
typedef unsigned u32x4 __attribute__((ext_vector_type(4), may_alias));
typedef unsigned u32x2 __attribute__((ext_vector_type(2), may_alias));
typedef short s16x4 __attribute__((ext_vector_type(4), may_alias));
typedef unsigned short u16a __attribute__((may_alias));
typedef unsigned u32a __attribute__((may_alias));

#define DI __device__ __forceinline__
#define AS3 __attribute__((address_space(3)))

constexpr unsigned IMG = 511, NW1 = 73;
constexpr unsigned PIXB = 192, ROWB = IMG * PIXB;
constexpr unsigned XBYTES = IMG * IMG * PIXB;
constexpr unsigned XSHIFT = (IMG + 1) * PIXB;

constexpr unsigned L_WQKV = 0;
constexpr unsigned L_X2 = 49152;
constexpr unsigned L_WO = 55296;
constexpr unsigned L_TAB = 73728;
constexpr unsigned L_ID = 76800;
constexpr unsigned L_CONV = 78848;
constexpr unsigned CONVF = 544;
constexpr unsigned L_SLAB = 95168;
constexpr unsigned SLABB = 5440;
constexpr unsigned L_REC = 138688;
constexpr unsigned RECB = 960;
constexpr unsigned L_PS = 158848;
constexpr unsigned L_ZERO = 159872;
constexpr unsigned L_FLAG = 160512;
constexpr unsigned L_JUNK = 160768;
constexpr unsigned L_TOTAL = 161792;
constexpr unsigned TABO = L_TAB - L_WO;
constexpr unsigned IDO = L_ID - L_WO;

constexpr unsigned LROW = 38 * 16;
constexpr int LEFT_I = 2, LEFT_J = 3;

constexpr float SQC = 0.54266764f;
constexpr float ISQC = 1.0f / 0.54266764f;
constexpr float GUARD_HI = 1152921504606846976.0f;
constexpr float GUARD_LO = 1.0f / 1152921504606846976.0f;
constexpr unsigned ONES = 0x3f803f80u;

__shared__ __attribute__((aligned(16))) char g_lds[L_TOTAL];

DI f32x4 mfma(u32x4 a, u32x4 b, f32x4 c) {
  return __builtin_amdgcn_mfma_f32_16x16x32_bf16(__builtin_bit_cast(bf16x8, a), __builtin_bit_cast(bf16x8, b), c, 0, 0, 0);
}
DI unsigned cvtpk(float lo, float hi) {
  f32x2 v = {lo, hi};
  bf16x2 b = __builtin_convertvector(v, bf16x2);
  return __builtin_bit_cast(unsigned, b);
}
DI AS3 char* ldsp(unsigned off) { return (AS3 char*)g_lds + off; }
DI u32x4 ldr128(unsigned off) { return *(AS3 u32x4*)ldsp(off); }
DI unsigned ldr16(unsigned off) { return *(AS3 u16a*)ldsp(off); }
DI void ldw128(unsigned off, u32x4 v) { *(AS3 u32x4*)ldsp(off) = v; }
DI void ldw32(unsigned off, unsigned v) { *(AS3 u32a*)ldsp(off) = v; }
DI void ldw16(unsigned off, unsigned v) { *(AS3 u16a*)ldsp(off) = (unsigned short)v; }
DI float ex2(float x) { return __builtin_amdgcn_exp2f(x); }
DI float bf_lo(unsigned d) { return __builtin_bit_cast(float, d << 16); }
DI float bf_hi(unsigned d) { return __builtin_bit_cast(float, d & 0xffff0000u); }
DI unsigned rfl(unsigned v) { return __builtin_amdgcn_readfirstlane(v); }
#define CBAR() asm volatile("" ::: "memory")
#define OPQ(x) asm volatile("" : "+v"(x))
#define SB() __builtin_amdgcn_sched_barrier(0)
DI void wg_barrier() {
  asm volatile("s_waitcnt vmcnt(0) lgkmcnt(0)" ::: "memory");
  __builtin_amdgcn_s_barrier();
  CBAR();
}
constexpr f32x4 Z4 = {0.f, 0.f, 0.f, 0.f};

#define PT(i)

DI unsigned tok_code(int t, unsigned n) {
  const unsigned long long lo = t == 0 ? 0x0c05180408010b00ull : t == 1 ? 0x1d16291519121c11ull : 0x343033262a232d22ull;
  const unsigned long long hi = t == 0 ? 0x0a0309020e100d06ull : t == 1 ? 0x1b141a2028211e24ull : 0x2c252b313632352eull;
  return (unsigned)(((n < 8 ? lo : hi) >> (8 * (n & 7))) & 0xff);
}

struct Lane {
  unsigned cA[3], cB[3];
  unsigned cW;
  unsigned l16;
  unsigned l16o;
  unsigned bperm;
  unsigned pw, pr;
  bool q0, q1, q2;
  bool lo2;
};

DI void dma16(unsigned m0v, unsigned voff, __amdgpu_buffer_rsrc_t srd, unsigned soff) {
  asm volatile("s_mov_b32 m0, %0\n\ts_nop 0\n\tbuffer_load_dwordx4 %1, %2, %3 offen lds" ::"s"(m0v), "v"(voff), "s"(srd), "s"(soff) : "m0", "memory");
}
DI void dma4(unsigned m0v, unsigned voff, __amdgpu_buffer_rsrc_t srd, unsigned soff) {
  asm volatile("s_mov_b32 m0, %0\n\ts_nop 0\n\tbuffer_load_dword %1, %2, %3 offen lds" ::"s"(m0v), "v"(voff), "s"(srd), "s"(soff) : "m0", "memory");
}
DI void dma16_last(unsigned m0v, unsigned voff, __amdgpu_buffer_rsrc_t srd, unsigned soff) {
  asm volatile("s_mov_b32 m0, %0\n\ts_mov_b64 exec, 0xfffff\n\tbuffer_load_dwordx4 %1, %2, %3 offen lds\n\ts_mov_b64 exec, -1" ::"s"(m0v), "v"(voff), "s"(srd), "s"(soff)
               : "m0", "memory");
}
DI void dma_slab(const u32x4 t0, const u32x4 t1, unsigned m0base, __amdgpu_buffer_rsrc_t srd, unsigned soff, unsigned bm) {
  asm volatile("s_waitcnt lgkmcnt(0)" ::: "memory");
  const unsigned vo[6] = {t0[0], t0[1], t0[2], t0[3], t1[0], t1[1]};
  if (bm == 0) {
#pragma unroll
    for (int i = 0; i < 5; ++i) dma16(m0base + 1024u * i, vo[i], srd, soff);
    dma16_last(m0base + 5120u, vo[5], srd, soff);
  } else {
#pragma unroll
    for (int i = 0; i < 6; ++i) {
      const unsigned v = ((t1[2] >> (4 * i)) & bm) ? 0x80000000u : vo[i];
      if (i < 5) dma16(m0base + 1024u * i, v, srd, soff);
      else dma16_last(m0base + 5120u, v, srd, soff);
    }
  }
}
DI void dma_slab_tab(const Lane& c, unsigned m0base, __amdgpu_buffer_rsrc_t srd, unsigned soff, unsigned bm) {
  dma_slab(ldr128(c.l16o + TABO), ldr128(c.l16o + TABO + 1024), m0base, srd, soff, bm);
}
DI void l2_prefetch(unsigned pf0, unsigned pf1, unsigned m0junk, __amdgpu_buffer_rsrc_t srd, unsigned soff, unsigned bm) {
  if (bm == 0) {
    dma4(m0junk, pf0, srd, soff);
    dma4(m0junk, pf1, srd, soff);
  }
}

template <int S>
DI void conv_slab(const Lane& c, f32x4 (&xa)[3][6]) {
  u32x4 A[3], B[3][3];
  auto load = [&](int gi) {
    const int hh = gi / 5, tp = gi % 5, ct = 2 * S + hh;
    A[gi % 3] = ldr128(c.cW + (ct * 5 + tp) * CONVF);
#pragma unroll
    for (int t = 0; t < 3; ++t) B[gi % 3][t] = ldr128((tp == 4 ? c.cB[t] : c.cA[t] + (tp == 3 ? LROW + 64u : 64u * tp)) + 32u * hh);
  };
  load(0);
  load(1);
#pragma unroll
  for (int gi = 0; gi < 10; ++gi) {
    if (gi + 2 < 10) load(gi + 2);
    const int hh = gi / 5, tp = gi % 5, ct = 2 * S + hh;
#pragma unroll
    for (int t = 0; t < 3; ++t) xa[t][ct] = mfma(A[gi % 3], B[gi % 3][t], tp == 0 ? Z4 : xa[t][ct]);
    SB();
  }
}

struct Stat { float rs, sg, mu; };
template <int NT>
DI void layer_norm(const Lane& c, const f32x4 (&xa)[NT][6], u32x4 (&x0p)[NT][3], u32x4 (&hp)[NT][3], Stat (&st)[NT]) {
  const u32x4 ones = {ONES, ONES, ONES, ONES};
  f32x4 sm[NT], sq[NT];
#pragma unroll
  for (int t = 0; t < NT; ++t) {
#pragma unroll
    for (int s = 0; s < 3; ++s) {
      x0p[t][s] = u32x4{cvtpk(xa[t][2 * s][0], xa[t][2 * s][1]), cvtpk(xa[t][2 * s][2], xa[t][2 * s][3]), cvtpk(xa[t][2 * s + 1][0], xa[t][2 * s + 1][1]),
                        cvtpk(xa[t][2 * s + 1][2], xa[t][2 * s + 1][3])};
      sq[t] = mfma(x0p[t][s], x0p[t][s], s == 0 ? Z4 : sq[t]);
      sm[t] = mfma(ones, x0p[t][s], s == 0 ? Z4 : sm[t]);
    }
    SB();
  }
  float d[NT];
#pragma unroll
  for (int t = 0; t < NT; ++t) {
    float v = sq[t][3];
    v = c.q2 ? sq[t][2] : v;
    v = c.q1 ? sq[t][1] : v;
    v = c.q0 ? sq[t][0] : v;
    d[t] = __builtin_bit_cast(float, __builtin_amdgcn_ds_bpermute((int)c.bperm, __builtin_bit_cast(int, v)));
  }
  SB();
#pragma unroll
  for (int t = 0; t < NT; ++t) {
    const float mu = sm[t][0] * (1.0f / 96.0f);
    float var = __builtin_fmaf(d[t], 1.0f / 96.0f, -mu * mu);
    var = __builtin_fmaxf(var, 0.f) + 1e-5f;
    const float rs = __builtin_amdgcn_rsqf(var);
    const float a = rs * SQC;
    const float b = -mu * a;
    st[t].rs = rs;
    st[t].sg = var * rs * ISQC;
    st[t].mu = mu;
#pragma unroll
    for (int s = 0; s < 3; ++s) {
      float h[8];
#pragma unroll
      for (int e = 0; e < 8; ++e) h[e] = __builtin_fmaf(xa[t][2 * s + (e >> 2)][e & 3], a, b);
      hp[t][s] = u32x4{cvtpk(h[0], h[1]), cvtpk(h[2], h[3]), cvtpk(h[4], h[5]), cvtpk(h[6], h[7])};
    }
  }
}

struct Rec {
  unsigned k3;
  unsigned q3[4];
  unsigned v3;
};

DI float grp_max(float m) {
  m = __builtin_fmaxf(m, __shfl_xor(m, 16));
  m = __builtin_fmaxf(m, __shfl_xor(m, 32));
  return m;
}
template <int CTRL>
DI float quad_bcast(float v) { return __builtin_bit_cast(float, __builtin_amdgcn_mov_dpp(__builtin_bit_cast(int, v), CTRL, 0xf, 0xf, true)); }

template <int H>
DI void norm_tile(const f32x4 O0, const f32x4 O1, float rs, u32x4 (&Op)[3], float& l_out) {
  constexpr int E = (H & 1) ? 2 : 0;
  const float l = O1[2 - E];
  l_out = l;
  const float f = __builtin_amdgcn_rcpf(l) * rs;
  Op[(3 * H) >> 2][(3 * H) & 3] = cvtpk(O0[0] * f, O0[1] * f);
  Op[(3 * H + 1) >> 2][(3 * H + 1) & 3] = cvtpk(O0[2] * f, O0[3] * f);
  Op[(3 * H + 2) >> 2][(3 * H + 2) & 3] = cvtpk(O1[E] * f, O1[E + 1] * f);
}
template <int H>
DI unsigned norm_left(const f32x4 L0, const f32x4 L1, float& l_out) {
  const float l = quad_bcast<(H & 1) ? 0x00 : 0xaa>(L1[0]);
  l_out = l;
  const float f = __builtin_amdgcn_rcpf(l) * ISQC;
  return cvtpk(L0[0] * f, L1[0] * f);
}
DI u32x4 pack8(const f32x4 a, const f32x4 b) { return u32x4{cvtpk(a[0], a[1]), cvtpk(a[2], a[3]), cvtpk(b[0], b[1]), cvtpk(b[2], b[3])}; }
DI f32x4 ex2x4(const f32x4 a) { return f32x4{ex2(a[0]), ex2(a[1]), ex2(a[2]), ex2(a[3])}; }

struct Heads {
  u32x4 F[3];
  unsigned Xp[6];
  f32x4 pO0, pO1;
  f32x4 pL0, pL1;
  float lmin, lmax;
};

template <int H>
DI void head_tail(Heads& hs, const Stat (&st)[3], u32x4 (&Op)[3][3], unsigned (&oL)[4], int part) {
  if (part == 0) {
    float l;
    norm_tile<H>(hs.pO0, hs.pO1, st[2].rs, Op[2], l);
    hs.lmin = __builtin_fminf(hs.lmin, l);
    hs.lmax = __builtin_fmaxf(hs.lmax, l);
  } else {
    float l;
    oL[H] = norm_left<H>(hs.pL0, hs.pL1, l);
    hs.lmin = __builtin_fminf(hs.lmin, l);
    hs.lmax = __builtin_fmaxf(hs.lmax, l);
  }
}

template <int H>
DI void head_fast(const Lane& c, const Rec& rc, const u32x4 (&hp)[3][3], const Stat (&st)[3], Heads& hs, u32x4 (&Op)[3][3], unsigned (&oL)[4]) {
  constexpr unsigned WB = L_WQKV + H * 12288;
  constexpr bool EVEN = (H & 1) == 0;
  u32x4(&F)[3] = hs.F;
  f32x4 T[3], U[3], W[3], Y[3];
  u32x4 Q[3], K[3], M[4], K3;
#pragma unroll
  for (int s = 0; s < 3; ++s) {
#pragma unroll
    for (int t = 0; t < 3; ++t) T[t] = mfma(F[s], hp[t][s], s == 0 ? Z4 : T[t]);
    F[s] = ldr128(c.l16 + WB + 3072 + s * 1024);
    if (H > 0 && s < 2) head_tail<(H > 0 ? H - 1 : 0)>(hs, st, Op, oL, s);
    SB();
  }
#pragma unroll
  for (int s = 0; s < 3; ++s) {
#pragma unroll
    for (int t = 0; t < 3; ++t) U[t] = mfma(F[s], hp[t][s], s == 0 ? Z4 : U[t]);
    F[s] = ldr128(c.l16 + WB + 2 * 3072 + s * 1024);
    K[s][0] = cvtpk(T[s][0], T[s][1]);
    K[s][1] = cvtpk(T[s][2], T[s][3]);
    K[s][3] = 0u;
    SB();
  }
#pragma unroll
  for (int s = 0; s < 3; ++s) {
#pragma unroll
    for (int t = 0; t < 3; ++t) W[t] = mfma(F[s], hp[t][s], s == 0 ? Z4 : W[t]);
    F[s] = ldr128(c.l16 + WB + 3 * 3072 + s * 1024);
    M[s] = ldr128(rc.q3[s] + H * 192);
    if (s == 2) { M[3] = ldr128(rc.q3[3] + H * 192); K3 = ldr128(rc.k3 + H * 192); }
    Q[s][2] = cvtpk(U[s][0], U[s][1]);
    K[s][2] = cvtpk(U[s][2], U[s][3]);
    Q[s][3] = 0u;
    SB();
  }
  f32x4 DL = Z4;
  unsigned v3a, v3b;
#pragma unroll
  for (int s = 0; s < 3; ++s) {
#pragma unroll
    for (int t = 0; t < 3; ++t) Y[t] = mfma(hp[t][s], F[s], s == 0 ? Z4 : Y[t]);
    DL = mfma(M[s], K[s], DL);
    F[s] = EVEN ? ldr128(c.l16 + L_X2 + (H >> 1) * 3072 + s * 1024) : ldr128(c.l16 + (H < 3 ? WB + 12288 : 0) + s * 1024);
    Q[s][0] = cvtpk(W[s][0], W[s][1]);
    Q[s][1] = cvtpk(W[s][2], W[s][3]);
    if (s == 2) { v3a = ldr16(rc.v3 + H * 192); v3b = ldr16(rc.v3 + H * 192 + 32); }
    SB();
  }
  f32x4 S0[4], S1[4], S2[4];
#pragma unroll
  for (int i = 0; i < 3; ++i) S0[i] = mfma(K[i], Q[0], Z4);
  S0[3] = mfma(K3, Q[0], Z4);
  DL = mfma(M[3], K3, DL);
  u32x4 Va0, Va1, Vb0, Vb1;
  Va0 = pack8(Y[0], Y[1]);
  SB();
#pragma unroll
  for (int i = 0; i < 3; ++i) S1[i] = mfma(K[i], Q[1], Z4);
  S1[3] = mfma(K3, Q[1], Z4);
  Va1 = u32x4{cvtpk(Y[2][0], Y[2][1]), cvtpk(Y[2][2], Y[2][3]), v3a, 0u};
  {
    const float p = ex2(DL[0]);
    ldw16(c.pw, cvtpk(p, p));
  }
  SB();
  u32x4 PL0, PL1;
  u32x4 P0, P1;
  if (EVEN) {
    f32x4 Z[3];
#pragma unroll
    for (int s = 0; s < 3; ++s) {
#pragma unroll
      for (int t = 0; t < 3; ++t) Z[t] = mfma(hp[t][s], F[s], s == 0 ? Z4 : Z[t]);
      F[s] = ldr128(c.l16 + WB + 12288 + s * 1024);
      S0[s] = ex2x4(S0[s]);
      if (s == 0) { PL0 = ldr128(c.pr); PL1 = ldr128(c.pr + 64); }
      if (s == 1) { P0 = pack8(S0[0], S0[1]); }
      SB();
    }
    const float p3 = ex2(S0[3][0]);
    P1 = u32x4{cvtpk(S0[2][0], S0[2][1]), cvtpk(S0[2][2], S0[2][3]), cvtpk(p3, p3), 0u};
#pragma unroll
    for (int i = 0; i < 3; ++i) S2[i] = mfma(K[i], Q[2], Z4);
    S2[3] = mfma(K3, Q[2], Z4);
    S1[0] = ex2x4(S1[0]);
    SB();
#pragma unroll
    for (int t = 0; t < 3; ++t) {
      hs.Xp[2 * t] = cvtpk(Z[t][0], Z[t][1]);
      hs.Xp[2 * t + 1] = cvtpk(Z[t][2], Z[t][3]);
    }
  } else {
    PL0 = ldr128(c.pr);
    PL1 = ldr128(c.pr + 64);
#pragma unroll
    for (int i = 0; i < 3; ++i) S2[i] = mfma(K[i], Q[2], Z4);
    S2[3] = mfma(K3, Q[2], Z4);
#pragma unroll
    for (int s = 0; s < 3; ++s) S0[s] = ex2x4(S0[s]);
    const float p3 = ex2(S0[3][0]);
    P0 = pack8(S0[0], S0[1]);
    P1 = u32x4{cvtpk(S0[2][0], S0[2][1]), cvtpk(S0[2][2], S0[2][3]), cvtpk(p3, p3), 0u};
    SB();
    S1[0] = ex2x4(S1[0]);
  }
  {
    const bool keep = EVEN ? c.lo2 : !c.lo2;
    Vb0 = u32x4{keep ? hs.Xp[0] : ONES, keep ? hs.Xp[1] : ONES, keep ? hs.Xp[2] : ONES, keep ? hs.Xp[3] : ONES};
    Vb1 = u32x4{keep ? hs.Xp[4] : ONES, keep ? hs.Xp[5] : ONES, v3b, 0u};
  }
  SB();
  f32x4 O0, O1;
  O0 = mfma(Va0, P0, Z4);
  O1 = mfma(Vb0, P0, Z4);
  O0 = mfma(Va1, P1, O0);
  O1 = mfma(Vb1, P1, O1);
  S1[1] = ex2x4(S1[1]);
  S1[2] = ex2x4(S1[2]);
  {
    const float p3 = ex2(S1[3][0]);
    P0 = pack8(S1[0], S1[1]);
    P1 = u32x4{cvtpk(S1[2][0], S1[2][1]), cvtpk(S1[2][2], S1[2][3]), cvtpk(p3, p3), 0u};
  }
  SB();
  f32x4 R0, R1;
  R0 = mfma(Va0, P0, Z4);
  R1 = mfma(Vb0, P0, Z4);
  R0 = mfma(Va1, P1, R0);
  R1 = mfma(Vb1, P1, R1);
  float la, lb;
  norm_tile<H>(O0, O1, st[0].rs, Op[0], la);
  S2[0] = ex2x4(S2[0]);
  SB();
  hs.pL0 = mfma(PL0, Va0, Z4);
  hs.pL1 = mfma(PL0, Vb0, Z4);
  hs.pL0 = mfma(PL1, Va1, hs.pL0);
  hs.pL1 = mfma(PL1, Vb1, hs.pL1);
  S2[1] = ex2x4(S2[1]);
  S2[2] = ex2x4(S2[2]);
  {
    const float p3 = ex2(S2[3][0]);
    P0 = pack8(S2[0], S2[1]);
    P1 = u32x4{cvtpk(S2[2][0], S2[2][1]), cvtpk(S2[2][2], S2[2][3]), cvtpk(p3, p3), 0u};
  }
  SB();
  hs.pO0 = mfma(Va0, P0, Z4);
  hs.pO1 = mfma(Vb0, P0, Z4);
  hs.pO0 = mfma(Va1, P1, hs.pO0);
  hs.pO1 = mfma(Vb1, P1, hs.pO1);
  norm_tile<H>(R0, R1, st[1].rs, Op[1], lb);
  hs.lmin = __builtin_fminf(__builtin_fminf(la, lb), hs.lmin);
  hs.lmax = __builtin_fmaxf(__builtin_fmaxf(la, lb), hs.lmax);
  SB();
}

template <int H>
DI void head_safe(const Lane& c, const Rec& rc, unsigned recq, const u32x4 (&hp)[3][3], const Stat (&st)[3], u32x4 (&Op)[3][3], unsigned (&oL)[4]) {
  constexpr unsigned WB = L_WQKV + H * 12288;
  f32x4 T[3], U[3], W[3], Y[3], Z[3];
#pragma unroll
  for (int s = 0; s < 3; ++s) {
    const u32x4 fk = ldr128(c.l16 + WB + s * 1024), fr = ldr128(c.l16 + WB + 3072 + s * 1024), fq = ldr128(c.l16 + WB + 6144 + s * 1024);
    const u32x4 fv = ldr128(c.l16 + WB + 9216 + s * 1024), fx = ldr128(c.l16 + L_X2 + (H >> 1) * 3072 + s * 1024);
#pragma unroll
    for (int t = 0; t < 3; ++t) {
      T[t] = mfma(fk, hp[t][s], s == 0 ? Z4 : T[t]);
      U[t] = mfma(fr, hp[t][s], s == 0 ? Z4 : U[t]);
      W[t] = mfma(fq, hp[t][s], s == 0 ? Z4 : W[t]);
      Y[t] = mfma(hp[t][s], fv, s == 0 ? Z4 : Y[t]);
      Z[t] = mfma(hp[t][s], fx, s == 0 ? Z4 : Z[t]);
    }
    SB();
  }
  u32x4 Q[3], K[4];
#pragma unroll
  for (int t = 0; t < 3; ++t) {
    Q[t] = u32x4{cvtpk(W[t][0], W[t][1]), cvtpk(W[t][2], W[t][3]), cvtpk(U[t][0], U[t][1]), 0u};
    K[t] = u32x4{cvtpk(T[t][0], T[t][1]), cvtpk(T[t][2], T[t][3]), cvtpk(U[t][2], U[t][3]), 0u};
  }
  K[3] = ldr128(rc.k3 + H * 192);
  const u32x4 Q3 = ldr128(recq + H * 192);
  const bool keep = (H & 1) == 0 ? c.lo2 : !c.lo2;
  const u32x4 Va0 = pack8(Y[0], Y[1]);
  const u32x4 Va1 = u32x4{cvtpk(Y[2][0], Y[2][1]), cvtpk(Y[2][2], Y[2][3]), ldr16(rc.v3 + H * 192), 0u};
  const u32x4 X0 = pack8(Z[0], Z[1]);
  const u32x4 Vb0 = u32x4{keep ? X0[0] : ONES, keep ? X0[1] : ONES, keep ? X0[2] : ONES, keep ? X0[3] : ONES};
  const u32x4 Vb1 = u32x4{keep ? cvtpk(Z[2][0], Z[2][1]) : ONES, keep ? cvtpk(Z[2][2], Z[2][3]) : ONES, ldr16(rc.v3 + H * 192 + 32), 0u};
  SB();
#pragma unroll
  for (int jq = 0; jq < 3; ++jq) {
    f32x4 S[4];
#pragma unroll
    for (int i = 0; i < 4; ++i) S[i] = mfma(K[i], Q[jq], Z4);
    float m = S[3][0];
#pragma unroll
    for (int i = 0; i < 3; ++i)
#pragma unroll
      for (int r = 0; r < 4; ++r) m = __builtin_fmaxf(m, S[i][r]);
    m = grp_max(m);
#pragma unroll
    for (int i = 0; i < 3; ++i)
#pragma unroll
      for (int r = 0; r < 4; ++r) S[i][r] = ex2(S[i][r] - m);
    const float p3 = ex2(S[3][0] - m);
    const u32x4 P0 = pack8(S[0], S[1]);
    const u32x4 P1 = u32x4{cvtpk(S[2][0], S[2][1]), cvtpk(S[2][2], S[2][3]), cvtpk(p3, p3), 0u};
    f32x4 O0 = mfma(Va0, P0, Z4), O1 = mfma(Vb0, P0, Z4);
    O0 = mfma(Va1, P1, O0);
    O1 = mfma(Vb1, P1, O1);
    float l;
    norm_tile<H>(O0, O1, st[jq].rs, Op[jq], l);
    OPQ(Op[jq][(3 * H) >> 2]);
    OPQ(Op[jq][(3 * H + 2) >> 2]);
    SB();
  }
  {
    f32x4 D[4];
#pragma unroll
    for (int i = 0; i < 4; ++i) D[i] = mfma(Q3, K[i], Z4);
    const unsigned g = (c.l16 >> 8) & 3;
    float s = D[3][0];
    s = g == 2 ? D[2][0] : s;
    s = g == 1 ? D[1][0] : s;
    s = g == 0 ? D[0][0] : s;
    float m = s;
#pragma unroll
    for (int o = 1; o < 64; o <<= 1) m = __builtin_fmaxf(m, __shfl_xor(m, o));
    const float p = ex2(s - m);
    ldw16(c.pw, cvtpk(p, p));
    const u32x4 PL0 = ldr128(c.pr), PL1 = ldr128(c.pr + 64);
    f32x4 L0 = mfma(PL0, Va0, Z4), L1 = mfma(PL0, Vb0, Z4);
    L0 = mfma(PL1, Va1, L0);
    L1 = mfma(PL1, Vb1, L1);
    float l;
    oL[H] = norm_left<H>(L0, L1, l);
    OPQ(oL[H]);
    SB();
  }
}

template <int CT0>
DI void proj_part(const Lane& c, const u32x4 (&hp)[3][3], const Stat (&st)[3], const u32x4 (&Op)[3][3], __amdgpu_buffer_rsrc_t rout, unsigned wbase) {
  u32x4 A[3], I[2];
  I[0] = ldr128(c.l16o + IDO);
  I[1] = ldr128(c.l16o + IDO + 1024);
#pragma unroll
  for (int s = 0; s < 3; ++s) A[s] = ldr128(c.l16o + (CT0 * 3 + s) * 1024);
  const u32x4 t2 = ldr128(c.l16o + TABO + 2048);
  f32x4 acc[2][3];
#pragma unroll
  for (int k = 0; k < 4; ++k) {
    const int ct = CT0 + k;
    if (k < 3) {
#pragma unroll
      for (int t = 0; t < 3; ++t) acc[k & 1][t] = mfma(I[ct & 1], hp[t][ct >> 1], Z4);
#pragma unroll
      for (int s = 0; s < 3; ++s) {
#pragma unroll
        for (int t = 0; t < 3; ++t) acc[k & 1][t] = mfma(A[s], Op[t][s], acc[k & 1][t]);
        if (k < 2) A[s] = ldr128(c.l16o + ((ct + 1) * 3 + s) * 1024);
      }
    }
    if (k > 0) {
#pragma unroll
      for (int t = 0; t < 3; ++t) {
        const f32x4 a = acc[(k - 1) & 1][t];
        const float sg = st[t].sg, mu = st[t].mu;
        const u32x2 o = {cvtpk(__builtin_fmaf(a[0], sg, mu), __builtin_fmaf(a[1], sg, mu)), cvtpk(__builtin_fmaf(a[2], sg, mu), __builtin_fmaf(a[3], sg, mu))};
        __builtin_amdgcn_raw_buffer_store_b64(o, rout, t2[1 + t] + (ct - 1) * 32, wbase, 0);
      }
    }
    SB();
  }
}

DI unsigned win_index(unsigned base_x, unsigned j, unsigned rho, unsigned w) { return base_x + (rho < 2 ? rho * 256 + j * 8 + w : 512 + 32 * w + j); }

DI void spin_until(unsigned off, unsigned target) {
  while (rfl(*(volatile AS3 unsigned*)ldsp(off)) < target) __builtin_amdgcn_s_sleep(2);
  CBAR();
}

DI u32x4 tr_frag(unsigned base, unsigned l8) {
  const s16x4 lo = __builtin_amdgcn_ds_read_tr16_b64_v4i16((AS3 s16x4*)ldsp(base + l8));
  const s16x4 hi = __builtin_amdgcn_ds_read_tr16_b64_v4i16((AS3 s16x4*)ldsp(base + 512 + l8));
  const u32x2 l2 = __builtin_bit_cast(u32x2, lo), h2 = __builtin_bit_cast(u32x2, hi);
  return u32x4{l2[0], l2[1], h2[0], h2[1]};
}
DI void build3(unsigned base, unsigned l8) {
  u32x4 f[3];
#pragma unroll
  for (int s = 0; s < 3; ++s) f[s] = tr_frag(base + s * 1024, l8);
#pragma unroll
  for (int s = 0; s < 3; ++s) ldw128(base + s * 1024 + 2 * l8, f[s]);
}

__global__ __launch_bounds__(512, 2) __attribute__((target("no-packed-fp32-ops")))
void wattn_kernel(const uint16_t* __restrict__ x, const uint16_t* __restrict__ dw, const uint16_t* __restrict__ wqkv, const uint16_t* __restrict__ wo,
                  uint16_t* __restrict__ out
                  ) {
  const unsigned tid = threadIdx.x;
  const unsigned wv = rfl(tid >> 6);
  Lane c;
  const unsigned lane = tid & 63, n = lane & 15, g = lane >> 4;
  const unsigned xcd = blockIdx.x & 7, jw = blockIdx.x >> 3;
  const unsigned base_x = xcd * 666 + (xcd ? 1 : 0), cnt_x = 666 + (xcd == 0 ? 1 : 0);
  const unsigned n2 = 4 + ((640 + jw < cnt_x) ? 1 : 0);
  const unsigned nW = 16 + n2;
  const unsigned nwin = wv < n2 ? 3 : 2;
  const unsigned lds0 = (unsigned)(size_t)(AS3 char*)g_lds;
  const unsigned slab = L_SLAB + wv * SLABB;

  const __amdgpu_buffer_rsrc_t rx = __builtin_amdgcn_make_buffer_rsrc((void*)((const char*)x - XSHIFT), (short)0, (int)(XBYTES + XSHIFT), 0x00020000);
  const __amdgpu_buffer_rsrc_t rw = __builtin_amdgcn_make_buffer_rsrc((void*)wqkv, (short)0, 96 * 288 * 2, 0x00020000);
  const __amdgpu_buffer_rsrc_t ro = __builtin_amdgcn_make_buffer_rsrc((void*)wo, (short)0, 96 * 96 * 2, 0x00020000);
  const __amdgpu_buffer_rsrc_t rout = __builtin_amdgcn_make_buffer_rsrc((void*)out, (short)0, (int)XBYTES, 0x00020000);

  PT(16);
  const unsigned hdw = wv >> 1;
  {
    const unsigned v16 = (lane >> 1) * 576 + (lane & 1) * 16;
    const unsigned vd = (lane >> 3) * 576 + ((lane & 7) >> 1) * 4;
    const unsigned wb = lds0 + L_WQKV + hdw * 12288;
    if ((wv & 1) == 0) {
#pragma unroll
      for (int s = 0; s < 3; ++s) {
        dma16(wb + 6144 + s * 1024, v16, rw, s * 32 * 576 + 48 * hdw);
        dma16(wb + 9216 + s * 1024, v16, rw, s * 32 * 576 + 384 + 48 * hdw);
      }
      if (wv < 4) {
        const unsigned v = vd + (lane & 1) * 48;
#pragma unroll 1
        for (unsigned i = 0; i < 12; ++i) dma4(lds0 + L_X2 + hdw * 3072 + i * 256, v, rw, i * 8 * 576 + 384 + 96 * hdw + 32);
      }
    } else {
#pragma unroll
      for (int s = 0; s < 3; ++s) dma16(wb + s * 1024, v16, rw, s * 32 * 576 + 192 + 48 * hdw);
      const unsigned v = vd + (lane & 1) * 192;
#pragma unroll 1
      for (unsigned i = 0; i < 12; ++i) dma4(wb + 3072 + i * 256, v, rw, i * 8 * 576 + 48 * hdw + 32);
    }
    if (wv < 6) {
      const unsigned R = lane >> 1, gg = (R >> 2) & 3, e = 4 * (R >> 4) + (R & 3);
#pragma unroll
      for (int s = 0; s < 3; ++s) {
        const unsigned sq = 8 * s + e, hd = sq / 6, ix = sq % 6;
        const unsigned d = ix < 4 ? 4 * gg + ix : 12 + 2 * gg + ix;
        dma16(lds0 + L_WO + (wv * 3 + s) * 1024, (hd * 24 + d) * 192 + (lane & 1) * 16, ro, wv * 32);
      }
    }
  }
  PT(17);
  unsigned vo[6], vflag = 0, pf[2];
#pragma unroll
  for (int i = 0; i < 6; ++i) {
    const unsigned sl = 64 * i + lane, r = sl / 38, t = sl % 38, cc = t >> 2, q = t & 3;
    const bool ok = t < 36 && sl < 340;
    vo[i] = ok ? (r * IMG + cc) * PIXB + q * 16 : 0x80000000u;
    vflag |= ((r == 0 ? 1u : 0u) | (r == 8 ? 2u : 0u) | (cc == 0 ? 4u : 0u) | (cc == 8 ? 8u : 0u)) << (4 * i);
  }
#pragma unroll
  for (int i = 0; i < 2; ++i) {
    const unsigned p = 64 * i + lane, r = p / 9, cc = p % 9;
    pf[i] = p < 81 ? (r * IMG + cc) * PIXB + 128 : 0x80000000u;
  }
  const unsigned m0slab = rfl(lds0 + slab), m0junk = rfl(lds0 + L_JUNK);
  unsigned widx = win_index(base_x, jw, 0, wv);
  unsigned wr = widx / NW1, wc = widx % NW1;
  unsigned wbase = (7 * wr * IMG + 7 * wc) * PIXB;
  unsigned bm = (wr == 0 ? 1u : 0u) | (wr == NW1 - 1 ? 2u : 0u) | (wc == 0 ? 4u : 0u) | (wc == NW1 - 1 ? 8u : 0u);
  PT(18);
  dma_slab(u32x4{vo[0], vo[1], vo[2], vo[3]}, u32x4{vo[4], vo[5], vflag, pf[0]}, m0slab, rx, wbase, bm);
  l2_prefetch(pf[0], pf[1], m0junk, rx, wbase, bm);

  PT(19);
  unsigned cvw[2], cve[2];
#pragma unroll
  for (int i = 0; i < 2; ++i) {
    const unsigned sl0 = tid + 512 * i, sl = sl0 < 1020 ? sl0 : 8;
    const unsigned f = sl / 34, k = sl % 34, ct = f / 5, tp = f % 5;
    const unsigned G = (k > 8 ? 1u : 0u) + (k > 16 ? 1u : 0u) + (k > 25 ? 1u : 0u);
    const unsigned idx = (k - ((0x1a110900u >> (8 * G)) & 0xff)) & 7;
    const unsigned hi = G >> 1, m = 8 * (idx >> 2) + 4 * (G & 1) + (idx & 3);
    const unsigned tap = (unsigned)(0x8674524130ull >> (4 * (2 * tp + hi))) & 15;
    cvw[i] = dw[tap * 96 + 16 * ct + m];
    cve[i] = (k == 8 || k == 25) ? 8u : ((m & 7) | ((tp == 3 && hi == 0) ? 16u : 0u));
  }
  PT(20);
  const bool lw = wv >= 6;
  const unsigned lt = wv == 7 ? 0 : 1;
  const unsigned lwl = 16 * lt + n;
  const bool lvalid = lwl < nW;
  unsigned lpix = 0;
  u32x4 lb[30];
  if (lw) {
    const unsigned wl = lvalid ? lwl : 0;
    const unsigned wi = win_index(base_x, jw, wl >> 3, wl & 7);
    const unsigned r = wi / NW1, cc = wi % NW1;
    lpix = ((7 * r + LEFT_I) * IMG + 7 * cc + LEFT_J) * PIXB;
    const unsigned hi = g >> 1;
    const char* px = (const char*)x + lpix + (g & 1) * 16;
    const char* p0 = px + ((int)hi - 1) * (int)ROWB - (int)PIXB;
    const char* p3 = px + hi * ROWB;
    const char* p4 = px + ROWB + (2 * (int)hi - 1) * (int)PIXB;
#pragma unroll
    for (int f = 0; f < 30; ++f) {
      const int tp = f % 5;
      const char* p = tp < 3 ? p0 + tp * PIXB : tp == 3 ? p3 : p4;
      lb[f] = *(const u32x4*)(p + (f / 5) * 32);
    }
  }

  PT(21);
  {
    unsigned ost[3];
#pragma unroll
    for (int t = 0; t < 3; ++t) {
      const unsigned code = tok_code(t, n), i = code >> 3, j = code & 7;
      c.cA[t] = slab + LROW * (i + (g >> 1)) + 64 * j + 16 * (g & 1);
      c.cB[t] = slab + LROW * (i + 2) + 64 * (j + 2 * (g >> 1)) + 16 * (g & 1);
      ost[t] = (i * IMG + j) * PIXB + g * 8;
    }
    if (wv == 4) {
      const unsigned one = (n >> 2) == g ? 0x3f80u << (16 * (n & 1)) : 0u;
      const unsigned d0 = (n & 2) ? 0u : one, d1 = (n & 2) ? one : 0u;
      ldw128(L_ID + lane * 16, u32x4{d0, d1, 0u, 0u});
      ldw128(L_ID + 1024 + lane * 16, u32x4{0u, 0u, d0, d1});
    }
    if (wv == 5) {
      ldw128(L_TAB + lane * 16, u32x4{vo[0], vo[1], vo[2], vo[3]});
      ldw128(L_TAB + 1024 + lane * 16, u32x4{vo[4], vo[5], vflag, pf[0]});
      ldw128(L_TAB + 2048 + lane * 16, u32x4{pf[1], ost[0], ost[1], ost[2]});
    }
  }
  {
    const bool act = (g & 1) == (n >> 3);
    const unsigned G = 2 * (g >> 1) + ((n >> 2) & 1), idx = 4 * (g & 1) + (n & 3);
    const unsigned k = (G == 0 ? 0 : G == 1 ? 9 : G == 2 ? 17 : 26) + idx;
    c.cW = L_CONV + 16 * (act ? k : (g < 2 ? 8 : 25));
  }
  c.l16 = lane * 16; c.l16o = L_WO + lane * 16;
  c.bperm = 4 * ((n >> 2) * 16 + n);
  c.pw = lane < 49 ? L_PS + wv * 128 + (g >> 1) * 64 + (n >> 2) * 16 + (g & 1) * 8 + (n & 3) * 2 : L_JUNK + lane * 2;
  c.pr = L_PS + wv * 128 + g * 16;
  c.q0 = (n & 3) == 0; c.q1 = (n & 3) == 1; c.q2 = (n & 3) == 2; c.lo2 = (n & 3) < 2;
  if (tid < (L_FLAG + 16 - L_PS) / 4) ldw32(L_PS + tid * 4, 0u);
  PT(0);
  asm volatile("s_waitcnt vmcnt(0)" ::: "memory");
  if (lw) {
#pragma unroll
    for (int f = 0; f < 30; ++f) asm volatile("" ::"v"(lb[f]));
  }
  PT(1);
  {
    const unsigned l8 = lane * 8, wb = L_WQKV + hdw * 12288;
    if ((wv & 1) == 0) {
      build3(wb + 6144, l8);
      build3(wb + 9216, l8);
      if (wv < 4) build3(L_X2 + hdw * 3072, l8);
    } else {
      build3(wb, l8);
      build3(wb + 3072, l8);
    }
    if (wv < 6) build3(L_WO + wv * 3072, l8);
#pragma unroll
    for (int i = 0; i < 2; ++i) {
      const unsigned e = cve[i];
      const unsigned w = (e & 16) ? 0x3f80u : cvw[i];
      const unsigned dd = w << (16 * (e & 1)), h = (e >> 1) & 7;
      const u32x4 v = {h == 0 ? dd : 0u, h == 1 ? dd : 0u, h == 2 ? dd : 0u, h == 3 ? dd : 0u};
      if (i == 0 || tid < 508) ldw128(L_CONV + (tid + 512 * i) * 16, v);
    }
    wg_barrier();
  }
  PT(2);

  if (lw) {
    f32x4 xa[1][6];
#pragma unroll
    for (int tp = 0; tp < 5; ++tp)
#pragma unroll
      for (int ct = 0; ct < 6; ++ct) xa[0][ct] = mfma(ldr128(c.cW + (ct * 5 + tp) * CONVF), lb[ct * 5 + tp], tp == 0 ? Z4 : xa[0][ct]);
    u32x4 x0p[1][3], hp[1][3];
    Stat st1[1];
    layer_norm<1>(c, xa, x0p, hp, st1);
    const unsigned rec = lvalid ? L_REC + lwl * RECB : L_JUNK;
#pragma unroll
    for (int s = 0; s < 3; ++s) ldw128(rec + 768 + g * 48 + s * 16, x0p[0][s]);
    unsigned recv[4];
#pragma unroll
    for (int r = 0; r < 4; ++r) {
      const unsigned wl = 16 * lt + 4 * g + r;
      recv[r] = (wl < nW ? L_REC + wl * RECB : L_JUNK) + 128 + n * 2;
    }
#pragma unroll
    for (int p = 0; p < 2; ++p) {
      f32x4 Z = Z4;
#pragma unroll
      for (int s = 0; s < 3; ++s) Z = mfma(hp[0][s], ldr128(c.l16 + L_X2 + p * 3072 + s * 1024), Z);
#pragma unroll
      for (int hh = 0; hh < 2; ++hh) {
        const int hd = 2 * p + hh;
        const unsigned wb = c.l16 + L_WQKV + hd * 12288;
        f32x4 T = Z4, U = Z4, W = Z4, Y = Z4;
#pragma unroll
        for (int s = 0; s < 3; ++s) {
          T = mfma(ldr128(wb + s * 1024), hp[0][s], T);
          U = mfma(ldr128(wb + 3072 + s * 1024), hp[0][s], U);
          W = mfma(ldr128(wb + 6144 + s * 1024), hp[0][s], W);
          Y = mfma(hp[0][s], ldr128(wb + 9216 + s * 1024), Y);
        }
        ldw128(rec + hd * 192 + g * 16, u32x4{cvtpk(T[0], T[1]), cvtpk(T[2], T[3]), cvtpk(U[2], U[3]), 0u});
        ldw128(rec + hd * 192 + 64 + g * 16, u32x4{cvtpk(W[0], W[1]), cvtpk(W[2], W[3]), cvtpk(U[0], U[1]), 0u});
        const bool keep = hh == 0 ? c.lo2 : !c.lo2;
#pragma unroll
        for (int r = 0; r < 4; ++r) {
          ldw16(recv[r] + hd * 192, cvtpk(Y[r], Y[r]));
          ldw16(recv[r] + hd * 192 + 32, keep ? cvtpk(Z[r], Z[r]) : ONES);
        }
      }
    }
    asm volatile("s_waitcnt lgkmcnt(0)" ::: "memory");
    if (lane == 0) *(volatile AS3 unsigned*)ldsp(L_FLAG + 4 * lt) = 1u;
    CBAR();
  }
  PT(3);

  u32x4 hp[3][3], Op[3][3];
  Stat st[3];
  unsigned pbase = 0;
#pragma unroll 1
  for (unsigned rho = 0; rho < nwin; ++rho) {
    f32x4 xa[3][6];
#pragma unroll
    for (int t = 0; t < 3; ++t) { OPQ(c.cA[t]); OPQ(c.cB[t]); }
    OPQ(c.cW); OPQ(c.l16); OPQ(c.l16o); OPQ(c.pw); OPQ(c.pr);
    asm volatile("s_waitcnt vmcnt(0)" ::: "memory");
    PT(4);
    conv_slab<0>(c, xa);
    PT(5);
    dma_slab_tab(c, m0slab, rx, wbase + 64, bm);
    if (rho > 0) {
      proj_part<0>(c, hp, st, Op, rout, pbase);
      asm volatile("s_waitcnt vmcnt(9)" ::: "memory");
    } else {
      asm volatile("s_waitcnt vmcnt(0)" ::: "memory");
    }
    PT(6);
    conv_slab<1>(c, xa);
    PT(7);
    dma_slab_tab(c, m0slab, rx, wbase + 128, bm);
    if (rho > 0) {
      proj_part<3>(c, hp, st, Op, rout, pbase);
      asm volatile("s_waitcnt vmcnt(9)" ::: "memory");
    } else {
      asm volatile("s_waitcnt vmcnt(0)" ::: "memory");
    }
    PT(8);
    conv_slab<2>(c, xa);
    pbase = wbase;
    const unsigned wl = rho * 8 + wv;
    if (rho + 1 < nwin) {
      widx = win_index(base_x, jw, rho + 1, wv);
      wr = widx / NW1; wc = widx % NW1;
      wbase = (7 * wr * IMG + 7 * wc) * PIXB;
      bm = (wr == 0 ? 1u : 0u) | (wr == NW1 - 1 ? 2u : 0u) | (wc == 0 ? 4u : 0u) | (wc == NW1 - 1 ? 8u : 0u);
      const u32x4 t1 = ldr128(c.l16o + TABO + 1024), t2 = ldr128(c.l16o + TABO + 2048);
      dma_slab(ldr128(c.l16o + TABO), t1, m0slab, rx, wbase, bm);
      l2_prefetch(t1[3], t2[0], m0junk, rx, wbase, bm);
    }
    PT(9);
    Heads hs;
#pragma unroll
    for (int s = 0; s < 3; ++s) hs.F[s] = ldr128(c.l16 + L_WQKV + s * 1024);
    u32x4 x0p[3][3];
    layer_norm<3>(c, xa, x0p, hp, st);
    PT(10);
    if (rho != 1) spin_until(L_FLAG + 4 * (rho >> 1), 1u);
    PT(11);
    const unsigned rec = L_REC + wl * RECB;
    const unsigned lnn = (c.l16 >> 4) & 15, lgg = c.l16 >> 8;
    Rec rc;
    rc.k3 = rec + lgg * 16;
#pragma unroll
    for (int i = 0; i < 4; ++i) rc.q3[i] = (lnn >> 2) == (unsigned)i ? rc.k3 + 64 : L_ZERO;
    rc.v3 = lgg == 0 ? rec + 128 + lnn * 2 : L_ZERO;
    hs.lmin = 1.0f; hs.lmax = 1.0f;
    unsigned oL[4];
    SB();
    head_fast<0>(c, rc, hp, st, hs, Op, oL);
    head_fast<1>(c, rc, hp, st, hs, Op, oL);
    head_fast<2>(c, rc, hp, st, hs, Op, oL);
    head_fast<3>(c, rc, hp, st, hs, Op, oL);
    head_tail<3>(hs, st, Op, oL, 0);
    head_tail<3>(hs, st, Op, oL, 1);
    const bool bad = !(hs.lmax < GUARD_HI) || !(hs.lmin > GUARD_LO);
    if (__builtin_expect(__builtin_amdgcn_ballot_w64(bad) != 0, 0)) {
      head_safe<0>(c, rc, rc.k3 + 64, hp, st, Op, oL);
      head_safe<1>(c, rc, rc.k3 + 64, hp, st, Op, oL);
      head_safe<2>(c, rc, rc.k3 + 64, hp, st, Op, oL);
      head_safe<3>(c, rc, rc.k3 + 64, hp, st, Op, oL);
    }
    {
      const unsigned oa = rec + 64 + (lnn >> 2) * 16 + (lnn & 3) * 2;
      const unsigned ob = rec + 64 + (lnn >> 2) * 16 + 8 + (lnn & 1) * 2;
      const unsigned obe = c.lo2 ? ob : L_JUNK, obo = c.lo2 ? L_JUNK : ob;
#pragma unroll
      for (int h = 0; h < 4; ++h) {
        ldw16(oa + h * 192, oL[h]);
        ldw16(((h & 1) ? obo : obe) + h * 192, oL[h] >> 16);
      }
    }
    CBAR();
    if (c.l16 == 0) __hip_atomic_fetch_add((AS3 unsigned*)ldsp(L_FLAG + 8 + 4 * (rho >> 1)), 1u, __ATOMIC_RELAXED, __HIP_MEMORY_SCOPE_WORKGROUP);
    CBAR();
    PT(12);
  }
  proj_part<0>(c, hp, st, Op, rout, pbase);
  proj_part<3>(c, hp, st, Op, rout, pbase);
  PT(13);

  if (lw) {
    OPQ(c.l16);
    const unsigned n = (c.l16 >> 4) & 15, g = c.l16 >> 8;
    const unsigned lwl = 16 * lt + n;
    const bool lvalid = lwl < nW;
    const unsigned wi = win_index(base_x, jw, lwl >> 3, lwl & 7);
    const unsigned lpix = ((7 * (wi / NW1) + LEFT_I) * IMG + 7 * (wi % NW1) + LEFT_J) * PIXB;
    spin_until(L_FLAG + 8 + 4 * lt, lt == 0 ? 16u : n2);
    const unsigned rec = L_REC + (lvalid ? lwl : 16 * lt) * RECB;
    u32x4 oh[4], xp[3];
#pragma unroll
    for (int hd = 0; hd < 4; ++hd) oh[hd] = ldr128(rec + hd * 192 + 64 + g * 16);
#pragma unroll
    for (int s = 0; s < 3; ++s) xp[s] = ldr128(rec + 768 + g * 48 + s * 16);
    const u32x4 O3[3] = {u32x4{oh[0][0], oh[0][1], oh[0][2], oh[1][0]}, u32x4{oh[1][1], oh[1][2], oh[2][0], oh[2][1]}, u32x4{oh[2][2], oh[3][0], oh[3][1], oh[3][2]}};
#pragma unroll
    for (int ct = 0; ct < 6; ++ct) {
      const unsigned d0 = xp[ct >> 1][2 * (ct & 1)], d1 = xp[ct >> 1][2 * (ct & 1) + 1];
      f32x4 acc = {bf_lo(d0), bf_hi(d0), bf_lo(d1), bf_hi(d1)};
#pragma unroll
      for (int s = 0; s < 3; ++s) acc = mfma(ldr128(c.l16o + (ct * 3 + s) * 1024), O3[s], acc);
      const u32x2 o = {cvtpk(acc[0], acc[1]), cvtpk(acc[2], acc[3])};
      __builtin_amdgcn_raw_buffer_store_b64(o, rout, lvalid ? lpix + g * 8 + ct * 32 : 0x80000000u, 0, 0);
    }
  }
}

extern "C" void kernel_launch(void* const* d_in, const int* in_sizes, int n_in, void* d_out, int out_size, void* d_ws, size_t ws_size, hipStream_t stream) {
  (void)in_sizes; (void)n_in; (void)out_size; (void)d_ws; (void)ws_size;
  wattn_kernel<<<256, 512, 0, stream>>>((const uint16_t*)d_in[0], (const uint16_t*)d_in[1], (const uint16_t*)d_in[2], (const uint16_t*)d_in[3], (uint16_t*)d_out
  );
}
```

```cpp
#include <hip/hip_runtime.h>
#include <hip/hip_bf16.h>
#include <stdint.h>

typedef __bf16 bf16x8 __attribute__((ext_vector_type(8)));
typedef __bf16 bf16x2 __attribute__((ext_vector_type(2)));
typedef float f32x4 __attribute__((ext_vector_type(4)));
typedef float f32x2 __attribute__((ext_vector_type(2)));
typedef unsigned u32x4 __attribute__((ext_vector_type(4), may_alias));
typedef unsigned u32x2 __attribute__((ext_vector_type(2), may_alias));
typedef short s16x4 __attribute__((ext_vector_type(4), may_alias));
typedef unsigned short u16a __attribute__((may_alias));
typedef unsigned u32a __attribute__((may_alias));

#define DI __device__ __forceinline__
#define AS3 __attribute__((address_space(3)))

constexpr unsigned IMG = 511, NW1 = 73;
constexpr unsigned PIXB = 192, ROWB = IMG * PIXB;
constexpr unsigned XBYTES = IMG * IMG * PIXB;
constexpr unsigned XSHIFT = (IMG + 1) * PIXB;

constexpr unsigned L_WQKV = 0;
constexpr unsigned L_X2 = 49152;
constexpr unsigned L_WO = 55296;
constexpr unsigned L_TAB = 73728;
constexpr unsigned L_ID = 76800;
constexpr unsigned L_CONV = 78848;
constexpr unsigned CONVF = 544;
constexpr unsigned L_SLAB = 95168;
constexpr unsigned SLABB = 5440;
constexpr unsigned L_REC = 138688;
constexpr unsigned RECB = 960;
constexpr unsigned L_PS = 158848;
constexpr unsigned L_ZERO = 159872;
constexpr unsigned L_FLAG = 160512;
constexpr unsigned L_JUNK = 160768;
constexpr unsigned L_DWRAW = 161792;
constexpr unsigned L_TOTAL = L_DWRAW + 2048;
constexpr unsigned L_STAGE = L_SLAB + 6 * SLABB;
constexpr unsigned STOK = 1744;
constexpr unsigned TABO = L_TAB - L_WO;
constexpr unsigned IDO = L_ID - L_WO;

constexpr unsigned LROW = 38 * 16;
constexpr int LEFT_I = 2, LEFT_J = 3;

constexpr float SQC = 0.54266764f;
constexpr float ISQC = 1.0f / 0.54266764f;
constexpr float GUARD_HI = 1152921504606846976.0f;
constexpr float GUARD_LO = 1.0f / 1152921504606846976.0f;
constexpr unsigned ONES = 0x3f803f80u;

__shared__ __attribute__((aligned(16))) char g_lds[L_TOTAL];

DI f32x4 mfma(u32x4 a, u32x4 b, f32x4 c) {
  return __builtin_amdgcn_mfma_f32_16x16x32_bf16(__builtin_bit_cast(bf16x8, a), __builtin_bit_cast(bf16x8, b), c, 0, 0, 0);
}
DI unsigned cvtpk(float lo, float hi) {
  f32x2 v = {lo, hi};
  bf16x2 b = __builtin_convertvector(v, bf16x2);
  return __builtin_bit_cast(unsigned, b);
}
DI AS3 char* ldsp(unsigned off) { return (AS3 char*)g_lds + off; }
DI u32x4 ldr128(unsigned off) { return *(AS3 u32x4*)ldsp(off); }
DI unsigned ldr16(unsigned off) { return *(AS3 u16a*)ldsp(off); }
DI void ldw128(unsigned off, u32x4 v) { *(AS3 u32x4*)ldsp(off) = v; }
DI void ldw32(unsigned off, unsigned v) { *(AS3 u32a*)ldsp(off) = v; }
DI void ldw16(unsigned off, unsigned v) { *(AS3 u16a*)ldsp(off) = (unsigned short)v; }
DI float ex2(float x) { return __builtin_amdgcn_exp2f(x); }
DI float bf_lo(unsigned d) { return __builtin_bit_cast(float, d << 16); }
DI float bf_hi(unsigned d) { return __builtin_bit_cast(float, d & 0xffff0000u); }
DI unsigned rfl(unsigned v) { return __builtin_amdgcn_readfirstlane(v); }
#define CBAR() asm volatile("" ::: "memory")
#define OPQ(x) asm volatile("" : "+v"(x))
#define SB() __builtin_amdgcn_sched_barrier(0)
constexpr f32x4 Z4 = {0.f, 0.f, 0.f, 0.f};
#define PRIOMODE 5
#define PRIO_M()
#define PRIO_V()

#define PT(i)

DI unsigned tok_code(int t, unsigned n) {
  const unsigned long long lo = t == 0 ? 0x0c05180408010b00ull : t == 1 ? 0x1d16291519121c11ull : 0x343033262a232d22ull;
  const unsigned long long hi = t == 0 ? 0x0a0309020e100d06ull : t == 1 ? 0x1b141a2028211e24ull : 0x2c252b313632352eull;
  return (unsigned)(((n < 8 ? lo : hi) >> (8 * (n & 7))) & 0xff);
}

struct Lane {
  unsigned cA[3], cB[3];
  unsigned cW;
  unsigned l16;
  unsigned l16o;
  unsigned bperm;
  unsigned pw, pr;
  bool q0, q1, q2;
  bool lo2;
};

DI void dma16(unsigned m0v, unsigned voff, __amdgpu_buffer_rsrc_t srd, unsigned soff) {
  asm volatile("s_mov_b32 m0, %0\n\ts_nop 0\n\tbuffer_load_dwordx4 %1, %2, %3 offen lds" ::"s"(m0v), "v"(voff), "s"(srd), "s"(soff) : "m0", "memory");
}
DI void dma4(unsigned m0v, unsigned voff, __amdgpu_buffer_rsrc_t srd, unsigned soff) {
  asm volatile("s_mov_b32 m0, %0\n\ts_nop 0\n\tbuffer_load_dword %1, %2, %3 offen lds" ::"s"(m0v), "v"(voff), "s"(srd), "s"(soff) : "m0", "memory");
}
DI void dma16_last(unsigned m0v, unsigned voff, __amdgpu_buffer_rsrc_t srd, unsigned soff) {
  asm volatile("s_mov_b32 m0, %0\n\ts_mov_b64 exec, 0xfffff\n\tbuffer_load_dwordx4 %1, %2, %3 offen lds\n\ts_mov_b64 exec, -1" ::"s"(m0v), "v"(voff), "s"(srd), "s"(soff)
               : "m0", "memory");
}
DI void dma16_row(unsigned m0v, unsigned voff, __amdgpu_buffer_rsrc_t srd, unsigned soff) {
  asm volatile("s_mov_b32 m0, %0\n\ts_mov_b32 exec_hi, 0xf\n\tbuffer_load_dwordx4 %1, %2, %3 offen lds\n\ts_mov_b32 exec_hi, -1" ::"s"(m0v), "v"(voff), "s"(srd), "s"(soff)
               : "m0", "memory");
}
DI void dma_slab(const u32x4 t0, const u32x4 t1, unsigned m0base, __amdgpu_buffer_rsrc_t srd, unsigned soff, unsigned bm) {
  asm volatile("s_waitcnt lgkmcnt(0)" ::: "memory");
  const unsigned vo[6] = {t0[0], t0[1], t0[2], t0[3], t1[0], t1[1]};
  if (bm == 0) {
#pragma unroll
    for (int i = 0; i < 5; ++i) dma16(m0base + 1024u * i, vo[i], srd, soff);
    dma16_last(m0base + 5120u, vo[5], srd, soff);
  } else {
#pragma unroll
    for (int i = 0; i < 6; ++i) {
      const unsigned v = ((t1[2] >> (4 * i)) & bm) ? 0x80000000u : vo[i];
      if (i < 5) dma16(m0base + 1024u * i, v, srd, soff);
      else dma16_last(m0base + 5120u, v, srd, soff);
    }
  }
}
DI void dma_slab_tab(const Lane& c, unsigned m0base, __amdgpu_buffer_rsrc_t srd, unsigned soff, unsigned bm) {
  dma_slab(ldr128(c.l16o + TABO), ldr128(c.l16o + TABO + 1024), m0base, srd, soff, bm);
}
DI void l2_prefetch(unsigned pf0, unsigned pf1, unsigned m0junk, __amdgpu_buffer_rsrc_t srd, unsigned soff, unsigned bm) {
  if (bm == 0) {
    dma4(m0junk, pf0, srd, soff);
    dma4(m0junk, pf1, srd, soff);
  }
}

DI void first_slab(unsigned l16o, unsigned m0slab, unsigned m0junk, __amdgpu_buffer_rsrc_t rx, unsigned wbase, unsigned bm) {
  const u32x4 t1 = ldr128(l16o + TABO + 1024), t2 = ldr128(l16o + TABO + 2048);
  dma_slab(ldr128(l16o + TABO), t1, m0slab, rx, wbase, bm);
  l2_prefetch(t1[3], t2[0], m0junk, rx, wbase, bm);
}

template <int S>
DI void conv_slab(const Lane& c, f32x4 (&xa)[3][6]) {
  u32x4 A[3], B[3][3];
  auto load = [&](int gi) {
    const int hh = gi / 5, tp = gi % 5, ct = 2 * S + hh;
    A[gi % 3] = ldr128(c.cW + (ct * 5 + tp) * CONVF);
#pragma unroll
    for (int t = 0; t < 3; ++t) B[gi % 3][t] = ldr128((tp == 4 ? c.cB[t] : c.cA[t] + (tp == 3 ? LROW + 64u : 64u * tp)) + 32u * hh);
  };
  load(0);
  load(1);
#pragma unroll
  for (int gi = 0; gi < 10; ++gi) {
    if (gi + 2 < 10) load(gi + 2);
    const int hh = gi / 5, tp = gi % 5, ct = 2 * S + hh;
#pragma unroll
    for (int t = 0; t < 3; ++t) xa[t][ct] = mfma(A[gi % 3], B[gi % 3][t], tp == 0 ? Z4 : xa[t][ct]);
    SB();
  }
}

struct Stat { float rs, sg, mu; };
template <int NT>
DI void layer_norm(const Lane& c, const f32x4 (&xa)[NT][6], u32x4 (&x0p)[NT][3], u32x4 (&hp)[NT][3], Stat (&st)[NT]) {
  const u32x4 ones = {ONES, ONES, ONES, ONES};
  f32x4 sm[NT], sq[NT];
#pragma unroll
  for (int t = 0; t < NT; ++t) {
#pragma unroll
    for (int s = 0; s < 3; ++s) {
      x0p[t][s] = u32x4{cvtpk(xa[t][2 * s][0], xa[t][2 * s][1]), cvtpk(xa[t][2 * s][2], xa[t][2 * s][3]), cvtpk(xa[t][2 * s + 1][0], xa[t][2 * s + 1][1]),
                        cvtpk(xa[t][2 * s + 1][2], xa[t][2 * s + 1][3])};
      sq[t] = mfma(x0p[t][s], x0p[t][s], s == 0 ? Z4 : sq[t]);
      sm[t] = mfma(ones, x0p[t][s], s == 0 ? Z4 : sm[t]);
    }
    SB();
  }
  float d[NT];
#pragma unroll
  for (int t = 0; t < NT; ++t) {
    float v = sq[t][3];
    v = c.q2 ? sq[t][2] : v;
    v = c.q1 ? sq[t][1] : v;
    v = c.q0 ? sq[t][0] : v;
    d[t] = __builtin_bit_cast(float, __builtin_amdgcn_ds_bpermute((int)c.bperm, __builtin_bit_cast(int, v)));
  }
  SB();
#pragma unroll
  for (int t = 0; t < NT; ++t) {
    const float mu = sm[t][0] * (1.0f / 96.0f);
    float var = __builtin_fmaf(d[t], 1.0f / 96.0f, -mu * mu);
    var = __builtin_fmaxf(var, 0.f) + 1e-5f;
    const float rs = __builtin_amdgcn_rsqf(var);
    const float a = rs * SQC;
    const float b = -mu * a;
    st[t].rs = rs;
    st[t].sg = var * rs * ISQC;
    st[t].mu = mu;
#pragma unroll
    for (int s = 0; s < 3; ++s) {
      float h[8];
#pragma unroll
      for (int e = 0; e < 8; ++e) h[e] = __builtin_fmaf(xa[t][2 * s + (e >> 2)][e & 3], a, b);
      hp[t][s] = u32x4{cvtpk(h[0], h[1]), cvtpk(h[2], h[3]), cvtpk(h[4], h[5]), cvtpk(h[6], h[7])};
    }
  }
}

struct Rec {
  unsigned k3;
  unsigned q3[4];
  unsigned v3;
};

DI float grp_max(float m) {
  m = __builtin_fmaxf(m, __shfl_xor(m, 16));
  m = __builtin_fmaxf(m, __shfl_xor(m, 32));
  return m;
}
template <int CTRL>
DI float quad_bcast(float v) { return __builtin_bit_cast(float, __builtin_amdgcn_mov_dpp(__builtin_bit_cast(int, v), CTRL, 0xf, 0xf, true)); }

template <int H>
DI void norm_tile(const f32x4 O0, const f32x4 O1, float rs, u32x4 (&Op)[3], float& l_out) {
  constexpr int E = (H & 1) ? 2 : 0;
  const float l = O1[2 - E];
  l_out = l;
  const float f = __builtin_amdgcn_rcpf(l) * rs;
  Op[(3 * H) >> 2][(3 * H) & 3] = cvtpk(O0[0] * f, O0[1] * f);
  Op[(3 * H + 1) >> 2][(3 * H + 1) & 3] = cvtpk(O0[2] * f, O0[3] * f);
  Op[(3 * H + 2) >> 2][(3 * H + 2) & 3] = cvtpk(O1[E] * f, O1[E + 1] * f);
}
template <int H>
DI unsigned norm_left(const f32x4 L0, const f32x4 L1, float& l_out) {
  const float l = quad_bcast<(H & 1) ? 0x00 : 0xaa>(L1[0]);
  l_out = l;
  const float f = __builtin_amdgcn_rcpf(l) * ISQC;
  return cvtpk(L0[0] * f, L1[0] * f);
}
DI u32x4 pack8(const f32x4 a, const f32x4 b) { return u32x4{cvtpk(a[0], a[1]), cvtpk(a[2], a[3]), cvtpk(b[0], b[1]), cvtpk(b[2], b[3])}; }
DI f32x4 ex2x4(const f32x4 a) { return f32x4{ex2(a[0]), ex2(a[1]), ex2(a[2]), ex2(a[3])}; }

struct Heads {
  u32x4 F[3];
  unsigned Xp[6];
  f32x4 pO0, pO1;
  f32x4 pL0, pL1;
  float lmin, lmax;
};

template <int H>
DI void head_tail(Heads& hs, const Stat (&st)[3], u32x4 (&Op)[3][3], unsigned (&oL)[4], int part) {
  if (part == 0) {
    float l;
    norm_tile<H>(hs.pO0, hs.pO1, st[2].rs, Op[2], l);
    hs.lmin = __builtin_fminf(hs.lmin, l);
    hs.lmax = __builtin_fmaxf(hs.lmax, l);
  } else {
    float l;
    oL[H] = norm_left<H>(hs.pL0, hs.pL1, l);
    hs.lmin = __builtin_fminf(hs.lmin, l);
    hs.lmax = __builtin_fmaxf(hs.lmax, l);
  }
}

template <int H>
DI void head_fast(const Lane& c, const Rec& rc, const u32x4 (&hp)[3][3], const Stat (&st)[3], Heads& hs, u32x4 (&Op)[3][3], unsigned (&oL)[4]) {
  constexpr unsigned WB = L_WQKV + H * 12288;
  constexpr bool EVEN = (H & 1) == 0;
  u32x4(&F)[3] = hs.F;
  f32x4 T[3], U[3], W[3], Y[3];
  u32x4 Q[3], K[3], M[4], K3;
  PRIO_M();
#pragma unroll
  for (int s = 0; s < 3; ++s) {
#pragma unroll
    for (int t = 0; t < 3; ++t) T[t] = mfma(F[s], hp[t][s], s == 0 ? Z4 : T[t]);
    F[s] = ldr128(c.l16 + WB + 3072 + s * 1024);
    if (H > 0 && s < 2) head_tail<(H > 0 ? H - 1 : 0)>(hs, st, Op, oL, s);
    SB();
  }
#pragma unroll
  for (int s = 0; s < 3; ++s) {
#pragma unroll
    for (int t = 0; t < 3; ++t) U[t] = mfma(F[s], hp[t][s], s == 0 ? Z4 : U[t]);
    F[s] = ldr128(c.l16 + WB + 2 * 3072 + s * 1024);
    K[s][0] = cvtpk(T[s][0], T[s][1]);
    K[s][1] = cvtpk(T[s][2], T[s][3]);
    K[s][3] = 0u;
    SB();
  }
#pragma unroll
  for (int s = 0; s < 3; ++s) {
#pragma unroll
    for (int t = 0; t < 3; ++t) W[t] = mfma(F[s], hp[t][s], s == 0 ? Z4 : W[t]);
    F[s] = ldr128(c.l16 + WB + 3 * 3072 + s * 1024);
    M[s] = ldr128(rc.q3[s] + H * 192);
    if (s == 2) { M[3] = ldr128(rc.q3[3] + H * 192); K3 = ldr128(rc.k3 + H * 192); }
    Q[s][2] = cvtpk(U[s][0], U[s][1]);
    K[s][2] = cvtpk(U[s][2], U[s][3]);
    Q[s][3] = 0u;
    SB();
  }
  f32x4 DL = Z4;
  unsigned v3a, v3b;
#pragma unroll
  for (int s = 0; s < 3; ++s) {
#pragma unroll
    for (int t = 0; t < 3; ++t) Y[t] = mfma(hp[t][s], F[s], s == 0 ? Z4 : Y[t]);
    DL = mfma(M[s], K[s], DL);
    F[s] = EVEN ? ldr128(c.l16 + L_X2 + (H >> 1) * 3072 + s * 1024) : ldr128(c.l16 + (H < 3 ? WB + 12288 : 0) + s * 1024);
    Q[s][0] = cvtpk(W[s][0], W[s][1]);
    Q[s][1] = cvtpk(W[s][2], W[s][3]);
    if (s == 2) { v3a = ldr16(rc.v3 + H * 192); v3b = ldr16(rc.v3 + H * 192 + 32); }
    SB();
  }
  PRIO_V();
  f32x4 S0[4], S1[4], S2[4];
#pragma unroll
  for (int i = 0; i < 3; ++i) S0[i] = mfma(K[i], Q[0], Z4);
  S0[3] = mfma(K3, Q[0], Z4);
  DL = mfma(M[3], K3, DL);
  u32x4 Va0, Va1, Vb0, Vb1;
  Va0 = pack8(Y[0], Y[1]);
  SB();
#pragma unroll
  for (int i = 0; i < 3; ++i) S1[i] = mfma(K[i], Q[1], Z4);
  S1[3] = mfma(K3, Q[1], Z4);
  Va1 = u32x4{cvtpk(Y[2][0], Y[2][1]), cvtpk(Y[2][2], Y[2][3]), v3a, 0u};
  {
    const float p = ex2(DL[0]);
    ldw16(c.pw, cvtpk(p, p));
  }
  SB();
  u32x4 PL0, PL1;
  u32x4 P0, P1;
  if (EVEN) {
    f32x4 Z[3];
#pragma unroll
    for (int s = 0; s < 3; ++s) {
#pragma unroll
      for (int t = 0; t < 3; ++t) Z[t] = mfma(hp[t][s], F[s], s == 0 ? Z4 : Z[t]);
      F[s] = ldr128(c.l16 + WB + 12288 + s * 1024);
      S0[s] = ex2x4(S0[s]);
      if (s == 0) { PL0 = ldr128(c.pr); PL1 = ldr128(c.pr + 64); }
      if (s == 1) { P0 = pack8(S0[0], S0[1]); }
      SB();
    }
    const float p3 = ex2(S0[3][0]);
    P1 = u32x4{cvtpk(S0[2][0], S0[2][1]), cvtpk(S0[2][2], S0[2][3]), cvtpk(p3, p3), 0u};
#pragma unroll
    for (int i = 0; i < 3; ++i) S2[i] = mfma(K[i], Q[2], Z4);
    S2[3] = mfma(K3, Q[2], Z4);
    S1[0] = ex2x4(S1[0]);
    SB();
#pragma unroll
    for (int t = 0; t < 3; ++t) {
      hs.Xp[2 * t] = cvtpk(Z[t][0], Z[t][1]);
      hs.Xp[2 * t + 1] = cvtpk(Z[t][2], Z[t][3]);
    }
  } else {
    PL0 = ldr128(c.pr);
    PL1 = ldr128(c.pr + 64);
#pragma unroll
    for (int i = 0; i < 3; ++i) S2[i] = mfma(K[i], Q[2], Z4);
    S2[3] = mfma(K3, Q[2], Z4);
#pragma unroll
    for (int s = 0; s < 3; ++s) S0[s] = ex2x4(S0[s]);
    const float p3 = ex2(S0[3][0]);
    P0 = pack8(S0[0], S0[1]);
    P1 = u32x4{cvtpk(S0[2][0], S0[2][1]), cvtpk(S0[2][2], S0[2][3]), cvtpk(p3, p3), 0u};
    SB();
    S1[0] = ex2x4(S1[0]);
  }
  {
    const bool keep = EVEN ? c.lo2 : !c.lo2;
    Vb0 = u32x4{keep ? hs.Xp[0] : ONES, keep ? hs.Xp[1] : ONES, keep ? hs.Xp[2] : ONES, keep ? hs.Xp[3] : ONES};
    Vb1 = u32x4{keep ? hs.Xp[4] : ONES, keep ? hs.Xp[5] : ONES, v3b, 0u};
  }
  SB();
  f32x4 O0, O1;
  O0 = mfma(Va0, P0, Z4);
  O1 = mfma(Vb0, P0, Z4);
  O0 = mfma(Va1, P1, O0);
  O1 = mfma(Vb1, P1, O1);
  S1[1] = ex2x4(S1[1]);
  S1[2] = ex2x4(S1[2]);
  {
    const float p3 = ex2(S1[3][0]);
    P0 = pack8(S1[0], S1[1]);
    P1 = u32x4{cvtpk(S1[2][0], S1[2][1]), cvtpk(S1[2][2], S1[2][3]), cvtpk(p3, p3), 0u};
  }
  SB();
  f32x4 R0, R1;
  R0 = mfma(Va0, P0, Z4);
  R1 = mfma(Vb0, P0, Z4);
  R0 = mfma(Va1, P1, R0);
  R1 = mfma(Vb1, P1, R1);
  float la, lb;
  norm_tile<H>(O0, O1, st[0].rs, Op[0], la);
  S2[0] = ex2x4(S2[0]);
  SB();
  hs.pL0 = mfma(PL0, Va0, Z4);
  hs.pL1 = mfma(PL0, Vb0, Z4);
  hs.pL0 = mfma(PL1, Va1, hs.pL0);
  hs.pL1 = mfma(PL1, Vb1, hs.pL1);
  S2[1] = ex2x4(S2[1]);
  S2[2] = ex2x4(S2[2]);
  {
    const float p3 = ex2(S2[3][0]);
    P0 = pack8(S2[0], S2[1]);
    P1 = u32x4{cvtpk(S2[2][0], S2[2][1]), cvtpk(S2[2][2], S2[2][3]), cvtpk(p3, p3), 0u};
  }
  SB();
  hs.pO0 = mfma(Va0, P0, Z4);
  hs.pO1 = mfma(Vb0, P0, Z4);
  hs.pO0 = mfma(Va1, P1, hs.pO0);
  hs.pO1 = mfma(Vb1, P1, hs.pO1);
  norm_tile<H>(R0, R1, st[1].rs, Op[1], lb);
  hs.lmin = __builtin_fminf(__builtin_fminf(la, lb), hs.lmin);
  hs.lmax = __builtin_fmaxf(__builtin_fmaxf(la, lb), hs.lmax);
  SB();
}

template <int H>
DI void head_safe(const Lane& c, const Rec& rc, unsigned recq, const u32x4 (&hp)[3][3], const Stat (&st)[3], u32x4 (&Op)[3][3], unsigned (&oL)[4]) {
  constexpr unsigned WB = L_WQKV + H * 12288;
  f32x4 T[3], U[3], W[3], Y[3], Z[3];
#pragma unroll
  for (int s = 0; s < 3; ++s) {
    const u32x4 fk = ldr128(c.l16 + WB + s * 1024), fr = ldr128(c.l16 + WB + 3072 + s * 1024), fq = ldr128(c.l16 + WB + 6144 + s * 1024);
    const u32x4 fv = ldr128(c.l16 + WB + 9216 + s * 1024), fx = ldr128(c.l16 + L_X2 + (H >> 1) * 3072 + s * 1024);
#pragma unroll
    for (int t = 0; t < 3; ++t) {
      T[t] = mfma(fk, hp[t][s], s == 0 ? Z4 : T[t]);
      U[t] = mfma(fr, hp[t][s], s == 0 ? Z4 : U[t]);
      W[t] = mfma(fq, hp[t][s], s == 0 ? Z4 : W[t]);
      Y[t] = mfma(hp[t][s], fv, s == 0 ? Z4 : Y[t]);
      Z[t] = mfma(hp[t][s], fx, s == 0 ? Z4 : Z[t]);
    }
    SB();
  }
  u32x4 Q[3], K[4];
#pragma unroll
  for (int t = 0; t < 3; ++t) {
    Q[t] = u32x4{cvtpk(W[t][0], W[t][1]), cvtpk(W[t][2], W[t][3]), cvtpk(U[t][0], U[t][1]), 0u};
    K[t] = u32x4{cvtpk(T[t][0], T[t][1]), cvtpk(T[t][2], T[t][3]), cvtpk(U[t][2], U[t][3]), 0u};
  }
  K[3] = ldr128(rc.k3 + H * 192);
  const u32x4 Q3 = ldr128(recq + H * 192);
  const bool keep = (H & 1) == 0 ? c.lo2 : !c.lo2;
  const u32x4 Va0 = pack8(Y[0], Y[1]);
  const u32x4 Va1 = u32x4{cvtpk(Y[2][0], Y[2][1]), cvtpk(Y[2][2], Y[2][3]), ldr16(rc.v3 + H * 192), 0u};
  const u32x4 X0 = pack8(Z[0], Z[1]);
  const u32x4 Vb0 = u32x4{keep ? X0[0] : ONES, keep ? X0[1] : ONES, keep ? X0[2] : ONES, keep ? X0[3] : ONES};
  const u32x4 Vb1 = u32x4{keep ? cvtpk(Z[2][0], Z[2][1]) : ONES, keep ? cvtpk(Z[2][2], Z[2][3]) : ONES, ldr16(rc.v3 + H * 192 + 32), 0u};
  SB();
#pragma unroll
  for (int jq = 0; jq < 3; ++jq) {
    f32x4 S[4];
#pragma unroll
    for (int i = 0; i < 4; ++i) S[i] = mfma(K[i], Q[jq], Z4);
    float m = S[3][0];
#pragma unroll
    for (int i = 0; i < 3; ++i)
#pragma unroll
      for (int r = 0; r < 4; ++r) m = __builtin_fmaxf(m, S[i][r]);
    m = grp_max(m);
#pragma unroll
    for (int i = 0; i < 3; ++i)
#pragma unroll
      for (int r = 0; r < 4; ++r) S[i][r] = ex2(S[i][r] - m);
    const float p3 = ex2(S[3][0] - m);
    const u32x4 P0 = pack8(S[0], S[1]);
    const u32x4 P1 = u32x4{cvtpk(S[2][0], S[2][1]), cvtpk(S[2][2], S[2][3]), cvtpk(p3, p3), 0u};
    f32x4 O0 = mfma(Va0, P0, Z4), O1 = mfma(Vb0, P0, Z4);
    O0 = mfma(Va1, P1, O0);
    O1 = mfma(Vb1, P1, O1);
    float l;
    norm_tile<H>(O0, O1, st[jq].rs, Op[jq], l);
    OPQ(Op[jq][(3 * H) >> 2]);
    OPQ(Op[jq][(3 * H + 2) >> 2]);
    SB();
  }
  {
    f32x4 D[4];
#pragma unroll
    for (int i = 0; i < 4; ++i) D[i] = mfma(Q3, K[i], Z4);
    const unsigned g = (c.l16 >> 8) & 3;
    float s = D[3][0];
    s = g == 2 ? D[2][0] : s;
    s = g == 1 ? D[1][0] : s;
    s = g == 0 ? D[0][0] : s;
    float m = s;
#pragma unroll
    for (int o = 1; o < 64; o <<= 1) m = __builtin_fmaxf(m, __shfl_xor(m, o));
    const float p = ex2(s - m);
    ldw16(c.pw, cvtpk(p, p));
    const u32x4 PL0 = ldr128(c.pr), PL1 = ldr128(c.pr + 64);
    f32x4 L0 = mfma(PL0, Va0, Z4), L1 = mfma(PL0, Vb0, Z4);
    L0 = mfma(PL1, Va1, L0);
    L1 = mfma(PL1, Vb1, L1);
    float l;
    oL[H] = norm_left<H>(L0, L1, l);
    OPQ(oL[H]);
    SB();
  }
}

template <int CT0>
DI void proj_part(const Lane& c, const u32x4 (&hp)[3][3], const Stat (&st)[3], const u32x4 (&Op)[3][3], __amdgpu_buffer_rsrc_t rout, unsigned wbase) {
  u32x4 A[3], I[2];
  I[0] = ldr128(c.l16o + IDO);
  I[1] = ldr128(c.l16o + IDO + 1024);
#pragma unroll
  for (int s = 0; s < 3; ++s) A[s] = ldr128(c.l16o + (CT0 * 3 + s) * 1024);
  const u32x4 t2 = ldr128(c.l16o + TABO + 2048);
  f32x4 acc[2][3];
#pragma unroll
  for (int k = 0; k < 4; ++k) {
    const int ct = CT0 + k;
    if (k < 3) {
#pragma unroll
      for (int t = 0; t < 3; ++t) acc[k & 1][t] = mfma(I[ct & 1], hp[t][ct >> 1], Z4);
#pragma unroll
      for (int s = 0; s < 3; ++s) {
#pragma unroll
        for (int t = 0; t < 3; ++t) acc[k & 1][t] = mfma(A[s], Op[t][s], acc[k & 1][t]);
        if (k < 2) A[s] = ldr128(c.l16o + ((ct + 1) * 3 + s) * 1024);
      }
    }
    if (k > 0) {
#pragma unroll
      for (int t = 0; t < 3; ++t) {
        const f32x4 a = acc[(k - 1) & 1][t];
        const float sg = st[t].sg, mu = st[t].mu;
        const u32x2 o = {cvtpk(__builtin_fmaf(a[0], sg, mu), __builtin_fmaf(a[1], sg, mu)), cvtpk(__builtin_fmaf(a[2], sg, mu), __builtin_fmaf(a[3], sg, mu))};
        __builtin_amdgcn_raw_buffer_store_b64(o, rout, t2[1 + t] + (ct - 1) * 32, wbase, 0);
      }
    }
    SB();
  }
}

DI unsigned win_index(unsigned base_x, unsigned j, unsigned rho, unsigned w) { return base_x + (rho < 2 ? rho * 256 + j * 8 + w : 512 + 32 * w + j); }

DI void spin_until(unsigned off, unsigned target) {
  while (rfl(*(volatile AS3 unsigned*)ldsp(off)) < target) __builtin_amdgcn_s_sleep(2);
  CBAR();
}

DI u32x4 tr_pair(unsigned a0, unsigned a1) {
  const s16x4 lo = __builtin_amdgcn_ds_read_tr16_b64_v4i16((AS3 s16x4*)ldsp(a0));
  const s16x4 hi = __builtin_amdgcn_ds_read_tr16_b64_v4i16((AS3 s16x4*)ldsp(a1));
  const u32x2 l2 = __builtin_bit_cast(u32x2, lo), h2 = __builtin_bit_cast(u32x2, hi);
  return u32x4{l2[0], l2[1], h2[0], h2[1]};
}
DI void lds_barrier() {
  asm volatile("s_waitcnt lgkmcnt(0)" ::: "memory");
  __builtin_amdgcn_s_barrier();
  CBAR();
}
DI void tr_plain(u32x4& f0, u32x4& f1, u32x4& f2, unsigned lane, unsigned colb) {
  const unsigned la = (lane >> 2) * 576 + (lane & 3) * 8 + colb * 2;
  f0 = tr_pair(la, la + 16 * 576);
  f1 = tr_pair(la + 32 * 576, la + 48 * 576);
  f2 = tr_pair(la + 64 * 576, la + 80 * 576);
}
DI unsigned wo_row(unsigned sq, unsigned g) {
  const unsigned hh = (sq * 43) >> 8, ix = sq - 6 * hh;
  return (hh * 24 + (ix < 4 ? 4 * g + ix : 12 + 2 * g + ix)) * 192;
}
DI void tr_wo(u32x4& f0, u32x4& f1, u32x4& f2, unsigned lane, unsigned ct) {
  const unsigned r = (lane >> 2) & 3, g = lane >> 4, b = L_WO + ct * 32 + (lane & 3) * 8;
  f0 = tr_pair(b + wo_row(r, g), b + wo_row(4 + r, g));
  f1 = tr_pair(b + wo_row(8 + r, g), b + wo_row(12 + r, g));
  f2 = tr_pair(b + wo_row(16 + r, g), b + wo_row(20 + r, g));
}
DI u32x4 gather1(unsigned ga) {
  u32x4 f;
#pragma unroll
  for (int d = 0; d < 4; ++d) {
    const unsigned k = 16 * (d >> 1) + 2 * (d & 1);
    f[d] = ldr16(ga + k * 576) | (ldr16(ga + (k + 1) * 576) << 16);
  }
  return f;
}
DI void gather(u32x4& f0, u32x4& f1, u32x4& f2, unsigned g, unsigned col) {
  const unsigned ga = 4 * g * 576 + col * 2;
  f0 = gather1(ga);
  f1 = gather1(ga + 32 * 576);
  f2 = gather1(ga + 64 * 576);
}

__global__ __launch_bounds__(512, 2) __attribute__((target("no-packed-fp32-ops")))
void wattn_kernel(const uint16_t* __restrict__ x, const uint16_t* __restrict__ dw, const uint16_t* __restrict__ wqkv, const uint16_t* __restrict__ wo,
                  uint16_t* __restrict__ out
                  ) {
  const unsigned tid = threadIdx.x;
  const unsigned wv = rfl(tid >> 6);
  Lane c;
  const unsigned lane = tid & 63, n = lane & 15, g = lane >> 4;
  const unsigned xcd = blockIdx.x & 7, jw = blockIdx.x >> 3;
  const unsigned base_x = xcd * 666 + (xcd ? 1 : 0), cnt_x = 666 + (xcd == 0 ? 1 : 0);
  const unsigned n2 = 4 + ((640 + jw < cnt_x) ? 1 : 0);
  const unsigned nW = 16 + n2;
  const unsigned nwin = wv < n2 ? 3 : 2;
  const unsigned lds0 = (unsigned)(size_t)(AS3 char*)g_lds;
  const unsigned slab = L_SLAB + wv * SLABB;

  const __amdgpu_buffer_rsrc_t rx = __builtin_amdgcn_make_buffer_rsrc((void*)((const char*)x - XSHIFT), (short)0, (int)(XBYTES + XSHIFT), 0x00020000);
  const __amdgpu_buffer_rsrc_t rw = __builtin_amdgcn_make_buffer_rsrc((void*)wqkv, (short)0, 96 * 288 * 2, 0x00020000);
  const __amdgpu_buffer_rsrc_t ro = __builtin_amdgcn_make_buffer_rsrc((void*)wo, (short)0, 96 * 96 * 2, 0x00020000);
  const __amdgpu_buffer_rsrc_t rout = __builtin_amdgcn_make_buffer_rsrc((void*)out, (short)0, (int)XBYTES, 0x00020000);

  PT(16);
  const __amdgpu_buffer_rsrc_t rd = __builtin_amdgcn_make_buffer_rsrc((void*)dw, (short)0, 9 * 96 * 2, 0x00020000);
  {
    const unsigned l16 = lane * 16;
#pragma unroll
    for (unsigned i = 0; i < 9; ++i) {
      const unsigned pc = wv + 8 * i;
      if (i < 6 || pc < 54) dma16(lds0 + pc * 1024, l16, rw, pc * 1024);
      else dma16(lds0 + pc * 1024, l16, ro, (pc - 54) * 1024);
    }
    if (wv == 5) {
      dma16(lds0 + L_DWRAW, l16, rd, 0);
      dma16(lds0 + L_DWRAW + 1024, l16, rd, 1024);
    }
  }
  PT(17);
  const unsigned m0slab = rfl(lds0 + slab), m0junk = rfl(lds0 + L_JUNK);
  unsigned widx = win_index(base_x, jw, 0, wv);
  unsigned wr = widx / NW1, wc = widx % NW1;
  unsigned wbase = (7 * wr * IMG + 7 * wc) * PIXB;
  unsigned bm = (wr == 0 ? 1u : 0u) | (wr == NW1 - 1 ? 2u : 0u) | (wc == 0 ? 4u : 0u) | (wc == NW1 - 1 ? 8u : 0u);
  unsigned pf0 = 0, pf1 = 0;
  if (wv < 4) {
    unsigned vo[6], vflag = 0, pf[2];
#pragma unroll
    for (int i = 0; i < 6; ++i) {
      const unsigned sl = 64 * i + lane, r = sl / 38, t = sl % 38, cc = t >> 2, q = t & 3;
      const bool ok = t < 36 && sl < 340;
      vo[i] = ok ? (r * IMG + cc) * PIXB + q * 16 : 0x80000000u;
      vflag |= ((r == 0 ? 1u : 0u) | (r == 8 ? 2u : 0u) | (cc == 0 ? 4u : 0u) | (cc == 8 ? 8u : 0u)) << (4 * i);
    }
#pragma unroll
    for (int i = 0; i < 2; ++i) {
      const unsigned p = 64 * i + lane, r = p / 9, cc = p % 9;
      pf[i] = p < 81 ? (r * IMG + cc) * PIXB + 128 : 0x80000000u;
    }
    pf0 = pf[0]; pf1 = pf[1];
    PT(18);
    dma_slab(u32x4{vo[0], vo[1], vo[2], vo[3]}, u32x4{vo[4], vo[5], vflag, pf[0]}, m0slab, rx, wbase, bm);
    if (wv == 3) {
      ldw128(L_TAB + lane * 16, u32x4{vo[0], vo[1], vo[2], vo[3]});
      ldw128(L_TAB + 1024 + lane * 16, u32x4{vo[4], vo[5], vflag, pf[0]});
      ldw32(L_TAB + 2048 + lane * 16, pf[1]);
    }
  }
  PT(19);
  const bool lw = wv >= 6;
  const unsigned lt = wv == 7 ? 0 : 1;
  const unsigned lwl = 16 * lt + n;
  const bool lvalid = lwl < nW;
  unsigned lpix = 0;
  if (lw) {
    const unsigned wl = lvalid ? lwl : 0;
    const unsigned wi = win_index(base_x, jw, wl >> 3, wl & 7);
    const unsigned r = wi / NW1, cc = wi % NW1;
    lpix = ((7 * r + LEFT_I) * IMG + 7 * cc + LEFT_J) * PIXB;
    if (wv == 7) {
      const unsigned l16 = lane * 16;
#pragma unroll
      for (int t = 0; t < 16; ++t) {
        const unsigned pb = __builtin_amdgcn_readlane(lpix, t);
#pragma unroll
        for (int r3 = 0; r3 < 3; ++r3) dma16_row(lds0 + L_STAGE + t * STOK + r3 * 576, l16, rx, pb + r3 * ROWB);
      }
    }
  }

  PT(21);
  {
    unsigned ost[3];
#pragma unroll
    for (int t = 0; t < 3; ++t) {
      const unsigned code = tok_code(t, n), i = code >> 3, j = code & 7;
      c.cA[t] = slab + LROW * (i + (g >> 1)) + 64 * j + 16 * (g & 1);
      c.cB[t] = slab + LROW * (i + 2) + 64 * (j + 2 * (g >> 1)) + 16 * (g & 1);
      ost[t] = (i * IMG + j) * PIXB + g * 8;
    }
    if (wv == 4) {
      const unsigned one = (n >> 2) == g ? 0x3f80u << (16 * (n & 1)) : 0u;
      const unsigned d0 = (n & 2) ? 0u : one, d1 = (n & 2) ? one : 0u;
      ldw128(L_ID + lane * 16, u32x4{d0, d1, 0u, 0u});
      ldw128(L_ID + 1024 + lane * 16, u32x4{0u, 0u, d0, d1});
    }
    if (wv == 5) {
#pragma unroll
      for (int t = 0; t < 3; ++t) ldw32(L_TAB + 2048 + lane * 16 + 4 + 4 * t, ost[t]);
    }
  }
  {
    const bool act = (g & 1) == (n >> 3);
    const unsigned G = 2 * (g >> 1) + ((n >> 2) & 1), idx = 4 * (g & 1) + (n & 3);
    const unsigned k = (G == 0 ? 0 : G == 1 ? 9 : G == 2 ? 17 : 26) + idx;
    c.cW = L_CONV + 16 * (act ? k : (g < 2 ? 8 : 25));
  }
  c.l16 = lane * 16; c.l16o = L_WO + lane * 16;
  c.bperm = 4 * ((n >> 2) * 16 + n);
  c.pw = lane < 49 ? L_PS + wv * 128 + (g >> 1) * 64 + (n >> 2) * 16 + (g & 1) * 8 + (n & 3) * 2 : L_JUNK + lane * 2;
  c.pr = L_PS + wv * 128 + g * 16;
  c.q0 = (n & 3) == 0; c.q1 = (n & 3) == 1; c.q2 = (n & 3) == 2; c.lo2 = (n & 3) < 2;
  if (tid < (L_FLAG + 32 - L_PS) / 4) ldw32(L_PS + tid * 4, 0u);
  unsigned cva[2], cve[2];
#pragma unroll
  for (int i = 0; i < 2; ++i) {
    const unsigned sl0 = tid + 512 * i, sl = sl0 < 1020 ? sl0 : 8;
    const unsigned f = sl / 34, k = sl % 34, ct = f / 5, tp = f % 5;
    const unsigned G = (k > 8 ? 1u : 0u) + (k > 16 ? 1u : 0u) + (k > 25 ? 1u : 0u);
    const unsigned idx = (k - ((0x1a110900u >> (8 * G)) & 0xff)) & 7;
    const unsigned hi = G >> 1, m = 8 * (idx >> 2) + 4 * (G & 1) + (idx & 3);
    const unsigned tap = (unsigned)(0x8674524130ull >> (4 * (2 * tp + hi))) & 15;
    cva[i] = L_DWRAW + (tap * 96 + 16 * ct + m) * 2;
    cve[i] = (k == 8 || k == 25) ? 8u : ((m & 7) | ((tp == 3 && hi == 0) ? 16u : 0u));
  }
  PT(0);
  if (wv == 7) asm volatile("s_waitcnt vmcnt(48) lgkmcnt(0)" ::: "memory");
  else if (wv < 4) asm volatile("s_waitcnt vmcnt(6) lgkmcnt(0)" ::: "memory");
  else asm volatile("s_waitcnt vmcnt(0) lgkmcnt(0)" ::: "memory");
  __builtin_amdgcn_s_barrier();
  CBAR();
  PT(1);
  if (wv < 4) l2_prefetch(pf0, pf1, m0junk, rx, wbase, bm);
  {
    const unsigned hd = wv & 3;
    u32x4 fa0, fa1, fa2, fb0, fb1, fb2, fc0, fc1, fc2;
    const unsigned rcol = 16 + 2 * (n >> 2) + (n & 1);
    unsigned da, db, dc;
    tr_plain(fa0, fa1, fa2, lane, (wv < 4 ? 96 : 192) + 24 * hd);
    da = hd * 12288 + (wv < 4 ? 0 : 9216);
    if (wv < 4) {
      gather(fb0, fb1, fb2, g, (c.lo2 ? 0 : 96) + 24 * hd + rcol);
      tr_plain(fc0, fc1, fc2, lane, 24 * hd);
      db = hd * 12288 + 3072;
      dc = hd * 12288 + 6144;
    } else {
      if ((wv & 1) == 0) {
        gather(fb0, fb1, fb2, g, 192 + (hd + (c.lo2 ? 0 : 1)) * 24 + rcol);
        db = L_X2 + (hd >> 1) * 3072;
      } else {
        tr_wo(fb0, fb1, fb2, lane, wv == 5 ? 1 : 4);
        db = L_WO + (wv == 5 ? 1 : 4) * 3072;
      }
      const unsigned ctc = wv == 4 ? 0 : wv == 5 ? 2 : wv == 6 ? 3 : 5;
      tr_wo(fc0, fc1, fc2, lane, ctc);
      dc = L_WO + ctc * 3072;
    }
#pragma unroll
    for (int i = 0; i < 2; ++i) {
      const unsigned e = cve[i];
      const unsigned w = (e & 16) ? 0x3f80u : ldr16(cva[i]);
      const unsigned dd = w << (16 * (e & 1)), h = (e >> 1) & 7;
      const u32x4 v = {h == 0 ? dd : 0u, h == 1 ? dd : 0u, h == 2 ? dd : 0u, h == 3 ? dd : 0u};
      if (i == 0 || tid < 508) ldw128(L_CONV + (tid + 512 * i) * 16, v);
    }
    lds_barrier();
    da += lane * 16; db += lane * 16; dc += lane * 16;
    ldw128(da, fa0); ldw128(da + 1024, fa1); ldw128(da + 2048, fa2);
    ldw128(db, fb0); ldw128(db + 1024, fb1); ldw128(db + 2048, fb2);
    ldw128(dc, fc0); ldw128(dc + 1024, fc1); ldw128(dc + 2048, fc2);
    lds_barrier();
  }
  PT(2);

  if (wv == 4 || wv == 5) first_slab(c.l16o, m0slab, m0junk, rx, wbase, bm);
  if (lw) {
    __builtin_amdgcn_s_setprio(2);
    f32x4 xa[1][6];
    if (wv == 7) {
      asm volatile("s_waitcnt vmcnt(0)" ::: "memory");
      const unsigned hi = g >> 1, sb = L_STAGE + n * STOK + (g & 1) * 16;
      const unsigned a0 = sb + hi * 576, a3 = sb + (hi + 1) * 576 + 192, a4 = sb + 2 * 576 + 2 * hi * 192;
#pragma unroll
      for (int ct = 0; ct < 6; ++ct) {
        u32x4 A[5], B[5];
#pragma unroll
        for (int tp = 0; tp < 5; ++tp) {
          A[tp] = ldr128(c.cW + (ct * 5 + tp) * CONVF);
          B[tp] = ldr128((tp < 3 ? a0 + tp * 192 : tp == 3 ? a3 : a4) + ct * 32);
        }
#pragma unroll
        for (int tp = 0; tp < 5; ++tp) xa[0][ct] = mfma(A[tp], B[tp], tp == 0 ? Z4 : xa[0][ct]);
      }
#pragma unroll
      for (int ct = 0; ct < 6; ++ct) OPQ(xa[0][ct]);
      asm volatile("s_waitcnt lgkmcnt(0)" ::: "memory");
      if (lane == 0) *(volatile AS3 unsigned*)ldsp(L_FLAG + 16) = 1u;
      CBAR();
      first_slab(c.l16o, m0slab, m0junk, rx, wbase, bm);
    } else {
      const unsigned hi = g >> 1;
      const char* px = (const char*)x + lpix + (g & 1) * 16;
      const char* p0 = px + ((int)hi - 1) * (int)ROWB - (int)PIXB;
      const char* p3 = px + hi * ROWB;
      const char* p4 = px + ROWB + (2 * (int)hi - 1) * (int)PIXB;
      u32x4 lb[30];
#pragma unroll
      for (int f = 0; f < 30; ++f) {
        const int tp = f % 5;
        const char* p = tp < 3 ? p0 + tp * PIXB : tp == 3 ? p3 : p4;
        lb[f] = *(const u32x4*)(p + (f / 5) * 32);
      }
#pragma unroll
      for (int tp = 0; tp < 5; ++tp)
#pragma unroll
        for (int ct = 0; ct < 6; ++ct) xa[0][ct] = mfma(ldr128(c.cW + (ct * 5 + tp) * CONVF), lb[ct * 5 + tp], tp == 0 ? Z4 : xa[0][ct]);
#pragma unroll
      for (int ct = 0; ct < 6; ++ct) OPQ(xa[0][ct]);
      spin_until(L_FLAG + 16, 1u);
      first_slab(c.l16o, m0slab, m0junk, rx, wbase, bm);
    }
    u32x4 x0p[1][3], hp[1][3];
    Stat st1[1];
    layer_norm<1>(c, xa, x0p, hp, st1);
    const unsigned rec = lvalid ? L_REC + lwl * RECB : L_JUNK;
#pragma unroll
    for (int s = 0; s < 3; ++s) ldw128(rec + 768 + g * 48 + s * 16, x0p[0][s]);
    unsigned recv[4];
#pragma unroll
    for (int r = 0; r < 4; ++r) {
      const unsigned wl = 16 * lt + 4 * g + r;
      recv[r] = (wl < nW ? L_REC + wl * RECB : L_JUNK) + 128 + n * 2;
    }
#pragma unroll
    for (int p = 0; p < 2; ++p) {
      f32x4 Z = Z4;
#pragma unroll
      for (int s = 0; s < 3; ++s) Z = mfma(hp[0][s], ldr128(c.l16 + L_X2 + p * 3072 + s * 1024), Z);
#pragma unroll
      for (int hh = 0; hh < 2; ++hh) {
        const int hd = 2 * p + hh;
        const unsigned wb = c.l16 + L_WQKV + hd * 12288;
        f32x4 T = Z4, U = Z4, W = Z4, Y = Z4;
#pragma unroll
        for (int s = 0; s < 3; ++s) {
          T = mfma(ldr128(wb + s * 1024), hp[0][s], T);
          U = mfma(ldr128(wb + 3072 + s * 1024), hp[0][s], U);
          W = mfma(ldr128(wb + 6144 + s * 1024), hp[0][s], W);
          Y = mfma(hp[0][s], ldr128(wb + 9216 + s * 1024), Y);
        }
        ldw128(rec + hd * 192 + g * 16, u32x4{cvtpk(T[0], T[1]), cvtpk(T[2], T[3]), cvtpk(U[2], U[3]), 0u});
        ldw128(rec + hd * 192 + 64 + g * 16, u32x4{cvtpk(W[0], W[1]), cvtpk(W[2], W[3]), cvtpk(U[0], U[1]), 0u});
        const bool keep = hh == 0 ? c.lo2 : !c.lo2;
#pragma unroll
        for (int r = 0; r < 4; ++r) {
          ldw16(recv[r] + hd * 192, cvtpk(Y[r], Y[r]));
          ldw16(recv[r] + hd * 192 + 32, keep ? cvtpk(Z[r], Z[r]) : ONES);
        }
      }
    }
    asm volatile("s_waitcnt lgkmcnt(0)" ::: "memory");
    if (lane == 0) *(volatile AS3 unsigned*)ldsp(L_FLAG + 4 * lt) = 1u;
    CBAR();
    __builtin_amdgcn_s_setprio(0);
  }
  PT(3);

  if (wv >= 4 && nwin == 3) __builtin_amdgcn_s_setprio(1);
  u32x4 hp[3][3], Op[3][3];
  Stat st[3];
  unsigned pbase = 0;
#pragma unroll 1
  for (unsigned rho = 0; rho < nwin; ++rho) {
    f32x4 xa[3][6];
#pragma unroll
    for (int t = 0; t < 3; ++t) { OPQ(c.cA[t]); OPQ(c.cB[t]); }
    OPQ(c.cW); OPQ(c.l16); OPQ(c.l16o); OPQ(c.pw); OPQ(c.pr);
    asm volatile("s_waitcnt vmcnt(0)" ::: "memory");
    PT(4);
    PRIO_M();
    conv_slab<0>(c, xa);
    PT(5);
    dma_slab_tab(c, m0slab, rx, wbase + 64, bm);
    if (rho > 0) {
      proj_part<0>(c, hp, st, Op, rout, pbase);
      asm volatile("s_waitcnt vmcnt(9)" ::: "memory");
    } else {
      asm volatile("s_waitcnt vmcnt(0)" ::: "memory");
    }
    PT(6);
    conv_slab<1>(c, xa);
    PT(7);
    dma_slab_tab(c, m0slab, rx, wbase + 128, bm);
    if (rho > 0) {
      proj_part<3>(c, hp, st, Op, rout, pbase);
      asm volatile("s_waitcnt vmcnt(9)" ::: "memory");
    } else {
      asm volatile("s_waitcnt vmcnt(0)" ::: "memory");
    }
    PT(8);
    conv_slab<2>(c, xa);
    pbase = wbase;
    const unsigned wl = rho * 8 + wv;
    if (rho + 1 < nwin) {
      widx = win_index(base_x, jw, rho + 1, wv);
      wr = widx / NW1; wc = widx % NW1;
      wbase = (7 * wr * IMG + 7 * wc) * PIXB;
      bm = (wr == 0 ? 1u : 0u) | (wr == NW1 - 1 ? 2u : 0u) | (wc == 0 ? 4u : 0u) | (wc == NW1 - 1 ? 8u : 0u);
      const u32x4 t1 = ldr128(c.l16o + TABO + 1024), t2 = ldr128(c.l16o + TABO + 2048);
      dma_slab(ldr128(c.l16o + TABO), t1, m0slab, rx, wbase, bm);
      l2_prefetch(t1[3], t2[0], m0junk, rx, wbase, bm);
    }
    PT(9);
    PRIO_V();
    Heads hs;
#pragma unroll
    for (int s = 0; s < 3; ++s) hs.F[s] = ldr128(c.l16 + L_WQKV + s * 1024);
    u32x4 x0p[3][3];
    layer_norm<3>(c, xa, x0p, hp, st);
    PT(10);
    if (rho != 1) spin_until(L_FLAG + 4 * (rho >> 1), 1u);
    PT(11);
    const unsigned rec = L_REC + wl * RECB;
    const unsigned lnn = (c.l16 >> 4) & 15, lgg = c.l16 >> 8;
    Rec rc;
    rc.k3 = rec + lgg * 16;
#pragma unroll
    for (int i = 0; i < 4; ++i) rc.q3[i] = (lnn >> 2) == (unsigned)i ? rc.k3 + 64 : L_ZERO;
    rc.v3 = lgg == 0 ? rec + 128 + lnn * 2 : L_ZERO;
    hs.lmin = 1.0f; hs.lmax = 1.0f;
    unsigned oL[4];
    if (rho == 1) __builtin_amdgcn_s_setprio(0);
    SB();
    head_fast<0>(c, rc, hp, st, hs, Op, oL);
    head_fast<1>(c, rc, hp, st, hs, Op, oL);
    head_fast<2>(c, rc, hp, st, hs, Op, oL);
    head_fast<3>(c, rc, hp, st, hs, Op, oL);
    head_tail<3>(hs, st, Op, oL, 0);
    head_tail<3>(hs, st, Op, oL, 1);
    const bool bad = !(hs.lmax < GUARD_HI) || !(hs.lmin > GUARD_LO);
    if (__builtin_expect(__builtin_amdgcn_ballot_w64(bad) != 0, 0)) {
      head_safe<0>(c, rc, rc.k3 + 64, hp, st, Op, oL);
      head_safe<1>(c, rc, rc.k3 + 64, hp, st, Op, oL);
      head_safe<2>(c, rc, rc.k3 + 64, hp, st, Op, oL);
      head_safe<3>(c, rc, rc.k3 + 64, hp, st, Op, oL);
    }
    {
      const unsigned oa = rec + 64 + (lnn >> 2) * 16 + (lnn & 3) * 2;
      const unsigned ob = rec + 64 + (lnn >> 2) * 16 + 8 + (lnn & 1) * 2;
      const unsigned obe = c.lo2 ? ob : L_JUNK, obo = c.lo2 ? L_JUNK : ob;
#pragma unroll
      for (int h = 0; h < 4; ++h) {
        ldw16(oa + h * 192, oL[h]);
        ldw16(((h & 1) ? obo : obe) + h * 192, oL[h] >> 16);
      }
    }
    CBAR();
    if (c.l16 == 0) __hip_atomic_fetch_add((AS3 unsigned*)ldsp(L_FLAG + 8 + 4 * (rho >> 1)), 1u, __ATOMIC_RELAXED, __HIP_MEMORY_SCOPE_WORKGROUP);
    CBAR();
    PT(12);
  }
  proj_part<0>(c, hp, st, Op, rout, pbase);
  proj_part<3>(c, hp, st, Op, rout, pbase);
  PT(13);

  if (lw) {
    OPQ(c.l16);
    const unsigned n = (c.l16 >> 4) & 15, g = c.l16 >> 8;
    const unsigned lwl = 16 * lt + n;
    const bool lvalid = lwl < nW;
    const unsigned wi = win_index(base_x, jw, lwl >> 3, lwl & 7);
    const unsigned lpix = ((7 * (wi / NW1) + LEFT_I) * IMG + 7 * (wi % NW1) + LEFT_J) * PIXB;
    spin_until(L_FLAG + 8 + 4 * lt, lt == 0 ? 16u : n2);
    const unsigned rec = L_REC + (lvalid ? lwl : 16 * lt) * RECB;
    u32x4 oh[4], xp[3];
#pragma unroll
    for (int hd = 0; hd < 4; ++hd) oh[hd] = ldr128(rec + hd * 192 + 64 + g * 16);
#pragma unroll
    for (int s = 0; s < 3; ++s) xp[s] = ldr128(rec + 768 + g * 48 + s * 16);
    const u32x4 O3[3] = {u32x4{oh[0][0], oh[0][1], oh[0][2], oh[1][0]}, u32x4{oh[1][1], oh[1][2], oh[2][0], oh[2][1]}, u32x4{oh[2][2], oh[3][0], oh[3][1], oh[3][2]}};
#pragma unroll
    for (int ct = 0; ct < 6; ++ct) {
      const unsigned d0 = xp[ct >> 1][2 * (ct & 1)], d1 = xp[ct >> 1][2 * (ct & 1) + 1];
      f32x4 acc = {bf_lo(d0), bf_hi(d0), bf_lo(d1), bf_hi(d1)};
#pragma unroll
      for (int s = 0; s < 3; ++s) acc = mfma(ldr128(c.l16o + (ct * 3 + s) * 1024), O3[s], acc);
      const u32x2 o = {cvtpk(acc[0], acc[1]), cvtpk(acc[2], acc[3])};
      __builtin_amdgcn_raw_buffer_store_b64(o, rout, lvalid ? lpix + g * 8 + ct * 32 : 0x80000000u, 0, 0);
    }
  }
}

extern "C" void kernel_launch(void* const* d_in, const int* in_sizes, int n_in, void* d_out, int out_size, void* d_ws, size_t ws_size, hipStream_t stream) {
  (void)in_sizes; (void)n_in; (void)out_size; (void)d_ws; (void)ws_size;
  wattn_kernel<<<256, 512, 0, stream>>>((const uint16_t*)d_in[0], (const uint16_t*)d_in[1], (const uint16_t*)d_in[2], (const uint16_t*)d_in[3], (uint16_t*)d_out
  );
}
```

```cpp
#include <hip/hip_runtime.h>
#include <hip/hip_bf16.h>
#include <stdint.h>

typedef __bf16 bf16x8 __attribute__((ext_vector_type(8)));
typedef __bf16 bf16x2 __attribute__((ext_vector_type(2)));
typedef float f32x4 __attribute__((ext_vector_type(4)));
typedef float f32x2 __attribute__((ext_vector_type(2)));
typedef unsigned u32x4 __attribute__((ext_vector_type(4), may_alias));
typedef unsigned u32x2 __attribute__((ext_vector_type(2), may_alias));
typedef short s16x4 __attribute__((ext_vector_type(4), may_alias));
typedef unsigned short u16a __attribute__((may_alias));
typedef unsigned u32a __attribute__((may_alias));

#define DI __device__ __forceinline__
#define AS3 __attribute__((address_space(3)))

#define PFBYTES 47000
constexpr unsigned IMG = 511, NW1 = 73;
constexpr unsigned PIXB = 192, ROWB = IMG * PIXB;
constexpr unsigned XBYTES = IMG * IMG * PIXB;
constexpr unsigned XSHIFT = (IMG + 1) * PIXB;

constexpr unsigned L_WQKV = 0;
constexpr unsigned L_X2 = 49152;
constexpr unsigned L_WO = 55296;
constexpr unsigned L_TAB = 73728;
constexpr unsigned L_ID = 76800;
constexpr unsigned L_CONV = 78848;
constexpr unsigned CONVF = 544;
constexpr unsigned L_SLAB = 95168;
constexpr unsigned SLABB = 5440;
constexpr unsigned L_REC = 138688;
constexpr unsigned RECB = 960;
constexpr unsigned L_PS = 158848;
constexpr unsigned L_ZERO = 159872;
constexpr unsigned L_FLAG = 160512;
constexpr unsigned L_JUNK = 160768;
constexpr unsigned L_DWRAW = 161792;
constexpr unsigned L_TOTAL = L_DWRAW + 2048;
constexpr unsigned L_STAGE = L_SLAB + 6 * SLABB;
constexpr unsigned STOK = 1744;
constexpr unsigned TABO = L_TAB - L_WO;
constexpr unsigned IDO = L_ID - L_WO;

constexpr unsigned LROW = 38 * 16;
constexpr int LEFT_I = 2, LEFT_J = 3;

constexpr float SQC = 0.54266764f;
constexpr float ISQC = 1.0f / 0.54266764f;
constexpr float GUARD_HI = 1152921504606846976.0f;
constexpr float GUARD_LO = 1.0f / 1152921504606846976.0f;
constexpr unsigned ONES = 0x3f803f80u;

__shared__ __attribute__((aligned(16))) char g_lds[L_TOTAL];

DI f32x4 mfma(u32x4 a, u32x4 b, f32x4 c) {
  return __builtin_amdgcn_mfma_f32_16x16x32_bf16(__builtin_bit_cast(bf16x8, a), __builtin_bit_cast(bf16x8, b), c, 0, 0, 0);
}
DI unsigned cvtpk(float lo, float hi) {
  f32x2 v = {lo, hi};
  bf16x2 b = __builtin_convertvector(v, bf16x2);
  return __builtin_bit_cast(unsigned, b);
}
DI AS3 char* ldsp(unsigned off) { return (AS3 char*)g_lds + off; }
DI u32x4 ldr128(unsigned off) { return *(AS3 u32x4*)ldsp(off); }
DI unsigned ldr16(unsigned off) { return *(AS3 u16a*)ldsp(off); }
DI void ldw128(unsigned off, u32x4 v) { *(AS3 u32x4*)ldsp(off) = v; }
DI void ldw32(unsigned off, unsigned v) { *(AS3 u32a*)ldsp(off) = v; }
DI void ldw16(unsigned off, unsigned v) { *(AS3 u16a*)ldsp(off) = (unsigned short)v; }
DI float ex2(float x) { return __builtin_amdgcn_exp2f(x); }
DI float bf_lo(unsigned d) { return __builtin_bit_cast(float, d << 16); }
DI float bf_hi(unsigned d) { return __builtin_bit_cast(float, d & 0xffff0000u); }
DI unsigned rfl(unsigned v) { return __builtin_amdgcn_readfirstlane(v); }
#define CBAR() asm volatile("" ::: "memory")
#define OPQ(x) asm volatile("" : "+v"(x))
#define SB() __builtin_amdgcn_sched_barrier(0)
constexpr f32x4 Z4 = {0.f, 0.f, 0.f, 0.f};
#define PRIOMODE 5
#define PRIO_M()
#define PRIO_V()

#define PT(i)

DI unsigned tok_code(int t, unsigned n) {
  const unsigned long long lo = t == 0 ? 0x0c05180408010b00ull : t == 1 ? 0x1d16291519121c11ull : 0x343033262a232d22ull;
  const unsigned long long hi = t == 0 ? 0x0a0309020e100d06ull : t == 1 ? 0x1b141a2028211e24ull : 0x2c252b313632352eull;
  return (unsigned)(((n < 8 ? lo : hi) >> (8 * (n & 7))) & 0xff);
}

struct Lane {
  unsigned cA[3], cB[3];
  unsigned cW;
  unsigned l16;
  unsigned l16o;
  unsigned bperm;
  unsigned pw, pr;
  bool q0, q1, q2;
  bool lo2;
};

DI void dma16(unsigned m0v, unsigned voff, __amdgpu_buffer_rsrc_t srd, unsigned soff) {
  asm volatile("s_mov_b32 m0, %0\n\ts_nop 0\n\tbuffer_load_dwordx4 %1, %2, %3 offen lds" ::"s"(m0v), "v"(voff), "s"(srd), "s"(soff) : "m0", "memory");
}
DI void dma4(unsigned m0v, unsigned voff, __amdgpu_buffer_rsrc_t srd, unsigned soff) {
  asm volatile("s_mov_b32 m0, %0\n\ts_nop 0\n\tbuffer_load_dword %1, %2, %3 offen lds" ::"s"(m0v), "v"(voff), "s"(srd), "s"(soff) : "m0", "memory");
}
DI void dma16_last(unsigned m0v, unsigned voff, __amdgpu_buffer_rsrc_t srd, unsigned soff) {
  asm volatile("s_mov_b32 m0, %0\n\ts_mov_b64 exec, 0xfffff\n\tbuffer_load_dwordx4 %1, %2, %3 offen lds\n\ts_mov_b64 exec, -1" ::"s"(m0v), "v"(voff), "s"(srd), "s"(soff)
               : "m0", "memory");
}
DI void dma16_row(unsigned m0v, unsigned voff, __amdgpu_buffer_rsrc_t srd, unsigned soff) {
  asm volatile("s_mov_b32 m0, %0\n\ts_mov_b32 exec_hi, 0xf\n\tbuffer_load_dwordx4 %1, %2, %3 offen lds\n\ts_mov_b32 exec_hi, -1" ::"s"(m0v), "v"(voff), "s"(srd), "s"(soff)
               : "m0", "memory");
}
DI void dma_slab(const u32x4 t0, const u32x4 t1, unsigned m0base, __amdgpu_buffer_rsrc_t srd, unsigned soff, unsigned bm) {
  asm volatile("s_waitcnt lgkmcnt(0)" ::: "memory");
  const unsigned vo[6] = {t0[0], t0[1], t0[2], t0[3], t1[0], t1[1]};
  if (bm == 0) {
#pragma unroll
    for (int i = 0; i < 5; ++i) dma16(m0base + 1024u * i, vo[i], srd, soff);
    dma16_last(m0base + 5120u, vo[5], srd, soff);
  } else {
#pragma unroll
    for (int i = 0; i < 6; ++i) {
      const unsigned v = ((t1[2] >> (4 * i)) & bm) ? 0x80000000u : vo[i];
      if (i < 5) dma16(m0base + 1024u * i, v, srd, soff);
      else dma16_last(m0base + 5120u, v, srd, soff);
    }
  }
}
DI void dma_slab_tab(const Lane& c, unsigned m0base, __amdgpu_buffer_rsrc_t srd, unsigned soff, unsigned bm) {
  dma_slab(ldr128(c.l16o + TABO), ldr128(c.l16o + TABO + 1024), m0base, srd, soff, bm);
}
DI void l2_prefetch(unsigned pf0, unsigned pf1, unsigned m0junk, __amdgpu_buffer_rsrc_t srd, unsigned soff, unsigned bm) {
  if (bm == 0) {
    dma4(m0junk, pf0, srd, soff);
    dma4(m0junk, pf1, srd, soff);
  }
}

DI void first_slab(unsigned l16o, unsigned m0slab, unsigned m0junk, __amdgpu_buffer_rsrc_t rx, unsigned wbase, unsigned bm) {
  const u32x4 t1 = ldr128(l16o + TABO + 1024), t2 = ldr128(l16o + TABO + 2048);
  dma_slab(ldr128(l16o + TABO), t1, m0slab, rx, wbase, bm);
  l2_prefetch(t1[3], t2[0], m0junk, rx, wbase, bm);
}

template <int S>
DI void conv_slab(const Lane& c, f32x4 (&xa)[3][6]) {
  u32x4 A[3], B[3][3];
  auto load = [&](int gi) {
    const int hh = gi / 5, tp = gi % 5, ct = 2 * S + hh;
    A[gi % 3] = ldr128(c.cW + (ct * 5 + tp) * CONVF);
#pragma unroll
    for (int t = 0; t < 3; ++t) B[gi % 3][t] = ldr128((tp == 4 ? c.cB[t] : c.cA[t] + (tp == 3 ? LROW + 64u : 64u * tp)) + 32u * hh);
  };
  load(0);
  load(1);
#pragma unroll
  for (int gi = 0; gi < 10; ++gi) {
    if (gi + 2 < 10) load(gi + 2);
    const int hh = gi / 5, tp = gi % 5, ct = 2 * S + hh;
#pragma unroll
    for (int t = 0; t < 3; ++t) xa[t][ct] = mfma(A[gi % 3], B[gi % 3][t], tp == 0 ? Z4 : xa[t][ct]);
    SB();
  }
}

struct Stat { float rs, sg, mu; };
template <int NT>
DI void layer_norm(const Lane& c, const f32x4 (&xa)[NT][6], u32x4 (&x0p)[NT][3], u32x4 (&hp)[NT][3], Stat (&st)[NT]) {
  const u32x4 ones = {ONES, ONES, ONES, ONES};
  f32x4 sm[NT], sq[NT];
#pragma unroll
  for (int t = 0; t < NT; ++t) {
#pragma unroll
    for (int s = 0; s < 3; ++s) {
      x0p[t][s] = u32x4{cvtpk(xa[t][2 * s][0], xa[t][2 * s][1]), cvtpk(xa[t][2 * s][2], xa[t][2 * s][3]), cvtpk(xa[t][2 * s + 1][0], xa[t][2 * s + 1][1]),
                        cvtpk(xa[t][2 * s + 1][2], xa[t][2 * s + 1][3])};
      sq[t] = mfma(x0p[t][s], x0p[t][s], s == 0 ? Z4 : sq[t]);
      sm[t] = mfma(ones, x0p[t][s], s == 0 ? Z4 : sm[t]);
    }
    SB();
  }
  float d[NT];
#pragma unroll
  for (int t = 0; t < NT; ++t) {
    float v = sq[t][3];
    v = c.q2 ? sq[t][2] : v;
    v = c.q1 ? sq[t][1] : v;
    v = c.q0 ? sq[t][0] : v;
    d[t] = __builtin_bit_cast(float, __builtin_amdgcn_ds_bpermute((int)c.bperm, __builtin_bit_cast(int, v)));
  }
  SB();
#pragma unroll
  for (int t = 0; t < NT; ++t) {
    const float mu = sm[t][0] * (1.0f / 96.0f);
    float var = __builtin_fmaf(d[t], 1.0f / 96.0f, -mu * mu);
    var = __builtin_fmaxf(var, 0.f) + 1e-5f;
    const float rs = __builtin_amdgcn_rsqf(var);
    const float a = rs * SQC;
    const float b = -mu * a;
    st[t].rs = rs;
    st[t].sg = var * rs * ISQC;
    st[t].mu = mu;
#pragma unroll
    for (int s = 0; s < 3; ++s) {
      float h[8];
#pragma unroll
      for (int e = 0; e < 8; ++e) h[e] = __builtin_fmaf(xa[t][2 * s + (e >> 2)][e & 3], a, b);
      hp[t][s] = u32x4{cvtpk(h[0], h[1]), cvtpk(h[2], h[3]), cvtpk(h[4], h[5]), cvtpk(h[6], h[7])};
    }
  }
}

struct Rec {
  unsigned k3;
  unsigned q3[4];
  unsigned v3;
};

DI float grp_max(float m) {
  m = __builtin_fmaxf(m, __shfl_xor(m, 16));
  m = __builtin_fmaxf(m, __shfl_xor(m, 32));
  return m;
}
template <int CTRL>
DI float quad_bcast(float v) { return __builtin_bit_cast(float, __builtin_amdgcn_mov_dpp(__builtin_bit_cast(int, v), CTRL, 0xf, 0xf, true)); }

template <int H>
DI void norm_tile(const f32x4 O0, const f32x4 O1, float rs, u32x4 (&Op)[3], float& l_out) {
  constexpr int E = (H & 1) ? 2 : 0;
  const float l = O1[2 - E];
  l_out = l;
  const float f = __builtin_amdgcn_rcpf(l) * rs;
  Op[(3 * H) >> 2][(3 * H) & 3] = cvtpk(O0[0] * f, O0[1] * f);
  Op[(3 * H + 1) >> 2][(3 * H + 1) & 3] = cvtpk(O0[2] * f, O0[3] * f);
  Op[(3 * H + 2) >> 2][(3 * H + 2) & 3] = cvtpk(O1[E] * f, O1[E + 1] * f);
}
template <int H>
DI unsigned norm_left(const f32x4 L0, const f32x4 L1, float& l_out) {
  const float l = quad_bcast<(H & 1) ? 0x00 : 0xaa>(L1[0]);
  l_out = l;
  const float f = __builtin_amdgcn_rcpf(l) * ISQC;
  return cvtpk(L0[0] * f, L1[0] * f);
}
DI u32x4 pack8(const f32x4 a, const f32x4 b) { return u32x4{cvtpk(a[0], a[1]), cvtpk(a[2], a[3]), cvtpk(b[0], b[1]), cvtpk(b[2], b[3])}; }
DI f32x4 ex2x4(const f32x4 a) { return f32x4{ex2(a[0]), ex2(a[1]), ex2(a[2]), ex2(a[3])}; }

struct Heads {
  u32x4 F[3];
  unsigned Xp[6];
  f32x4 pO0, pO1;
  f32x4 pL0, pL1;
  float lmin, lmax;
};

template <int H>
DI void head_tail(Heads& hs, const Stat (&st)[3], u32x4 (&Op)[3][3], unsigned (&oL)[4], int part) {
  if (part == 0) {
    float l;
    norm_tile<H>(hs.pO0, hs.pO1, st[2].rs, Op[2], l);
    hs.lmin = __builtin_fminf(hs.lmin, l);
    hs.lmax = __builtin_fmaxf(hs.lmax, l);
  } else {
    float l;
    oL[H] = norm_left<H>(hs.pL0, hs.pL1, l);
    hs.lmin = __builtin_fminf(hs.lmin, l);
    hs.lmax = __builtin_fmaxf(hs.lmax, l);
  }
}

template <int H>
DI void head_fast(const Lane& c, const Rec& rc, const u32x4 (&hp)[3][3], const Stat (&st)[3], Heads& hs, u32x4 (&Op)[3][3], unsigned (&oL)[4]) {
  constexpr unsigned WB = L_WQKV + H * 12288;
  constexpr bool EVEN = (H & 1) == 0;
  u32x4(&F)[3] = hs.F;
  f32x4 T[3], U[3], W[3], Y[3];
  u32x4 Q[3], K[3], M[4], K3;
  PRIO_M();
#pragma unroll
  for (int s = 0; s < 3; ++s) {
#pragma unroll
    for (int t = 0; t < 3; ++t) T[t] = mfma(F[s], hp[t][s], s == 0 ? Z4 : T[t]);
    F[s] = ldr128(c.l16 + WB + 3072 + s * 1024);
    if (H > 0 && s < 2) head_tail<(H > 0 ? H - 1 : 0)>(hs, st, Op, oL, s);
    SB();
  }
#pragma unroll
  for (int s = 0; s < 3; ++s) {
#pragma unroll
    for (int t = 0; t < 3; ++t) U[t] = mfma(F[s], hp[t][s], s == 0 ? Z4 : U[t]);
    F[s] = ldr128(c.l16 + WB + 2 * 3072 + s * 1024);
    K[s][0] = cvtpk(T[s][0], T[s][1]);
    K[s][1] = cvtpk(T[s][2], T[s][3]);
    K[s][3] = 0u;
    SB();
  }
#pragma unroll
  for (int s = 0; s < 3; ++s) {
#pragma unroll
    for (int t = 0; t < 3; ++t) W[t] = mfma(F[s], hp[t][s], s == 0 ? Z4 : W[t]);
    F[s] = ldr128(c.l16 + WB + 3 * 3072 + s * 1024);
    M[s] = ldr128(rc.q3[s] + H * 192);
    if (s == 2) { M[3] = ldr128(rc.q3[3] + H * 192); K3 = ldr128(rc.k3 + H * 192); }
    Q[s][2] = cvtpk(U[s][0], U[s][1]);
    K[s][2] = cvtpk(U[s][2], U[s][3]);
    Q[s][3] = 0u;
    SB();
  }
  f32x4 DL = Z4;
  unsigned v3a, v3b;
#pragma unroll
  for (int s = 0; s < 3; ++s) {
#pragma unroll
    for (int t = 0; t < 3; ++t) Y[t] = mfma(hp[t][s], F[s], s == 0 ? Z4 : Y[t]);
    DL = mfma(M[s], K[s], DL);
    F[s] = EVEN ? ldr128(c.l16 + L_X2 + (H >> 1) * 3072 + s * 1024) : ldr128(c.l16 + (H < 3 ? WB + 12288 : 0) + s * 1024);
    Q[s][0] = cvtpk(W[s][0], W[s][1]);
    Q[s][1] = cvtpk(W[s][2], W[s][3]);
    if (s == 2) { v3a = ldr16(rc.v3 + H * 192); v3b = ldr16(rc.v3 + H * 192 + 32); }
    SB();
  }
  PRIO_V();
  f32x4 S0[4], S1[4], S2[4];
#pragma unroll
  for (int i = 0; i < 3; ++i) S0[i] = mfma(K[i], Q[0], Z4);
  S0[3] = mfma(K3, Q[0], Z4);
  DL = mfma(M[3], K3, DL);
  u32x4 Va0, Va1, Vb0, Vb1;
  Va0 = pack8(Y[0], Y[1]);
  SB();
#pragma unroll
  for (int i = 0; i < 3; ++i) S1[i] = mfma(K[i], Q[1], Z4);
  S1[3] = mfma(K3, Q[1], Z4);
  Va1 = u32x4{cvtpk(Y[2][0], Y[2][1]), cvtpk(Y[2][2], Y[2][3]), v3a, 0u};
  {
    const float p = ex2(DL[0]);
    ldw16(c.pw, cvtpk(p, p));
  }
  SB();
  u32x4 PL0, PL1;
  u32x4 P0, P1;
  if (EVEN) {
    f32x4 Z[3];
#pragma unroll
    for (int s = 0; s < 3; ++s) {
#pragma unroll
      for (int t = 0; t < 3; ++t) Z[t] = mfma(hp[t][s], F[s], s == 0 ? Z4 : Z[t]);
      F[s] = ldr128(c.l16 + WB + 12288 + s * 1024);
      S0[s] = ex2x4(S0[s]);
      if (s == 0) { PL0 = ldr128(c.pr); PL1 = ldr128(c.pr + 64); }
      if (s == 1) { P0 = pack8(S0[0], S0[1]); }
      SB();
    }
    const float p3 = ex2(S0[3][0]);
    P1 = u32x4{cvtpk(S0[2][0], S0[2][1]), cvtpk(S0[2][2], S0[2][3]), cvtpk(p3, p3), 0u};
#pragma unroll
    for (int i = 0; i < 3; ++i) S2[i] = mfma(K[i], Q[2], Z4);
    S2[3] = mfma(K3, Q[2], Z4);
    S1[0] = ex2x4(S1[0]);
    SB();
#pragma unroll
    for (int t = 0; t < 3; ++t) {
      hs.Xp[2 * t] = cvtpk(Z[t][0], Z[t][1]);
      hs.Xp[2 * t + 1] = cvtpk(Z[t][2], Z[t][3]);
    }
  } else {
    PL0 = ldr128(c.pr);
    PL1 = ldr128(c.pr + 64);
#pragma unroll
    for (int i = 0; i < 3; ++i) S2[i] = mfma(K[i], Q[2], Z4);
    S2[3] = mfma(K3, Q[2], Z4);
#pragma unroll
    for (int s = 0; s < 3; ++s) S0[s] = ex2x4(S0[s]);
    const float p3 = ex2(S0[3][0]);
    P0 = pack8(S0[0], S0[1]);
    P1 = u32x4{cvtpk(S0[2][0], S0[2][1]), cvtpk(S0[2][2], S0[2][3]), cvtpk(p3, p3), 0u};
    SB();
    S1[0] = ex2x4(S1[0]);
  }
  {
    const bool keep = EVEN ? c.lo2 : !c.lo2;
    Vb0 = u32x4{keep ? hs.Xp[0] : ONES, keep ? hs.Xp[1] : ONES, keep ? hs.Xp[2] : ONES, keep ? hs.Xp[3] : ONES};
    Vb1 = u32x4{keep ? hs.Xp[4] : ONES, keep ? hs.Xp[5] : ONES, v3b, 0u};
  }
  SB();
  f32x4 O0, O1;
  O0 = mfma(Va0, P0, Z4);
  O1 = mfma(Vb0, P0, Z4);
  O0 = mfma(Va1, P1, O0);
  O1 = mfma(Vb1, P1, O1);
  S1[1] = ex2x4(S1[1]);
  S1[2] = ex2x4(S1[2]);
  {
    const float p3 = ex2(S1[3][0]);
    P0 = pack8(S1[0], S1[1]);
    P1 = u32x4{cvtpk(S1[2][0], S1[2][1]), cvtpk(S1[2][2], S1[2][3]), cvtpk(p3, p3), 0u};
  }
  SB();
  f32x4 R0, R1;
  R0 = mfma(Va0, P0, Z4);
  R1 = mfma(Vb0, P0, Z4);
  R0 = mfma(Va1, P1, R0);
  R1 = mfma(Vb1, P1, R1);
  float la, lb;
  norm_tile<H>(O0, O1, st[0].rs, Op[0], la);
  S2[0] = ex2x4(S2[0]);
  SB();
  hs.pL0 = mfma(PL0, Va0, Z4);
  hs.pL1 = mfma(PL0, Vb0, Z4);
  hs.pL0 = mfma(PL1, Va1, hs.pL0);
  hs.pL1 = mfma(PL1, Vb1, hs.pL1);
  S2[1] = ex2x4(S2[1]);
  S2[2] = ex2x4(S2[2]);
  {
    const float p3 = ex2(S2[3][0]);
    P0 = pack8(S2[0], S2[1]);
    P1 = u32x4{cvtpk(S2[2][0], S2[2][1]), cvtpk(S2[2][2], S2[2][3]), cvtpk(p3, p3), 0u};
  }
  SB();
  hs.pO0 = mfma(Va0, P0, Z4);
  hs.pO1 = mfma(Vb0, P0, Z4);
  hs.pO0 = mfma(Va1, P1, hs.pO0);
  hs.pO1 = mfma(Vb1, P1, hs.pO1);
  norm_tile<H>(R0, R1, st[1].rs, Op[1], lb);
  hs.lmin = __builtin_fminf(__builtin_fminf(la, lb), hs.lmin);
  hs.lmax = __builtin_fmaxf(__builtin_fmaxf(la, lb), hs.lmax);
  SB();
}

template <int H>
DI void head_safe(const Lane& c, const Rec& rc, unsigned recq, const u32x4 (&hp)[3][3], const Stat (&st)[3], u32x4 (&Op)[3][3], unsigned (&oL)[4]) {
  constexpr unsigned WB = L_WQKV + H * 12288;
  f32x4 T[3], U[3], W[3], Y[3], Z[3];
#pragma unroll
  for (int s = 0; s < 3; ++s) {
    const u32x4 fk = ldr128(c.l16 + WB + s * 1024), fr = ldr128(c.l16 + WB + 3072 + s * 1024), fq = ldr128(c.l16 + WB + 6144 + s * 1024);
    const u32x4 fv = ldr128(c.l16 + WB + 9216 + s * 1024), fx = ldr128(c.l16 + L_X2 + (H >> 1) * 3072 + s * 1024);
#pragma unroll
    for (int t = 0; t < 3; ++t) {
      T[t] = mfma(fk, hp[t][s], s == 0 ? Z4 : T[t]);
      U[t] = mfma(fr, hp[t][s], s == 0 ? Z4 : U[t]);
      W[t] = mfma(fq, hp[t][s], s == 0 ? Z4 : W[t]);
      Y[t] = mfma(hp[t][s], fv, s == 0 ? Z4 : Y[t]);
      Z[t] = mfma(hp[t][s], fx, s == 0 ? Z4 : Z[t]);
    }
    SB();
  }
  u32x4 Q[3], K[4];
#pragma unroll
  for (int t = 0; t < 3; ++t) {
    Q[t] = u32x4{cvtpk(W[t][0], W[t][1]), cvtpk(W[t][2], W[t][3]), cvtpk(U[t][0], U[t][1]), 0u};
    K[t] = u32x4{cvtpk(T[t][0], T[t][1]), cvtpk(T[t][2], T[t][3]), cvtpk(U[t][2], U[t][3]), 0u};
  }
  K[3] = ldr128(rc.k3 + H * 192);
  const u32x4 Q3 = ldr128(recq + H * 192);
  const bool keep = (H & 1) == 0 ? c.lo2 : !c.lo2;
  const u32x4 Va0 = pack8(Y[0], Y[1]);
  const u32x4 Va1 = u32x4{cvtpk(Y[2][0], Y[2][1]), cvtpk(Y[2][2], Y[2][3]), ldr16(rc.v3 + H * 192), 0u};
  const u32x4 X0 = pack8(Z[0], Z[1]);
  const u32x4 Vb0 = u32x4{keep ? X0[0] : ONES, keep ? X0[1] : ONES, keep ? X0[2] : ONES, keep ? X0[3] : ONES};
  const u32x4 Vb1 = u32x4{keep ? cvtpk(Z[2][0], Z[2][1]) : ONES, keep ? cvtpk(Z[2][2], Z[2][3]) : ONES, ldr16(rc.v3 + H * 192 + 32), 0u};
  SB();
#pragma unroll
  for (int jq = 0; jq < 3; ++jq) {
    f32x4 S[4];
#pragma unroll
    for (int i = 0; i < 4; ++i) S[i] = mfma(K[i], Q[jq], Z4);
    float m = S[3][0];
#pragma unroll
    for (int i = 0; i < 3; ++i)
#pragma unroll
      for (int r = 0; r < 4; ++r) m = __builtin_fmaxf(m, S[i][r]);
    m = grp_max(m);
#pragma unroll
    for (int i = 0; i < 3; ++i)
#pragma unroll
      for (int r = 0; r < 4; ++r) S[i][r] = ex2(S[i][r] - m);
    const float p3 = ex2(S[3][0] - m);
    const u32x4 P0 = pack8(S[0], S[1]);
    const u32x4 P1 = u32x4{cvtpk(S[2][0], S[2][1]), cvtpk(S[2][2], S[2][3]), cvtpk(p3, p3), 0u};
    f32x4 O0 = mfma(Va0, P0, Z4), O1 = mfma(Vb0, P0, Z4);
    O0 = mfma(Va1, P1, O0);
    O1 = mfma(Vb1, P1, O1);
    float l;
    norm_tile<H>(O0, O1, st[jq].rs, Op[jq], l);
    OPQ(Op[jq][(3 * H) >> 2]);
    OPQ(Op[jq][(3 * H + 2) >> 2]);
    SB();
  }
  {
    f32x4 D[4];
#pragma unroll
    for (int i = 0; i < 4; ++i) D[i] = mfma(Q3, K[i], Z4);
    const unsigned g = (c.l16 >> 8) & 3;
    float s = D[3][0];
    s = g == 2 ? D[2][0] : s;
    s = g == 1 ? D[1][0] : s;
    s = g == 0 ? D[0][0] : s;
    float m = s;
#pragma unroll
    for (int o = 1; o < 64; o <<= 1) m = __builtin_fmaxf(m, __shfl_xor(m, o));
    const float p = ex2(s - m);
    ldw16(c.pw, cvtpk(p, p));
    const u32x4 PL0 = ldr128(c.pr), PL1 = ldr128(c.pr + 64);
    f32x4 L0 = mfma(PL0, Va0, Z4), L1 = mfma(PL0, Vb0, Z4);
    L0 = mfma(PL1, Va1, L0);
    L1 = mfma(PL1, Vb1, L1);
    float l;
    oL[H] = norm_left<H>(L0, L1, l);
    OPQ(oL[H]);
    SB();
  }
}

template <int CT0>
DI void proj_part(const Lane& c, const u32x4 (&hp)[3][3], const Stat (&st)[3], const u32x4 (&Op)[3][3], __amdgpu_buffer_rsrc_t rout, unsigned wbase) {
  u32x4 A[3], I[2];
  I[0] = ldr128(c.l16o + IDO);
  I[1] = ldr128(c.l16o + IDO + 1024);
#pragma unroll
  for (int s = 0; s < 3; ++s) A[s] = ldr128(c.l16o + (CT0 * 3 + s) * 1024);
  const u32x4 t2 = ldr128(c.l16o + TABO + 2048);
  f32x4 acc[2][3];
#pragma unroll
  for (int k = 0; k < 4; ++k) {
    const int ct = CT0 + k;
    if (k < 3) {
#pragma unroll
      for (int t = 0; t < 3; ++t) acc[k & 1][t] = mfma(I[ct & 1], hp[t][ct >> 1], Z4);
#pragma unroll
      for (int s = 0; s < 3; ++s) {
#pragma unroll
        for (int t = 0; t < 3; ++t) acc[k & 1][t] = mfma(A[s], Op[t][s], acc[k & 1][t]);
        if (k < 2) A[s] = ldr128(c.l16o + ((ct + 1) * 3 + s) * 1024);
      }
    }
    if (k > 0) {
#pragma unroll
      for (int t = 0; t < 3; ++t) {
        const f32x4 a = acc[(k - 1) & 1][t];
        const float sg = st[t].sg, mu = st[t].mu;
        const u32x2 o = {cvtpk(__builtin_fmaf(a[0], sg, mu), __builtin_fmaf(a[1], sg, mu)), cvtpk(__builtin_fmaf(a[2], sg, mu), __builtin_fmaf(a[3], sg, mu))};
        __builtin_amdgcn_raw_buffer_store_b64(o, rout, t2[1 + t] + (ct - 1) * 32, wbase, 0);
      }
    }
    SB();
  }
}

DI unsigned win_index(unsigned base_x, unsigned j, unsigned rho, unsigned w) { return base_x + (rho < 2 ? rho * 256 + j * 8 + w : 512 + 32 * w + j); }

DI void spin_until(unsigned off, unsigned target) {
  while (rfl(*(volatile AS3 unsigned*)ldsp(off)) < target) __builtin_amdgcn_s_sleep(2);
  CBAR();
}

DI u32x4 tr_pair(unsigned a0, unsigned a1) {
  const s16x4 lo = __builtin_amdgcn_ds_read_tr16_b64_v4i16((AS3 s16x4*)ldsp(a0));
  const s16x4 hi = __builtin_amdgcn_ds_read_tr16_b64_v4i16((AS3 s16x4*)ldsp(a1));
  const u32x2 l2 = __builtin_bit_cast(u32x2, lo), h2 = __builtin_bit_cast(u32x2, hi);
  return u32x4{l2[0], l2[1], h2[0], h2[1]};
}
DI void lds_barrier() {
  asm volatile("s_waitcnt lgkmcnt(0)" ::: "memory");
  __builtin_amdgcn_s_barrier();
  CBAR();
}
DI void tr_plain(u32x4& f0, u32x4& f1, u32x4& f2, unsigned lane, unsigned colb) {
  const unsigned la = (lane >> 2) * 576 + (lane & 3) * 8 + colb * 2;
  f0 = tr_pair(la, la + 16 * 576);
  f1 = tr_pair(la + 32 * 576, la + 48 * 576);
  f2 = tr_pair(la + 64 * 576, la + 80 * 576);
}
DI unsigned wo_row(unsigned sq, unsigned g) {
  const unsigned hh = (sq * 43) >> 8, ix = sq - 6 * hh;
  return (hh * 24 + (ix < 4 ? 4 * g + ix : 12 + 2 * g + ix)) * 192;
}
DI void tr_wo(u32x4& f0, u32x4& f1, u32x4& f2, unsigned lane, unsigned ct) {
  const unsigned r = (lane >> 2) & 3, g = lane >> 4, b = L_WO + ct * 32 + (lane & 3) * 8;
  f0 = tr_pair(b + wo_row(r, g), b + wo_row(4 + r, g));
  f1 = tr_pair(b + wo_row(8 + r, g), b + wo_row(12 + r, g));
  f2 = tr_pair(b + wo_row(16 + r, g), b + wo_row(20 + r, g));
}
DI u32x4 gather1(unsigned ga) {
  u32x4 f;
#pragma unroll
  for (int d = 0; d < 4; ++d) {
    const unsigned k = 16 * (d >> 1) + 2 * (d & 1);
    f[d] = ldr16(ga + k * 576) | (ldr16(ga + (k + 1) * 576) << 16);
  }
  return f;
}
DI void gather(u32x4& f0, u32x4& f1, u32x4& f2, unsigned g, unsigned col) {
  const unsigned ga = 4 * g * 576 + col * 2;
  f0 = gather1(ga);
  f1 = gather1(ga + 32 * 576);
  f2 = gather1(ga + 64 * 576);
}

__global__ __launch_bounds__(512, 2) __attribute__((target("no-packed-fp32-ops")))
void wattn_kernel(const uint16_t* __restrict__ x, const uint16_t* __restrict__ dw, const uint16_t* __restrict__ wqkv, const uint16_t* __restrict__ wo,
                  uint16_t* __restrict__ out
                  ) {
  const unsigned tid = threadIdx.x;
  const unsigned wv = rfl(tid >> 6);
  Lane c;
  const unsigned lane = tid & 63, n = lane & 15, g = lane >> 4;
  const unsigned xcd = blockIdx.x & 7, jw = blockIdx.x >> 3;
  const unsigned base_x = xcd * 666 + (xcd ? 1 : 0), cnt_x = 666 + (xcd == 0 ? 1 : 0);
  const unsigned n2 = 4 + ((640 + jw < cnt_x) ? 1 : 0);
  const unsigned nW = 16 + n2;
  const unsigned nwin = wv < n2 ? 3 : 2;
  const unsigned lds0 = (unsigned)(size_t)(AS3 char*)g_lds;
  const unsigned slab = L_SLAB + wv * SLABB;

  const __amdgpu_buffer_rsrc_t rx = __builtin_amdgcn_make_buffer_rsrc((void*)((const char*)x - XSHIFT), (short)0, (int)(XBYTES + XSHIFT), 0x00020000);
  const __amdgpu_buffer_rsrc_t rw = __builtin_amdgcn_make_buffer_rsrc((void*)wqkv, (short)0, 96 * 288 * 2, 0x00020000);
  const __amdgpu_buffer_rsrc_t ro = __builtin_amdgcn_make_buffer_rsrc((void*)wo, (short)0, 96 * 96 * 2, 0x00020000);
  const __amdgpu_buffer_rsrc_t rout = __builtin_amdgcn_make_buffer_rsrc((void*)out, (short)0, (int)XBYTES, 0x00020000);

  {
    uint64_t pc;
    asm volatile("s_getpc_b64 %0" : "=s"(pc));
    const __amdgpu_buffer_rsrc_t rc = __builtin_amdgcn_make_buffer_rsrc((void*)pc, (short)0, PFBYTES, 0x00020000);
    dma4(rfl(lds0 + L_JUNK), (wv * 64 + lane) * 128, rc, 0);
  }
  PT(16);
  const __amdgpu_buffer_rsrc_t rd = __builtin_amdgcn_make_buffer_rsrc((void*)dw, (short)0, 9 * 96 * 2, 0x00020000);
  {
    const unsigned l16 = lane * 16;
#pragma unroll
    for (unsigned i = 0; i < 9; ++i) {
      const unsigned pc = wv + 8 * i;
      if (i < 6 || pc < 54) dma16(lds0 + pc * 1024, l16, rw, pc * 1024);
      else dma16(lds0 + pc * 1024, l16, ro, (pc - 54) * 1024);
    }
    if (wv == 5) {
      dma16(lds0 + L_DWRAW, l16, rd, 0);
      dma16(lds0 + L_DWRAW + 1024, l16, rd, 1024);
    }
  }
  PT(17);
  const unsigned m0slab = rfl(lds0 + slab), m0junk = rfl(lds0 + L_JUNK);
  unsigned widx = win_index(base_x, jw, 0, wv);
  unsigned wr = widx / NW1, wc = widx % NW1;
  unsigned wbase = (7 * wr * IMG + 7 * wc) * PIXB;
  unsigned bm = (wr == 0 ? 1u : 0u) | (wr == NW1 - 1 ? 2u : 0u) | (wc == 0 ? 4u : 0u) | (wc == NW1 - 1 ? 8u : 0u);
  unsigned pf0 = 0, pf1 = 0;
  if (wv < 4) {
    unsigned vo[6], vflag = 0, pf[2];
#pragma unroll
    for (int i = 0; i < 6; ++i) {
      const unsigned sl = 64 * i + lane, r = sl / 38, t = sl % 38, cc = t >> 2, q = t & 3;
      const bool ok = t < 36 && sl < 340;
      vo[i] = ok ? (r * IMG + cc) * PIXB + q * 16 : 0x80000000u;
      vflag |= ((r == 0 ? 1u : 0u) | (r == 8 ? 2u : 0u) | (cc == 0 ? 4u : 0u) | (cc == 8 ? 8u : 0u)) << (4 * i);
    }
#pragma unroll
    for (int i = 0; i < 2; ++i) {
      const unsigned p = 64 * i + lane, r = p / 9, cc = p % 9;
      pf[i] = p < 81 ? (r * IMG + cc) * PIXB + 128 : 0x80000000u;
    }
    pf0 = pf[0]; pf1 = pf[1];
    PT(18);
    dma_slab(u32x4{vo[0], vo[1], vo[2], vo[3]}, u32x4{vo[4], vo[5], vflag, pf[0]}, m0slab, rx, wbase, bm);
    if (wv == 3) {
      ldw128(L_TAB + lane * 16, u32x4{vo[0], vo[1], vo[2], vo[3]});
      ldw128(L_TAB + 1024 + lane * 16, u32x4{vo[4], vo[5], vflag, pf[0]});
      ldw32(L_TAB + 2048 + lane * 16, pf[1]);
    }
  }
  PT(19);
  const bool lw = wv >= 6;
  const unsigned lt = wv == 7 ? 0 : 1;
  const unsigned lwl = 16 * lt + n;
  const bool lvalid = lwl < nW;
  unsigned lpix = 0;
  if (lw) {
    const unsigned wl = lvalid ? lwl : 0;
    const unsigned wi = win_index(base_x, jw, wl >> 3, wl & 7);
    const unsigned r = wi / NW1, cc = wi % NW1;
    lpix = ((7 * r + LEFT_I) * IMG + 7 * cc + LEFT_J) * PIXB;
    if (wv == 7) {
      const unsigned l16 = lane * 16;
#pragma unroll
      for (int t = 0; t < 16; ++t) {
        const unsigned pb = __builtin_amdgcn_readlane(lpix, t);
#pragma unroll
        for (int r3 = 0; r3 < 3; ++r3) dma16_row(lds0 + L_STAGE + t * STOK + r3 * 576, l16, rx, pb + r3 * ROWB);
      }
    }
  }

  PT(21);
  {
    unsigned ost[3];
#pragma unroll
    for (int t = 0; t < 3; ++t) {
      const unsigned code = tok_code(t, n), i = code >> 3, j = code & 7;
      c.cA[t] = slab + LROW * (i + (g >> 1)) + 64 * j + 16 * (g & 1);
      c.cB[t] = slab + LROW * (i + 2) + 64 * (j + 2 * (g >> 1)) + 16 * (g & 1);
      ost[t] = (i * IMG + j) * PIXB + g * 8;
    }
    if (wv == 4) {
      const unsigned one = (n >> 2) == g ? 0x3f80u << (16 * (n & 1)) : 0u;
      const unsigned d0 = (n & 2) ? 0u : one, d1 = (n & 2) ? one : 0u;
      ldw128(L_ID + lane * 16, u32x4{d0, d1, 0u, 0u});
      ldw128(L_ID + 1024 + lane * 16, u32x4{0u, 0u, d0, d1});
    }
    if (wv == 5) {
#pragma unroll
      for (int t = 0; t < 3; ++t) ldw32(L_TAB + 2048 + lane * 16 + 4 + 4 * t, ost[t]);
    }
  }
  {
    const bool act = (g & 1) == (n >> 3);
    const unsigned G = 2 * (g >> 1) + ((n >> 2) & 1), idx = 4 * (g & 1) + (n & 3);
    const unsigned k = (G == 0 ? 0 : G == 1 ? 9 : G == 2 ? 17 : 26) + idx;
    c.cW = L_CONV + 16 * (act ? k : (g < 2 ? 8 : 25));
  }
  c.l16 = lane * 16; c.l16o = L_WO + lane * 16;
  c.bperm = 4 * ((n >> 2) * 16 + n);
  c.pw = lane < 49 ? L_PS + wv * 128 + (g >> 1) * 64 + (n >> 2) * 16 + (g & 1) * 8 + (n & 3) * 2 : L_JUNK + lane * 2;
  c.pr = L_PS + wv * 128 + g * 16;
  c.q0 = (n & 3) == 0; c.q1 = (n & 3) == 1; c.q2 = (n & 3) == 2; c.lo2 = (n & 3) < 2;
  if (tid < (L_FLAG + 32 - L_PS) / 4) ldw32(L_PS + tid * 4, 0u);
  unsigned cva[2], cve[2];
#pragma unroll
  for (int i = 0; i < 2; ++i) {
    const unsigned sl0 = tid + 512 * i, sl = sl0 < 1020 ? sl0 : 8;
    const unsigned f = sl / 34, k = sl % 34, ct = f / 5, tp = f % 5;
    const unsigned G = (k > 8 ? 1u : 0u) + (k > 16 ? 1u : 0u) + (k > 25 ? 1u : 0u);
    const unsigned idx = (k - ((0x1a110900u >> (8 * G)) & 0xff)) & 7;
    const unsigned hi = G >> 1, m = 8 * (idx >> 2) + 4 * (G & 1) + (idx & 3);
    const unsigned tap = (unsigned)(0x8674524130ull >> (4 * (2 * tp + hi))) & 15;
    cva[i] = L_DWRAW + (tap * 96 + 16 * ct + m) * 2;
    cve[i] = (k == 8 || k == 25) ? 8u : ((m & 7) | ((tp == 3 && hi == 0) ? 16u : 0u));
  }
  PT(0);
  if (wv == 7) asm volatile("s_waitcnt vmcnt(48) lgkmcnt(0)" ::: "memory");
  else if (wv < 4) asm volatile("s_waitcnt vmcnt(6) lgkmcnt(0)" ::: "memory");
  else asm volatile("s_waitcnt vmcnt(0) lgkmcnt(0)" ::: "memory");
  __builtin_amdgcn_s_barrier();
  CBAR();
  PT(1);
  if (wv < 4) l2_prefetch(pf0, pf1, m0junk, rx, wbase, bm);
  {
    const unsigned hd = wv & 3;
    u32x4 fa0, fa1, fa2, fb0, fb1, fb2, fc0, fc1, fc2;
    const unsigned rcol = 16 + 2 * (n >> 2) + (n & 1);
    unsigned da, db, dc;
    tr_plain(fa0, fa1, fa2, lane, (wv < 4 ? 96 : 192) + 24 * hd);
    da = hd * 12288 + (wv < 4 ? 0 : 9216);
    if (wv < 4) {
      gather(fb0, fb1, fb2, g, (c.lo2 ? 0 : 96) + 24 * hd + rcol);
      tr_plain(fc0, fc1, fc2, lane, 24 * hd);
      db = hd * 12288 + 3072;
      dc = hd * 12288 + 6144;
    } else {
      if ((wv & 1) == 0) {
        gather(fb0, fb1, fb2, g, 192 + (hd + (c.lo2 ? 0 : 1)) * 24 + rcol);
        db = L_X2 + (hd >> 1) * 3072;
      } else {
        tr_wo(fb0, fb1, fb2, lane, wv == 5 ? 1 : 4);
        db = L_WO + (wv == 5 ? 1 : 4) * 3072;
      }
      const unsigned ctc = wv == 4 ? 0 : wv == 5 ? 2 : wv == 6 ? 3 : 5;
      tr_wo(fc0, fc1, fc2, lane, ctc);
      dc = L_WO + ctc * 3072;
    }
#pragma unroll
    for (int i = 0; i < 2; ++i) {
      const unsigned e = cve[i];
      const unsigned w = (e & 16) ? 0x3f80u : ldr16(cva[i]);
      const unsigned dd = w << (16 * (e & 1)), h = (e >> 1) & 7;
      const u32x4 v = {h == 0 ? dd : 0u, h == 1 ? dd : 0u, h == 2 ? dd : 0u, h == 3 ? dd : 0u};
      if (i == 0 || tid < 508) ldw128(L_CONV + (tid + 512 * i) * 16, v);
    }
    lds_barrier();
    da += lane * 16; db += lane * 16; dc += lane * 16;
    ldw128(da, fa0); ldw128(da + 1024, fa1); ldw128(da + 2048, fa2);
    ldw128(db, fb0); ldw128(db + 1024, fb1); ldw128(db + 2048, fb2);
    ldw128(dc, fc0); ldw128(dc + 1024, fc1); ldw128(dc + 2048, fc2);
    lds_barrier();
  }
  PT(2);

  if (wv == 4 || wv == 5) first_slab(c.l16o, m0slab, m0junk, rx, wbase, bm);
  if (lw) {
    __builtin_amdgcn_s_setprio(2);
    f32x4 xa[1][6];
    if (wv == 7) {
      asm volatile("s_waitcnt vmcnt(0)" ::: "memory");
      const unsigned hi = g >> 1, sb = L_STAGE + n * STOK + (g & 1) * 16;
      const unsigned a0 = sb + hi * 576, a3 = sb + (hi + 1) * 576 + 192, a4 = sb + 2 * 576 + 2 * hi * 192;
#pragma unroll
      for (int ct = 0; ct < 6; ++ct) {
        u32x4 A[5], B[5];
#pragma unroll
        for (int tp = 0; tp < 5; ++tp) {
          A[tp] = ldr128(c.cW + (ct * 5 + tp) * CONVF);
          B[tp] = ldr128((tp < 3 ? a0 + tp * 192 : tp == 3 ? a3 : a4) + ct * 32);
        }
#pragma unroll
        for (int tp = 0; tp < 5; ++tp) xa[0][ct] = mfma(A[tp], B[tp], tp == 0 ? Z4 : xa[0][ct]);
      }
#pragma unroll
      for (int ct = 0; ct < 6; ++ct) OPQ(xa[0][ct]);
      asm volatile("s_waitcnt lgkmcnt(0)" ::: "memory");
      if (lane == 0) *(volatile AS3 unsigned*)ldsp(L_FLAG + 16) = 1u;
      CBAR();
      first_slab(c.l16o, m0slab, m0junk, rx, wbase, bm);
    } else {
      const unsigned hi = g >> 1;
      const char* px = (const char*)x + lpix + (g & 1) * 16;
      const char* p0 = px + ((int)hi - 1) * (int)ROWB - (int)PIXB;
      const char* p3 = px + hi * ROWB;
      const char* p4 = px + ROWB + (2 * (int)hi - 1) * (int)PIXB;
      u32x4 lb[30];
#pragma unroll
      for (int f = 0; f < 30; ++f) {
        const int tp = f % 5;
        const char* p = tp < 3 ? p0 + tp * PIXB : tp == 3 ? p3 : p4;
        lb[f] = *(const u32x4*)(p + (f / 5) * 32);
      }
#pragma unroll
      for (int tp = 0; tp < 5; ++tp)
#pragma unroll
        for (int ct = 0; ct < 6; ++ct) xa[0][ct] = mfma(ldr128(c.cW + (ct * 5 + tp) * CONVF), lb[ct * 5 + tp], tp == 0 ? Z4 : xa[0][ct]);
#pragma unroll
      for (int ct = 0; ct < 6; ++ct) OPQ(xa[0][ct]);
      spin_until(L_FLAG + 16, 1u);
      first_slab(c.l16o, m0slab, m0junk, rx, wbase, bm);
    }
    u32x4 x0p[1][3], hp[1][3];
    Stat st1[1];
    layer_norm<1>(c, xa, x0p, hp, st1);
    const unsigned rec = lvalid ? L_REC + lwl * RECB : L_JUNK;
#pragma unroll
    for (int s = 0; s < 3; ++s) ldw128(rec + 768 + g * 48 + s * 16, x0p[0][s]);
    unsigned recv[4];
#pragma unroll
    for (int r = 0; r < 4; ++r) {
      const unsigned wl = 16 * lt + 4 * g + r;
      recv[r] = (wl < nW ? L_REC + wl * RECB : L_JUNK) + 128 + n * 2;
    }
#pragma unroll
    for (int p = 0; p < 2; ++p) {
      f32x4 Z = Z4;
#pragma unroll
      for (int s = 0; s < 3; ++s) Z = mfma(hp[0][s], ldr128(c.l16 + L_X2 + p * 3072 + s * 1024), Z);
#pragma unroll
      for (int hh = 0; hh < 2; ++hh) {
        const int hd = 2 * p + hh;
        const unsigned wb = c.l16 + L_WQKV + hd * 12288;
        f32x4 T = Z4, U = Z4, W = Z4, Y = Z4;
#pragma unroll
        for (int s = 0; s < 3; ++s) {
          T = mfma(ldr128(wb + s * 1024), hp[0][s], T);
          U = mfma(ldr128(wb + 3072 + s * 1024), hp[0][s], U);
          W = mfma(ldr128(wb + 6144 + s * 1024), hp[0][s], W);
          Y = mfma(hp[0][s], ldr128(wb + 9216 + s * 1024), Y);
        }
        ldw128(rec + hd * 192 + g * 16, u32x4{cvtpk(T[0], T[1]), cvtpk(T[2], T[3]), cvtpk(U[2], U[3]), 0u});
        ldw128(rec + hd * 192 + 64 + g * 16, u32x4{cvtpk(W[0], W[1]), cvtpk(W[2], W[3]), cvtpk(U[0], U[1]), 0u});
        const bool keep = hh == 0 ? c.lo2 : !c.lo2;
#pragma unroll
        for (int r = 0; r < 4; ++r) {
          ldw16(recv[r] + hd * 192, cvtpk(Y[r], Y[r]));
          ldw16(recv[r] + hd * 192 + 32, keep ? cvtpk(Z[r], Z[r]) : ONES);
        }
      }
    }
    asm volatile("s_waitcnt lgkmcnt(0)" ::: "memory");
    if (lane == 0) *(volatile AS3 unsigned*)ldsp(L_FLAG + 4 * lt) = 1u;
    CBAR();
    __builtin_amdgcn_s_setprio(0);
  }
  PT(3);

  if (wv >= 4 && nwin == 3) __builtin_amdgcn_s_setprio(1);
  u32x4 hp[3][3], Op[3][3];
  Stat st[3];
  unsigned pbase = 0;
#pragma unroll 1
  for (unsigned rho = 0; rho < nwin; ++rho) {
    f32x4 xa[3][6];
#pragma unroll
    for (int t = 0; t < 3; ++t) { OPQ(c.cA[t]); OPQ(c.cB[t]); }
    OPQ(c.cW); OPQ(c.l16); OPQ(c.l16o); OPQ(c.pw); OPQ(c.pr);
    asm volatile("s_waitcnt vmcnt(0)" ::: "memory");
    PT(4);
    PRIO_M();
    conv_slab<0>(c, xa);
    PT(5);
    dma_slab_tab(c, m0slab, rx, wbase + 64, bm);
    if (rho > 0) {
      proj_part<0>(c, hp, st, Op, rout, pbase);
      asm volatile("s_waitcnt vmcnt(9)" ::: "memory");
    } else {
      asm volatile("s_waitcnt vmcnt(0)" ::: "memory");
    }
    PT(6);
    conv_slab<1>(c, xa);
    PT(7);
    dma_slab_tab(c, m0slab, rx, wbase + 128, bm);
    if (rho > 0) {
      proj_part<3>(c, hp, st, Op, rout, pbase);
      asm volatile("s_waitcnt vmcnt(9)" ::: "memory");
    } else {
      asm volatile("s_waitcnt vmcnt(0)" ::: "memory");
    }
    PT(8);
    conv_slab<2>(c, xa);
    pbase = wbase;
    const unsigned wl = rho * 8 + wv;
    if (rho + 1 < nwin) {
      widx = win_index(base_x, jw, rho + 1, wv);
      wr = widx / NW1; wc = widx % NW1;
      wbase = (7 * wr * IMG + 7 * wc) * PIXB;
      bm = (wr == 0 ? 1u : 0u) | (wr == NW1 - 1 ? 2u : 0u) | (wc == 0 ? 4u : 0u) | (wc == NW1 - 1 ? 8u : 0u);
      const u32x4 t1 = ldr128(c.l16o + TABO + 1024), t2 = ldr128(c.l16o + TABO + 2048);
      dma_slab(ldr128(c.l16o + TABO), t1, m0slab, rx, wbase, bm);
      l2_prefetch(t1[3], t2[0], m0junk, rx, wbase, bm);
    }
    PT(9);
    PRIO_V();
    Heads hs;
#pragma unroll
    for (int s = 0; s < 3; ++s) hs.F[s] = ldr128(c.l16 + L_WQKV + s * 1024);
    u32x4 x0p[3][3];
    layer_norm<3>(c, xa, x0p, hp, st);
    PT(10);
    if (rho != 1) spin_until(L_FLAG + 4 * (rho >> 1), 1u);
    PT(11);
    const unsigned rec = L_REC + wl * RECB;
    const unsigned lnn = (c.l16 >> 4) & 15, lgg = c.l16 >> 8;
    Rec rc;
    rc.k3 = rec + lgg * 16;
#pragma unroll
    for (int i = 0; i < 4; ++i) rc.q3[i] = (lnn >> 2) == (unsigned)i ? rc.k3 + 64 : L_ZERO;
    rc.v3 = lgg == 0 ? rec + 128 + lnn * 2 : L_ZERO;
    hs.lmin = 1.0f; hs.lmax = 1.0f;
    unsigned oL[4];
    if (rho == 1) __builtin_amdgcn_s_setprio(0);
    SB();
    head_fast<0>(c, rc, hp, st, hs, Op, oL);
    head_fast<1>(c, rc, hp, st, hs, Op, oL);
    head_fast<2>(c, rc, hp, st, hs, Op, oL);
    head_fast<3>(c, rc, hp, st, hs, Op, oL);
    head_tail<3>(hs, st, Op, oL, 0);
    head_tail<3>(hs, st, Op, oL, 1);
    const bool bad = !(hs.lmax < GUARD_HI) || !(hs.lmin > GUARD_LO);
    if (__builtin_expect(__builtin_amdgcn_ballot_w64(bad) != 0, 0)) {
      head_safe<0>(c, rc, rc.k3 + 64, hp, st, Op, oL);
      head_safe<1>(c, rc, rc.k3 + 64, hp, st, Op, oL);
      head_safe<2>(c, rc, rc.k3 + 64, hp, st, Op, oL);
      head_safe<3>(c, rc, rc.k3 + 64, hp, st, Op, oL);
    }
    {
      const unsigned oa = rec + 64 + (lnn >> 2) * 16 + (lnn & 3) * 2;
      const unsigned ob = rec + 64 + (lnn >> 2) * 16 + 8 + (lnn & 1) * 2;
      const unsigned obe = c.lo2 ? ob : L_JUNK, obo = c.lo2 ? L_JUNK : ob;
#pragma unroll
      for (int h = 0; h < 4; ++h) {
        ldw16(oa + h * 192, oL[h]);
        ldw16(((h & 1) ? obo : obe) + h * 192, oL[h] >> 16);
      }
    }
    CBAR();
    if (c.l16 == 0) __hip_atomic_fetch_add((AS3 unsigned*)ldsp(L_FLAG + 8 + 4 * (rho >> 1)), 1u, __ATOMIC_RELAXED, __HIP_MEMORY_SCOPE_WORKGROUP);
    CBAR();
    PT(12);
  }
  proj_part<0>(c, hp, st, Op, rout, pbase);
  proj_part<3>(c, hp, st, Op, rout, pbase);
  PT(13);

  if (lw) {
    OPQ(c.l16);
    const unsigned n = (c.l16 >> 4) & 15, g = c.l16 >> 8;
    const unsigned lwl = 16 * lt + n;
    const bool lvalid = lwl < nW;
    const unsigned wi = win_index(base_x, jw, lwl >> 3, lwl & 7);
    const unsigned lpix = ((7 * (wi / NW1) + LEFT_I) * IMG + 7 * (wi % NW1) + LEFT_J) * PIXB;
    spin_until(L_FLAG + 8 + 4 * lt, lt == 0 ? 16u : n2);
    const unsigned rec = L_REC + (lvalid ? lwl : 16 * lt) * RECB;
    u32x4 oh[4], xp[3];
#pragma unroll
    for (int hd = 0; hd < 4; ++hd) oh[hd] = ldr128(rec + hd * 192 + 64 + g * 16);
#pragma unroll
    for (int s = 0; s < 3; ++s) xp[s] = ldr128(rec + 768 + g * 48 + s * 16);
    const u32x4 O3[3] = {u32x4{oh[0][0], oh[0][1], oh[0][2], oh[1][0]}, u32x4{oh[1][1], oh[1][2], oh[2][0], oh[2][1]}, u32x4{oh[2][2], oh[3][0], oh[3][1], oh[3][2]}};
#pragma unroll
    for (int ct = 0; ct < 6; ++ct) {
      const unsigned d0 = xp[ct >> 1][2 * (ct & 1)], d1 = xp[ct >> 1][2 * (ct & 1) + 1];
      f32x4 acc = {bf_lo(d0), bf_hi(d0), bf_lo(d1), bf_hi(d1)};
#pragma unroll
      for (int s = 0; s < 3; ++s) acc = mfma(ldr128(c.l16o + (ct * 3 + s) * 1024), O3[s], acc);
      const u32x2 o = {cvtpk(acc[0], acc[1]), cvtpk(acc[2], acc[3])};
      __builtin_amdgcn_raw_buffer_store_b64(o, rout, lvalid ? lpix + g * 8 + ct * 32 : 0x80000000u, 0, 0);
    }
  }
}

extern "C" void kernel_launch(void* const* d_in, const int* in_sizes, int n_in, void* d_out, int out_size, void* d_ws, size_t ws_size, hipStream_t stream) {
  (void)in_sizes; (void)n_in; (void)out_size; (void)d_ws; (void)ws_size;
  wattn_kernel<<<256, 512, 0, stream>>>((const uint16_t*)d_in[0], (const uint16_t*)d_in[1], (const uint16_t*)d_in[2], (const uint16_t*)d_in[3], (uint16_t*)d_out
  );
}
```

```cpp
#define PRIO21 0x11140000u
#define PRIO20 0x11110000u
#define WBL2 0x50403120u
#include <hip/hip_runtime.h>
#include <hip/hip_bf16.h>
#include <stdint.h>

typedef __bf16 bf16x8 __attribute__((ext_vector_type(8)));
typedef __bf16 bf16x2 __attribute__((ext_vector_type(2)));
typedef float f32x4 __attribute__((ext_vector_type(4)));
typedef float f32x2 __attribute__((ext_vector_type(2)));
typedef unsigned u32x4 __attribute__((ext_vector_type(4), may_alias));
typedef unsigned u32x2 __attribute__((ext_vector_type(2), may_alias));
typedef short s16x4 __attribute__((ext_vector_type(4), may_alias));
typedef unsigned short u16a __attribute__((may_alias));
typedef unsigned u32a __attribute__((may_alias));

#define DI __device__ __forceinline__
#define AS3 __attribute__((address_space(3)))

#define PFBYTES 31168
constexpr unsigned IMG = 511, NW1 = 73;
constexpr unsigned PIXB = 192, ROWB = IMG * PIXB;
constexpr unsigned XBYTES = IMG * IMG * PIXB;
constexpr unsigned XSHIFT = (IMG + 1) * PIXB;

constexpr unsigned L_WQKV = 0;
constexpr unsigned L_X2 = 49152;
constexpr unsigned L_WO = 55296;
constexpr unsigned L_TAB = 73728;
constexpr unsigned L_ID = 76800;
constexpr unsigned L_CONV = 78848;
constexpr unsigned CONVF = 544;
constexpr unsigned L_SLAB = 95168;
constexpr unsigned SLABB = 5440;
constexpr unsigned L_REC = 138688;
constexpr unsigned RECB = 960;
constexpr unsigned L_PS = 158848;
constexpr unsigned L_ZERO = 159872;
constexpr unsigned L_FLAG = 160512;
constexpr unsigned L_JUNK = 160768;
constexpr unsigned L_LANE = L_JUNK + 1024;
constexpr unsigned L_TOTAL = L_LANE + 2048;
static_assert(L_TOTAL == 163840, "LDS map");
constexpr unsigned F_EPA = L_FLAG + 8, F_BLD1 = L_FLAG + 16, F_BLD2 = L_FLAG + 20;
constexpr unsigned TABO = L_TAB - L_WO;
constexpr unsigned IDO = L_ID - L_WO;

constexpr unsigned LROW = 38 * 16;
constexpr int LEFT_I = 2, LEFT_J = 3;

constexpr float SQC = 0.54266764f;
constexpr float ISQC = 1.0f / 0.54266764f;
constexpr float GUARD_HI = 1152921504606846976.0f;
constexpr float GUARD_LO = 1.0f / 1152921504606846976.0f;
constexpr unsigned ONES = 0x3f803f80u;

#define GUARD_HI_STR "0x5d800000"
#define CLOBBERS "v9", "v18", "v19", "v20", "v21", "v22", "v23", "v24", "v25", "v26", "v27", "v28", "v29", "v30", "v31", "v32", "v33", "v34", "v35", "v36", "v37", "v38", "v39", "v40", "v41", "v42", "v43", "v44", "v45", "v46", "v47", "v48", "v49", "v50", "v51", "v52", "v53", "v54", "v55", "v56", "v57", "v58", "v59", "v60", "v61", "v62", "v63", "v64", "v65", "v66", "v67", "v68", "v69", "v70", "v71", "v72", "v73", "v74", "v75", "v76", "v77", "v78", "v79", "v80", "v81", "v82", "v83", "v84", "v85", "v86", "v87", "v88", "v89", "v90", "v91", "v92", "v93", "v94", "v95", "v96", "v97", "v98", "v99", "v100", "v101", "v102", "v103", "v104", "v105", "v106", "v107", "v108", "v109", "v110", "v111", "v112", "v113", "v114", "v115", "v116", "v117", "v118", "v119", "v120", "v121", "v122", "v123", "v124", "v125", "v126", "v127", "v128", "v129", "v130", "v131", "v132", "v133", "v134", "v135", "v136", "v137", "v138", "v139", "v140", "v141", "v142", "v143", "v144", "v145", "v146", "v147", "v148", "v149", "v150", "v151", "v152", "v153", "v154", "v155", "v156", "v157", "v158", "v159", "v160", "v161", "v162", "v163", "v164", "v165", "v166", "v167", "v168", "v169", "v170", "v171", "v172", "v173", "v174", "v175", "v176", "v177", "v178", "v179", "v180", "v181", "v182", "v183", "v184", "v185", "v186", "v187", "v188", "v189", "v190", "v191", "v192", "v193", "v194", "v195", "v196", "v197", "v198", "v199", "v200", "v201", "v202", "v203", "v204", "v205", "v206", "v207", "v208", "v209", "v210", "v211", "v212", "v213", "v214", "v215", "v216", "v217", "v218", "v219", "v220", "v221", "v222", "v223", "v224", "v225", "v226", "v227", "v228", "v229", "v230", "v231", "v232", "v233", "v234", "v235", "v236", "v237", "v238", "v239", "v240", "v241", "v242", "v243", "s44", "s45", "s46", "s47", "s48", "s49", "s50", "s51", "s53", "s54", "s55", "s60", "s61", "s62", "s63", "s64", "s65", "s66", "s67", "s68", "s76", "s77", "s78", "s79", "s80", "s81", "s82", "s83", "s84", "s85", "s86", "s87", "s88", "s89", "s90", "s91", "s92", "s93",
__shared__ __attribute__((aligned(16))) char g_lds[L_TOTAL];

DI f32x4 mfma(u32x4 a, u32x4 b, f32x4 c) {
  return __builtin_amdgcn_mfma_f32_16x16x32_bf16(__builtin_bit_cast(bf16x8, a), __builtin_bit_cast(bf16x8, b), c, 0, 0, 0);
}
DI unsigned cvtpk(float lo, float hi) {
  f32x2 v = {lo, hi};
  bf16x2 b = __builtin_convertvector(v, bf16x2);
  return __builtin_bit_cast(unsigned, b);
}
DI AS3 char* ldsp(unsigned off) { return (AS3 char*)g_lds + off; }
DI u32x4 ldr128(unsigned off) { return *(AS3 u32x4*)ldsp(off); }
DI unsigned ldr16(unsigned off) { return *(AS3 u16a*)ldsp(off); }
DI void ldw128(unsigned off, u32x4 v) { *(AS3 u32x4*)ldsp(off) = v; }
DI void ldw32(unsigned off, unsigned v) { *(AS3 u32a*)ldsp(off) = v; }
DI void ldw16(unsigned off, unsigned v) { *(AS3 u16a*)ldsp(off) = (unsigned short)v; }
DI float ex2(float x) { return __builtin_amdgcn_exp2f(x); }
DI float bf_lo(unsigned d) { return __builtin_bit_cast(float, d << 16); }
DI float bf_hi(unsigned d) { return __builtin_bit_cast(float, d & 0xffff0000u); }
DI unsigned rfl(unsigned v) { return __builtin_amdgcn_readfirstlane(v); }
#define CBAR() asm volatile("" ::: "memory")
#define OPQ(x) asm volatile("" : "+v"(x))
#define SB() __builtin_amdgcn_sched_barrier(0)
constexpr f32x4 Z4 = {0.f, 0.f, 0.f, 0.f};
#define PRIOMODE 5
#define PRIO_M()
#define PRIO_V()

#define PT(i)

constexpr unsigned tok_code(int t, unsigned n) {
  const unsigned long long lo = t == 0 ? 0x0c05180408010b00ull : t == 1 ? 0x1d16291519121c11ull : 0x343033262a232d22ull;
  const unsigned long long hi = t == 0 ? 0x0a0309020e100d06ull : t == 1 ? 0x1b141a2028211e24ull : 0x2c252b313632352eull;
  return (unsigned)(((n < 8 ? lo : hi) >> (8 * (n & 7))) & 0xff);
}

struct Tabs {
  unsigned lds[5 * 256];
  unsigned lane[2][256];
  unsigned conv[1024];
};
constexpr Tabs make_tabs() {
  Tabs t{};
  for (unsigned lane = 0; lane < 64; ++lane) {
    const unsigned n = lane & 15, g = lane >> 4;
    unsigned vo[6] = {}, vflag = 0, pf[2] = {};
    for (unsigned i = 0; i < 6; ++i) {
      const unsigned sl = 64 * i + lane, r = sl / 38, tt = sl % 38, cc = tt >> 2, q = tt & 3;
      const bool ok = tt < 36 && sl < 340;
      vo[i] = ok ? (r * IMG + cc) * PIXB + q * 16 - (i & 3) * 1024 : 0x80000000u;
      vflag |= ((r == 0 ? 1u : 0u) | (r == 8 ? 2u : 0u) | (cc == 0 ? 4u : 0u) | (cc == 8 ? 8u : 0u)) << (4 * i);
    }
    for (unsigned i = 0; i < 2; ++i) {
      const unsigned p = 64 * i + lane, r = p / 9, cc = p % 9;
      pf[i] = p < 81 ? (r * IMG + cc) * PIXB + 128 : 0x80000000u;
    }
    for (unsigned i = 0; i < 4; ++i) t.lds[lane * 4 + i] = vo[i];
    t.lds[256 + lane * 4] = vo[4]; t.lds[256 + lane * 4 + 1] = vo[5]; t.lds[256 + lane * 4 + 2] = vflag; t.lds[256 + lane * 4 + 3] = pf[0];
    t.lds[512 + lane * 4] = pf[1];
    for (int k = 0; k < 3; ++k) {
      const unsigned code = tok_code(k, n), i = code >> 3, j = code & 7;
      t.lds[512 + lane * 4 + 1 + k] = (i * IMG + j) * PIXB + g * 16;
      const unsigned a = LROW * (i + (g >> 1)) + 64 * j + 16 * (g & 1), b = LROW * (i + 2) + 64 * (j + 2 * (g >> 1)) + 16 * (g & 1);
      t.lane[0][lane * 4 + k] = a;
      if (k == 0) t.lane[0][lane * 4 + 3] = b; else t.lane[1][lane * 4 + k - 1] = b;
    }
    const unsigned j2 = ((n & 3) >> 1) + 2 * (n >> 3);
    for (unsigned e = 0; e < 2; ++e)
      t.lds[768 + e * 256 + lane * 4 + j2] = g == 2 * ((n >> 2) & 1) + e ? 0x3f80u << (16 * (n & 1)) : 0u;
    const bool act = (g & 1) == (n >> 3);
    const unsigned G = 2 * (g >> 1) + ((n >> 2) & 1), idx = 4 * (g & 1) + (n & 3);
    const unsigned k = (G == 0 ? 0 : G == 1 ? 9 : G == 2 ? 17 : 26) + idx;
    t.lane[1][lane * 4 + 2] = L_CONV + 16 * (act ? k : (g < 2 ? 8 : 25));
    t.lane[1][lane * 4 + 3] = lane < 49 ? L_PS + (g >> 1) * 64 + (n >> 2) * 16 + (g & 1) * 8 + (n & 3) * 2 : L_JUNK + lane * 2;
  }
  for (unsigned sl = 0; sl < 1024; ++sl) {
    const unsigned ix = 4 * (sl & 255) + (sl >> 8);
    if (sl >= 1020) { t.conv[ix] = ((L_JUNK - L_CONV) / 2 + 8 * (sl - 1020)) | (1792u << 16); continue; }
    const unsigned f = sl / 34, k = sl % 34, ct = f / 5, tp = f % 5;
    if (k == 8 || k == 25) { t.conv[ix] = (8 * sl) | (1792u << 16); continue; }
    const unsigned G = (k > 8 ? 1u : 0u) + (k > 16 ? 1u : 0u) + (k > 25 ? 1u : 0u);
    const unsigned idx = (k - ((0x1a110900u >> (8 * G)) & 0xff)) & 7;
    const unsigned hi = G >> 1, m = 8 * (idx >> 2) + 4 * (G & 1) + (idx & 3);
    const unsigned tap = (unsigned)(0x8674524130ull >> (4 * (2 * tp + hi))) & 15;
    t.conv[ix] = (8 * sl + (m & 7)) | (((tap * 96 + 16 * ct + m) * 2) << 16) | ((tp == 3 && hi == 0) ? 0x80000000u : 0u);
  }
  return t;
}
static __device__ constexpr Tabs g_tabs = make_tabs();
#define PACE_PRE 0x33303330u
#define PACE_LT 0x07002000u
constexpr unsigned T_LANE = 5 * 1024, T_CONV = 7 * 1024;

struct Lane {
  unsigned cA[3], cB[3];
  unsigned cW;
  unsigned l16;
  unsigned l16o;
  unsigned bperm;
  unsigned pw, pr;
  bool q0, q1, q2;
  bool lo2;
};

#define NTPF ""
DI void dma16(unsigned m0v, unsigned voff, __amdgpu_buffer_rsrc_t srd, unsigned soff) {
  asm volatile("s_mov_b32 m0, %0\n\ts_nop 0\n\tbuffer_load_dwordx4 %1, %2, %3 offen lds" ::"s"(m0v), "v"(voff), "s"(srd), "s"(soff) : "m0", "memory");
}
DI void dma4(unsigned m0v, unsigned voff, __amdgpu_buffer_rsrc_t srd, unsigned soff) {
  asm volatile("s_mov_b32 m0, %0\n\ts_nop 0\n\tbuffer_load_dword %1, %2, %3 offen lds" ::"s"(m0v), "v"(voff), "s"(srd), "s"(soff) : "m0", "memory");
}
DI void dma4x(unsigned m0v, unsigned voff, __amdgpu_buffer_rsrc_t srd, unsigned soff) {
  asm volatile("s_mov_b32 m0, %0\n\ts_nop 0\n\tbuffer_load_dword %1, %2, %3 offen" NTPF " lds" ::"s"(m0v), "v"(voff), "s"(srd), "s"(soff) : "m0", "memory");
}
template <int OFF>
DI void dma16o(unsigned m0v, unsigned voff, __amdgpu_buffer_rsrc_t srd, unsigned soff) {
  asm volatile("s_mov_b32 m0, %0\n\ts_nop 0\n\tbuffer_load_dwordx4 %1, %2, %3 offen offset:%4 lds" ::"s"(m0v), "v"(voff), "s"(srd), "s"(soff), "i"(OFF) : "m0", "memory");
}
DI void dma16x4(unsigned m0v, unsigned voff, __amdgpu_buffer_rsrc_t srd, unsigned soff) {
  asm volatile("s_mov_b32 m0, %0\n\ts_nop 0\n\tbuffer_load_dwordx4 %1, %2, %3 offen lds\n\tbuffer_load_dwordx4 %1, %2, %3 offen offset:1024 lds\n\t"
               "buffer_load_dwordx4 %1, %2, %3 offen offset:2048 lds\n\tbuffer_load_dwordx4 %1, %2, %3 offen offset:3072 lds" ::"s"(m0v), "v"(voff), "s"(srd), "s"(soff) : "m0", "memory");
}
DI void dma16_last(unsigned m0v, unsigned voff, __amdgpu_buffer_rsrc_t srd, unsigned soff) {
  asm volatile("s_mov_b32 m0, %0\n\ts_mov_b64 exec, 0xfffff\n\tbuffer_load_dwordx4 %1, %2, %3 offen offset:1024 lds\n\ts_mov_b64 exec, -1" ::"s"(m0v), "v"(voff), "s"(srd), "s"(soff)
               : "m0", "memory");
}
DI void dma_slab(const u32x4 t0, const u32x4 t1, unsigned m0base, __amdgpu_buffer_rsrc_t srd, unsigned soff, unsigned bm) {
  asm volatile("s_waitcnt lgkmcnt(0)" ::: "memory");
  unsigned vo[6] = {t0[0], t0[1], t0[2], t0[3], t1[0], t1[1]};
  if (bm != 0) {
#pragma unroll
    for (int i = 0; i < 6; ++i) vo[i] = ((t1[2] >> (4 * i)) & bm) ? 0x80000000u : vo[i];
  }
  dma16o<0>(m0base, vo[0], srd, soff);
  dma16o<1024>(m0base, vo[1], srd, soff);
  dma16o<2048>(m0base, vo[2], srd, soff);
  dma16o<3072>(m0base, vo[3], srd, soff);
  dma16o<0>(m0base + 4096u, vo[4], srd, soff);
  dma16_last(m0base + 4096u, vo[5], srd, soff);
}
DI void dma_slab_tab(const Lane& c, unsigned m0base, __amdgpu_buffer_rsrc_t srd, unsigned soff, unsigned bm) {
  dma_slab(ldr128(c.l16o + TABO), ldr128(c.l16o + TABO + 1024), m0base, srd, soff, bm);
}
template <int S>
DI void conv_slab(const Lane& c, f32x4 (&xa)[3][6]) {
  u32x4 A[3], B[3][3];
  auto load = [&](int gi) {
    const int hh = gi / 5, tp = gi % 5, ct = 2 * S + hh;
    A[gi % 3] = ldr128(c.cW + (ct * 5 + tp) * CONVF);
#pragma unroll
    for (int t = 0; t < 3; ++t) B[gi % 3][t] = ldr128((tp == 4 ? c.cB[t] : c.cA[t] + (tp == 3 ? LROW + 64u : 64u * tp)) + 32u * hh);
  };
  load(0);
  load(1);
#pragma unroll
  for (int gi = 0; gi < 10; ++gi) {
    if (gi + 2 < 10) load(gi + 2);
    const int hh = gi / 5, tp = gi % 5, ct = 2 * S + hh;
#pragma unroll
    for (int t = 0; t < 3; ++t) xa[t][ct] = mfma(A[gi % 3], B[gi % 3][t], tp == 0 ? Z4 : xa[t][ct]);
    SB();
  }
}

template <int NT>
DI void layer_norm(const Lane& c, const f32x4 (&xa)[NT][6], u32x4 (&x0p)[NT][3], u32x4 (&hp)[NT][3]) {
  const u32x4 ones = {ONES, ONES, ONES, ONES};
  f32x4 sm[NT], sq[NT];
#pragma unroll
  for (int t = 0; t < NT; ++t) {
#pragma unroll
    for (int s = 0; s < 3; ++s) {
      x0p[t][s] = u32x4{cvtpk(xa[t][2 * s][0], xa[t][2 * s][1]), cvtpk(xa[t][2 * s][2], xa[t][2 * s][3]), cvtpk(xa[t][2 * s + 1][0], xa[t][2 * s + 1][1]),
                        cvtpk(xa[t][2 * s + 1][2], xa[t][2 * s + 1][3])};
      sq[t] = mfma(x0p[t][s], x0p[t][s], s == 0 ? Z4 : sq[t]);
      sm[t] = mfma(ones, x0p[t][s], s == 0 ? Z4 : sm[t]);
    }
    SB();
  }
  float d[NT];
#pragma unroll
  for (int t = 0; t < NT; ++t) {
    float v = sq[t][3];
    v = c.q2 ? sq[t][2] : v;
    v = c.q1 ? sq[t][1] : v;
    v = c.q0 ? sq[t][0] : v;
    d[t] = __builtin_bit_cast(float, __builtin_amdgcn_ds_bpermute((int)c.bperm, __builtin_bit_cast(int, v)));
  }
  SB();
#pragma unroll
  for (int t = 0; t < NT; ++t) {
    const float mu = sm[t][0] * (1.0f / 96.0f);
    float var = __builtin_fmaf(d[t], 1.0f / 96.0f, -mu * mu);
    var = __builtin_fmaxf(var, 0.f) + 1e-5f;
    const float rs = __builtin_amdgcn_rsqf(var);
    const float a = rs * SQC;
    const float b = -mu * a;
#pragma unroll
    for (int s = 0; s < 3; ++s) {
      float h[8];
#pragma unroll
      for (int e = 0; e < 8; ++e) h[e] = __builtin_fmaf(xa[t][2 * s + (e >> 2)][e & 3], a, b);
      hp[t][s] = u32x4{cvtpk(h[0], h[1]), cvtpk(h[2], h[3]), cvtpk(h[4], h[5]), cvtpk(h[6], h[7])};
    }
  }
}

struct Rec {
  unsigned k3;
  unsigned q3[4];
  unsigned v3;
};

DI float grp_max(float m) {
  m = __builtin_fmaxf(m, __shfl_xor(m, 16));
  m = __builtin_fmaxf(m, __shfl_xor(m, 32));
  return m;
}
template <int CTRL>
DI float quad_bcast(float v) { return __builtin_bit_cast(float, __builtin_amdgcn_mov_dpp(__builtin_bit_cast(int, v), CTRL, 0xf, 0xf, true)); }

template <int H>
DI void norm_tile(const f32x4 O0, const f32x4 O1, u32x4 (&Op)[3], float& l_out) {
  constexpr int E = (H & 1) ? 2 : 0;
  const float l = O1[2 - E];
  l_out = l;
  const float f = __builtin_amdgcn_rcpf(l) * ISQC;
  Op[(3 * H) >> 2][(3 * H) & 3] = cvtpk(O0[0] * f, O0[1] * f);
  Op[(3 * H + 1) >> 2][(3 * H + 1) & 3] = cvtpk(O0[2] * f, O0[3] * f);
  Op[(3 * H + 2) >> 2][(3 * H + 2) & 3] = cvtpk(O1[E] * f, O1[E + 1] * f);
}
template <int H>
DI unsigned norm_left(const f32x4 L0, const f32x4 L1, float& l_out) {
  const float l = quad_bcast<(H & 1) ? 0x00 : 0xaa>(L1[0]);
  l_out = l;
  const float f = __builtin_amdgcn_rcpf(l) * ISQC;
  return cvtpk(L0[0] * f, L1[0] * f);
}
DI u32x4 pack8(const f32x4 a, const f32x4 b) { return u32x4{cvtpk(a[0], a[1]), cvtpk(a[2], a[3]), cvtpk(b[0], b[1]), cvtpk(b[2], b[3])}; }
DI f32x4 ex2x4(const f32x4 a) { return f32x4{ex2(a[0]), ex2(a[1]), ex2(a[2]), ex2(a[3])}; }

template <int H>
DI void head_safe(const Lane& c, const Rec& rc, unsigned recq, const u32x4 (&hp)[3][3], u32x4 (&Op)[3][3], unsigned (&oL)[4]) {
  constexpr unsigned WB = L_WQKV + H * 12288;
  f32x4 T[3], U[3], W[3], Y[3], Z[3];
#pragma unroll
  for (int s = 0; s < 3; ++s) {
    const u32x4 fk = ldr128(c.l16 + WB + s * 1024), fr = ldr128(c.l16 + WB + 3072 + s * 1024), fq = ldr128(c.l16 + WB + 6144 + s * 1024);
    const u32x4 fv = ldr128(c.l16 + WB + 9216 + s * 1024), fx = ldr128(c.l16 + L_X2 + (H >> 1) * 3072 + s * 1024);
#pragma unroll
    for (int t = 0; t < 3; ++t) {
      T[t] = mfma(fk, hp[t][s], s == 0 ? Z4 : T[t]);
      U[t] = mfma(fr, hp[t][s], s == 0 ? Z4 : U[t]);
      W[t] = mfma(fq, hp[t][s], s == 0 ? Z4 : W[t]);
      Y[t] = mfma(hp[t][s], fv, s == 0 ? Z4 : Y[t]);
      Z[t] = mfma(hp[t][s], fx, s == 0 ? Z4 : Z[t]);
    }
    SB();
  }
  u32x4 Q[3], K[4];
#pragma unroll
  for (int t = 0; t < 3; ++t) {
    Q[t] = u32x4{cvtpk(W[t][0], W[t][1]), cvtpk(W[t][2], W[t][3]), cvtpk(U[t][0], U[t][1]), 0u};
    K[t] = u32x4{cvtpk(T[t][0], T[t][1]), cvtpk(T[t][2], T[t][3]), cvtpk(U[t][2], U[t][3]), 0u};
  }
  K[3] = ldr128(rc.k3 + H * 192);
  const u32x4 Q3 = ldr128(recq + H * 192);
  const bool keep = (H & 1) == 0 ? c.lo2 : !c.lo2;
  const u32x4 Va0 = pack8(Y[0], Y[1]);
  const u32x4 Va1 = u32x4{cvtpk(Y[2][0], Y[2][1]), cvtpk(Y[2][2], Y[2][3]), ldr16(rc.v3 + H * 192), 0u};
  const u32x4 X0 = pack8(Z[0], Z[1]);
  const u32x4 Vb0 = u32x4{keep ? X0[0] : ONES, keep ? X0[1] : ONES, keep ? X0[2] : ONES, keep ? X0[3] : ONES};
  const u32x4 Vb1 = u32x4{keep ? cvtpk(Z[2][0], Z[2][1]) : ONES, keep ? cvtpk(Z[2][2], Z[2][3]) : ONES, ldr16(rc.v3 + H * 192 + 32), 0u};
  SB();
#pragma unroll
  for (int jq = 0; jq < 3; ++jq) {
    f32x4 S[4];
#pragma unroll
    for (int i = 0; i < 4; ++i) S[i] = mfma(K[i], Q[jq], Z4);
    float m = S[3][0];
#pragma unroll
    for (int i = 0; i < 3; ++i)
#pragma unroll
      for (int r = 0; r < 4; ++r) m = __builtin_fmaxf(m, S[i][r]);
    m = grp_max(m);
#pragma unroll
    for (int i = 0; i < 3; ++i)
#pragma unroll
      for (int r = 0; r < 4; ++r) S[i][r] = ex2(S[i][r] - m);
    const float p3 = ex2(S[3][0] - m);
    const u32x4 P0 = pack8(S[0], S[1]);
    const u32x4 P1 = u32x4{cvtpk(S[2][0], S[2][1]), cvtpk(S[2][2], S[2][3]), cvtpk(p3, p3), 0u};
    f32x4 O0 = mfma(Va0, P0, Z4), O1 = mfma(Vb0, P0, Z4);
    O0 = mfma(Va1, P1, O0);
    O1 = mfma(Vb1, P1, O1);
    float l;
    norm_tile<H>(O0, O1, Op[jq], l);
    OPQ(Op[jq][(3 * H) >> 2]);
    OPQ(Op[jq][(3 * H + 2) >> 2]);
    SB();
  }
  {
    f32x4 D[4];
#pragma unroll
    for (int i = 0; i < 4; ++i) D[i] = mfma(Q3, K[i], Z4);
    const unsigned g = (c.l16 >> 8) & 3;
    float s = D[3][0];
    s = g == 2 ? D[2][0] : s;
    s = g == 1 ? D[1][0] : s;
    s = g == 0 ? D[0][0] : s;
    float m = s;
#pragma unroll
    for (int o = 1; o < 64; o <<= 1) m = __builtin_fmaxf(m, __shfl_xor(m, o));
    const float p = ex2(s - m);
    ldw16(c.pw, cvtpk(p, p));
    const u32x4 PL0 = ldr128(c.pr), PL1 = ldr128(c.pr + 64);
    f32x4 L0 = mfma(PL0, Va0, Z4), L1 = mfma(PL0, Vb0, Z4);
    L0 = mfma(PL1, Va1, L0);
    L1 = mfma(PL1, Vb1, L1);
    float l;
    oL[H] = norm_left<H>(L0, L1, l);
    OPQ(oL[H]);
    SB();
  }
}

template <int NT>
DI void proj_pair(const Lane& c, int p, const u32x4 (&x0p)[NT][3], const u32x4 (&Op)[NT][3], u32x4 (&o)[NT]) {
#pragma unroll
  for (int e = 0; e < 2; ++e) {
    f32x4 acc[NT];
    const u32x4 I = ldr128(c.l16o + IDO + e * 1024);
#pragma unroll
    for (int t = 0; t < NT; ++t) acc[t] = mfma(I, x0p[t][p], Z4);
#pragma unroll
    for (int s = 0; s < 3; ++s) {
      const u32x4 A = ldr128(c.l16o + ((2 * p + e) * 3 + s) * 1024);
#pragma unroll
      for (int t = 0; t < NT; ++t) acc[t] = mfma(A, Op[t][s], acc[t]);
    }
#pragma unroll
    for (int t = 0; t < NT; ++t) {
      o[t][2 * e] = cvtpk(acc[t][0], acc[t][1]);
      o[t][2 * e + 1] = cvtpk(acc[t][2], acc[t][3]);
    }
  }
}

DI unsigned win_index(unsigned base_x, unsigned j, unsigned rho, unsigned w) { return base_x + (rho < 2 ? rho * 256 + j * 8 + w : 512 + 32 * w + j); }

DI void spin_until(unsigned off, unsigned target) {
  while (rfl(*(volatile AS3 unsigned*)ldsp(off)) < target) __builtin_amdgcn_s_sleep(2);
  CBAR();
}

DI u32x4 tr_pair(unsigned a0, unsigned a1) {
  const s16x4 lo = __builtin_amdgcn_ds_read_tr16_b64_v4i16((AS3 s16x4*)ldsp(a0));
  const s16x4 hi = __builtin_amdgcn_ds_read_tr16_b64_v4i16((AS3 s16x4*)ldsp(a1));
  const u32x2 l2 = __builtin_bit_cast(u32x2, lo), h2 = __builtin_bit_cast(u32x2, hi);
  return u32x4{l2[0], l2[1], h2[0], h2[1]};
}
DI void lds_barrier() {
  asm volatile("s_waitcnt lgkmcnt(0)" ::: "memory");
  __builtin_amdgcn_s_barrier();
  CBAR();
}
DI void tr_plain(u32x4& f0, u32x4& f1, u32x4& f2, unsigned lane, unsigned colb) {
  const unsigned la = (lane >> 2) * 576 + (lane & 3) * 8 + colb * 2;
  f0 = tr_pair(la, la + 16 * 576);
  f1 = tr_pair(la + 32 * 576, la + 48 * 576);
  f2 = tr_pair(la + 64 * 576, la + 80 * 576);
}
DI unsigned wo_row(unsigned sq, unsigned g) {
  const unsigned hh = (sq * 43) >> 8, ix = sq - 6 * hh;
  return (hh * 24 + (ix < 4 ? 4 * g + ix : 12 + 2 * g + ix)) * 192;
}
DI void tr_wo(u32x4& f0, u32x4& f1, u32x4& f2, unsigned lane, unsigned ct) {
  const unsigned r = (lane >> 2) & 3, g = lane >> 4, b = L_WO + (ct >> 1) * 64 + (ct & 1) * 8 + (lane & 3) * 16;
  f0 = tr_pair(b + wo_row(r, g), b + wo_row(4 + r, g));
  f1 = tr_pair(b + wo_row(8 + r, g), b + wo_row(12 + r, g));
  f2 = tr_pair(b + wo_row(16 + r, g), b + wo_row(20 + r, g));
}
DI void gather_ld(unsigned (&raw)[24], unsigned g, unsigned col) {
  const unsigned ga = 4 * g * 576 + col * 2;
#pragma unroll
  for (int s = 0; s < 3; ++s)
#pragma unroll
    for (int d = 0; d < 4; ++d) {
      const unsigned k = 32 * s + 16 * (d >> 1) + 2 * (d & 1);
      raw[8 * s + 2 * d] = ldr16(ga + k * 576);
      raw[8 * s + 2 * d + 1] = ldr16(ga + (k + 1) * 576);
    }
}
DI void gather_pk(const unsigned (&raw)[24], u32x4& f0, u32x4& f1, u32x4& f2) {
  f0 = u32x4{raw[0] | (raw[1] << 16), raw[2] | (raw[3] << 16), raw[4] | (raw[5] << 16), raw[6] | (raw[7] << 16)};
  f1 = u32x4{raw[8] | (raw[9] << 16), raw[10] | (raw[11] << 16), raw[12] | (raw[13] << 16), raw[14] | (raw[15] << 16)};
  f2 = u32x4{raw[16] | (raw[17] << 16), raw[18] | (raw[19] << 16), raw[20] | (raw[21] << 16), raw[22] | (raw[23] << 16)};
}

__global__ __launch_bounds__(512, 2) __attribute__((target("no-packed-fp32-ops")))
void wattn_kernel(const uint16_t* __restrict__ x, const uint16_t* __restrict__ dw, const uint16_t* __restrict__ wqkv, const uint16_t* __restrict__ wo,
                  uint16_t* __restrict__ out
                  ) {
  const unsigned tid = threadIdx.x;
  const unsigned wv = rfl(tid >> 6);
  Lane c;
  const unsigned lane = tid & 63, n = lane & 15, g = lane >> 4;
  const unsigned lds0 = (unsigned)(size_t)(AS3 char*)g_lds;
  const unsigned slab = L_SLAB + wv * SLABB;
  const unsigned m0slab = rfl(lds0 + slab);
  c.l16 = lane * 16;
  const __amdgpu_buffer_rsrc_t rt = __builtin_amdgcn_make_buffer_rsrc((void*)&g_tabs, (short)0, sizeof(Tabs), 0x00020000);
  u32x4 ce;
  if (wv < 4) {
    uint64_t pc;
    asm volatile("s_getpc_b64 %0" : "=s"(pc));
    const __amdgpu_buffer_rsrc_t rc = __builtin_amdgcn_make_buffer_rsrc((void*)pc, (short)0, PFBYTES, 0x00020000);
    dma4(rfl(lds0 + L_JUNK), tid * 128, rc, 0);
    asm volatile("buffer_load_dwordx4 %0, %1, %2, %3 offen" : "=&v"(ce) : "v"(tid * 16), "s"(rt), "s"(T_CONV) : "memory");
    dma16(lds0 + L_TAB + wv * 1024, c.l16, rt, wv * 1024);
    if (wv < 3) dma16(wv == 0 ? lds0 + L_TAB + 4096 : lds0 + L_LANE - 1024 + wv * 1024, c.l16, rt, 4096 + wv * 1024);
  }
  PT(16);
  const unsigned xcd = blockIdx.x & 7, jw = blockIdx.x >> 3;
  const unsigned base_x = xcd * 666 + (xcd ? 1 : 0), cnt_x = 666 + (xcd == 0 ? 1 : 0);
  const unsigned n2 = 4 + ((640 + jw < cnt_x) ? 1 : 0);
  const unsigned nW = 16 + n2;
  const unsigned nwin = wv < n2 ? 3 : 2;
  unsigned wb[3], bmp = 0;
#pragma unroll
  for (unsigned rho = 0; rho < 3; ++rho) {
    const unsigned wi = win_index(base_x, jw, rho, wv), r = wi / NW1, cc = wi % NW1;
    const unsigned b = (r == 0 ? 1u : 0u) | (r == NW1 - 1 ? 2u : 0u) | (cc == 0 ? 4u : 0u) | (cc == NW1 - 1 ? 8u : 0u);
    wb[rho] = (7 * r * IMG + 7 * cc) * PIXB;
    bmp |= (rho < nwin ? b : 16u) << (8 * rho);
  }
  unsigned pfoff;
  {
    const unsigned wi = win_index(base_x, jw, 0, wv & 2 ? 4 : 0), wr = wi / NW1, wc = wi % NW1;
    const unsigned p = 64 * (wv & 1) + lane, r = (p * 4682) >> 16, off = (7 * wr * IMG + 7 * wc) * PIXB + r * ROWB + (p - 14 * r) * 128;
    pfoff = (p < 126 && off >= XSHIFT) ? off : 0x80000000u;
  }
  const bool lw = wv == 3 || wv == 6;
  const unsigned lt = wv == 3 ? 0 : 1;
  unsigned lp0 = 0, lp3 = 0, lp4 = 0, lrec = 0, lrecx = 0, lrv0 = 0, lrv1 = 0, lrv2 = 0, lrv3 = 0;
  if (lw) {
    const unsigned lim = lt ? nW : 8, lwl = 8 * lt + n;
    const bool lvalid = lwl < lim;
    const unsigned wi = win_index(base_x, jw, lwl >> 3, lwl & 7);
    const unsigned r = wi / NW1, cc = wi % NW1, hi = g >> 1;
    const unsigned base = lvalid ? ((7 * r + LEFT_I) * IMG + 7 * cc + LEFT_J) * PIXB + (g & 1) * 16 : 0x80000000u;
    lp0 = base + hi * ROWB;
    lp3 = lp0 + XSHIFT;
    lp4 = base + (XSHIFT + ROWB - PIXB) + hi * 2 * PIXB;
    const unsigned rec = lvalid ? L_REC + lwl * RECB : L_JUNK;
    lrec = rec + g * 16;
    lrecx = rec + g * 48;
    const unsigned w0 = 8 * lt + 4 * g, ra = L_REC + w0 * RECB + 128 + n * 2, rj = L_JUNK + 128 + n * 2;
    lrv0 = w0 < lim ? ra : rj;
    lrv1 = w0 + 1 < lim ? ra + RECB : rj;
    lrv2 = w0 + 2 < lim ? ra + 2 * RECB : rj;
    lrv3 = w0 + 3 < lim ? ra + 3 * RECB : rj;
  }
  c.l16o = L_WO + lane * 16;
  c.bperm = 4 * ((n >> 2) * 16 + n);
  c.pr = L_PS + wv * 128 + g * 16;
  u32x4 sa = {rfl(wb[0]), rfl(wb[1]), rfl(wb[2]), rfl(bmp)};
  u32x4 sb = {rfl(nwin - 1), m0slab, rfl(L_REC + wv * RECB), rfl(L_PS + wv * 128)};
  const bool builder = wv >= 5;
  const unsigned pcnt = ((n2 == 5 ? PRIO21 : PRIO20) >> (4 * wv)) & 7;
  unsigned flags = rfl((pcnt ? 1u : 0u) | (pcnt ? (pcnt - 1) << 28 : 0u) | (lw ? 4u : 0u) | (wv == 6 ? 8u : 0u) | (((PACE_PRE >> (4 * wv)) & 7) << 5) | (((PACE_LT >> (4 * wv)) & 7) << 8));
  asm volatile("" : "+s"(sa), "+s"(sb), "+s"(flags), "+v"(c.l16), "+v"(c.l16o), "+v"(c.bperm), "+v"(c.pr), "+v"(lp0), "+v"(lp3), "+v"(lp4), "+v"(lrec), "+v"(lrecx),
               "+v"(lrv0), "+v"(lrv1), "+v"(lrv2), "+v"(lrv3), "+v"(pfoff));
  SB();
  PT(17);
  const __amdgpu_buffer_rsrc_t rx = __builtin_amdgcn_make_buffer_rsrc((void*)((const char*)x - XSHIFT), (short)0, (int)(XBYTES + XSHIFT), 0x00020000);
  const __amdgpu_buffer_rsrc_t rout = __builtin_amdgcn_make_buffer_rsrc((void*)out, (short)0, (int)XBYTES, 0x00020000);
  if (wv < 4) {
    const __amdgpu_buffer_rsrc_t rd = __builtin_amdgcn_make_buffer_rsrc((void*)dw, (short)0, 9 * 96 * 2, 0x00020000);
    dma16o<0>(m0slab, c.l16, rd, 0);
    dma16o<1024>(m0slab, c.l16, rd, 0);
    const __amdgpu_buffer_rsrc_t rw = __builtin_amdgcn_make_buffer_rsrc((void*)(wv < 3 ? wqkv : wo), (short)0, wv < 3 ? 96 * 288 * 2 : 96 * 96 * 2, 0x00020000);
    const unsigned wl = lds0 + wv * 18432, ws = wv < 3 ? wv * 18432 : 0;
#pragma unroll
    for (unsigned i = 0; i < 4; ++i) {
      dma16x4(wl + i * 4096, c.l16, rw, ws + i * 4096);
    }
    dma16o<0>(wl + 16384, c.l16, rw, ws + 16384);
    dma16o<1024>(wl + 16384, c.l16, rw, ws + 16384);
    PT(18);
    asm volatile("s_waitcnt vmcnt(18)" : "+v"(ce)::"memory");
    PT(19);
    unsigned w[4];
#pragma unroll
    for (int k = 0; k < 4; ++k) w[k] = ldr16(slab + ((ce[k] >> 16) & 0x7fff));
    const u32x4 z = {0u, 0u, 0u, 0u};
#pragma unroll
    for (int k = 0; k < 4; ++k) ldw128((L_CONV + 2 * (ce[k] & 0xffff)) & ~15u, z);
    CBAR();
#pragma unroll
    for (int k = 0; k < 4; ++k) ldw16(L_CONV + 2 * (ce[k] & 0xffff), (int)ce[k] < 0 ? 0x3f80u : w[k]);
    CBAR();
    dma4x(rfl(lds0 + L_JUNK), pfoff, rx, 0);
    PT(0);
    asm volatile("s_waitcnt vmcnt(1) lgkmcnt(0)" ::: "memory");
  } else {
    if (tid - 256 < (L_FLAG + 32 - L_PS) / 8) *(AS3 u32x2*)ldsp(L_PS + (tid - 256) * 8) = u32x2{0u, 0u};
    PT(0);
    asm volatile("s_waitcnt lgkmcnt(0)" ::: "memory");
  }
  __builtin_amdgcn_s_barrier();
  CBAR();
  PT(1);
  if (builder) {
    __builtin_amdgcn_s_setprio(3);
    const bool lo2 = (n & 3) < 2;
    const unsigned rcol = 16 + 2 * (n >> 2) + (n & 1);
    u32x4 f[24];
    unsigned raw[2][24];
    if (wv < 7) {
#pragma unroll
      for (int hh = 0; hh < 2; ++hh) gather_ld(raw[hh], g, (lo2 ? 0 : 96) + 48 * (wv - 5) + 24 * hh + rcol);
#pragma unroll
      for (int hh = 0; hh < 2; ++hh) {
        const unsigned cb = 48 * (wv - 5) + 24 * hh;
        tr_plain(f[12 * hh], f[12 * hh + 1], f[12 * hh + 2], lane, 96 + cb);
        tr_plain(f[12 * hh + 6], f[12 * hh + 7], f[12 * hh + 8], lane, cb);
        tr_plain(f[12 * hh + 9], f[12 * hh + 10], f[12 * hh + 11], lane, 192 + cb);
      }
      SB();
#pragma unroll
      for (int hh = 0; hh < 2; ++hh) gather_pk(raw[hh], f[12 * hh + 3], f[12 * hh + 4], f[12 * hh + 5]);
    } else {
#pragma unroll
      for (int p = 0; p < 2; ++p) gather_ld(raw[p], g, 192 + 48 * p + (lo2 ? 0 : 24) + rcol);
#pragma unroll
      for (int ct = 0; ct < 6; ++ct) tr_wo(f[6 + 3 * ct], f[7 + 3 * ct], f[8 + 3 * ct], lane, ct);
      SB();
#pragma unroll
      for (int p = 0; p < 2; ++p) gather_pk(raw[p], f[3 * p], f[3 * p + 1], f[3 * p + 2]);
    }
#pragma unroll
    for (int j = 0; j < 24; ++j) OPQ(f[j]);
    asm volatile("s_waitcnt lgkmcnt(0)" ::: "memory");
    if (lane == 0) __hip_atomic_fetch_add((AS3 unsigned*)ldsp(F_BLD1), 1u, __ATOMIC_RELAXED, __HIP_MEMORY_SCOPE_WORKGROUP);
    CBAR();
    spin_until(F_BLD1, 3u);
    const unsigned da = (wv - 5) * 24576 + lane * 16;
#pragma unroll
    for (int j = 0; j < 24; ++j) ldw128(da + j * 1024, f[j]);
    asm volatile("s_waitcnt lgkmcnt(0)" ::: "memory");
    if (lane == 0) __hip_atomic_fetch_add((AS3 unsigned*)ldsp(F_BLD2), 1u, __ATOMIC_RELAXED, __HIP_MEMORY_SCOPE_WORKGROUP);
    CBAR();
    __builtin_amdgcn_s_setprio(0);
  }
  u32x4 ft0 = ldr128(L_TAB + c.l16), ft1 = ldr128(L_TAB + 1024 + c.l16);
  {
    const u32x4 lc0 = ldr128(L_LANE + c.l16), lc1 = ldr128(L_LANE + 1024 + c.l16);
    c.cA[0] = lc0[0] + slab; c.cA[1] = lc0[1] + slab; c.cA[2] = lc0[2] + slab;
    c.cB[0] = lc0[3] + slab; c.cB[1] = lc1[0] + slab; c.cB[2] = lc1[1] + slab;
    c.cW = lc1[2];
    c.pw = lc1[3] + (lane < 49 ? wv * 128 : 0u);
    asm volatile("" : "+v"(c.cA[0]), "+v"(c.cA[1]), "+v"(c.cA[2]), "+v"(c.cB[0]), "+v"(c.cB[1]), "+v"(c.cB[2]), "+v"(c.cW), "+v"(c.pw));
  }
  PT(2);

  unsigned badmask = 0;
  {
    asm volatile(
    "s_mov_b32 s76, 0x11111111\n\t"
    "s_mov_b32 s77, 0x11111111\n\t"
    "s_mov_b32 s78, 0x22222222\n\t"
    "s_mov_b32 s79, 0x22222222\n\t"
    "s_mov_b32 s80, 0x44444444\n\t"
    "s_mov_b32 s81, 0x44444444\n\t"
    "s_mov_b32 s82, 0x33333333\n\t"
    "s_mov_b32 s83, 0x33333333\n\t"
    "s_mov_b32 s84, 0xf000f\n\t"
    "s_mov_b32 s85, 0xf000f\n\t"
    "s_mov_b32 s86, 0xf000f0\n\t"
    "s_mov_b32 s87, 0xf000f0\n\t"
    "s_mov_b32 s88, 0xf000f00\n\t"
    "s_mov_b32 s89, 0xf000f00\n\t"
    "s_mov_b32 s90, 0xf000f000\n\t"
    "s_mov_b32 s91, 0xf000f000\n\t"
    "s_mov_b32 s92, 0xffff\n\t"
    "s_mov_b32 s93, 0x0\n\t"
    "s_mov_b32 s45, s20\n\t"
    "s_mov_b32 s46, s21\n\t"
    "s_mov_b32 s47, s22\n\t"
    "s_and_b32 s48, s23, 0xff\n\t"
    "s_bfe_u32 s49, s23, 0x80008\n\t"
    "s_bfe_u32 s50, s23, 0x80010\n\t"
    "s_mov_b32 s51, s24\n\t"
    "s_mov_b32 s53, s26\n\t"
    "s_mov_b32 s67, " GUARD_HI_STR "\n\t"
    "s_mov_b32 s68, 0x21800000\n\t"
    "s_mov_b32 s55, 0\n\t"
    "s_mov_b32 s54, 0\n\t"
    "s_bitcmp1_b32 s28, 2\n\t"
    "s_cbranch_scc1 .Lw_lt_%=\n\t"
    ".Lw_pre_%=:\n\t"
    "s_bitcmp0_b32 s28, 0\n\t"
    "s_cbranch_scc1 .Lw_noprio_%=\n\t"
    "s_setprio 1\n\t"
    ".Lw_noprio_%=:\n\t"
    "ds_read_b128 v[72:75], v252 offset:20480\n\t"
    "s_bfe_u32 s61, s28, 0x30005\n\t"
    "s_cmp_lg_u32 s61, 0\n\t"
    "s_cbranch_scc1 .Lw_sp4_%=\n\t"
    ".Lw_spb4_%=:\n\t"
    "s_cmp_lg_u32 s48, 0\n\t"
    "s_cbranch_scc1 .Lw_stub5_%=\n\t"
    ".Lw_back5_%=:\n\t"
    "s_mov_b32 m0, s25\n\t"
    "s_mov_b32 s60, s45\n\t"
    "buffer_load_dwordx4 v10, s[36:39], s60 offen lds\n\t"
    "buffer_load_dwordx4 v11, s[36:39], s60 offen offset:1024 lds\n\t"
    "ds_read_b128 v[0:3], v250 offset:0\n\t"
    "buffer_load_dwordx4 v12, s[36:39], s60 offen offset:2048 lds\n\t"
    "ds_read_b128 v[4:7], v250 offset:544\n\t"
    "buffer_load_dwordx4 v13, s[36:39], s60 offen offset:3072 lds\n\t"
    "ds_read_b128 v[16:19], v250 offset:1088\n\t"
    "s_add_u32 m0, s25, 4096\n\t"
    "s_nop 0\n\t"
    "buffer_load_dwordx4 v14, s[36:39], s60 offen lds\n\t"
    "s_mov_b64 exec, 0xfffff\n\t"
    "buffer_load_dwordx4 v15, s[36:39], s60 offen offset:1024 lds\n\t"
    "s_mov_b64 exec, -1\n\t"
    "s_add_u32 s60, s45, 64\n\t"
    "buffer_load_dwordx4 v[20:23], v10, s[36:39], s60 offen\n\t"
    "buffer_load_dwordx4 v[24:27], v11, s[36:39], s60 offen offset:1024\n\t"
    "ds_read_b128 v[28:31], v250 offset:1632\n\t"
    "buffer_load_dwordx4 v[52:55], v12, s[36:39], s60 offen offset:2048\n\t"
    "v_add_u32 v8, s25, v251\n\t"
    "buffer_load_dwordx4 v[240:243], v13, s[36:39], s60 offen offset:3072\n\t"
    "v_mov_b32 v128, 0x3f803f80\n\t"
    "buffer_load_dwordx4 v[64:67], v14, s[36:39], s60 offen\n\t"
    "v_mov_b32 v129, v128\n\t"
    "buffer_load_dwordx4 v[68:71], v15, s[36:39], s60 offen offset:1024\n\t"
    "v_mov_b32 v130, v128\n\t"
    "s_add_u32 s60, s45, 128\n\t"
    "buffer_load_dwordx4 v[76:79], v10, s[36:39], s60 offen\n\t"
    "buffer_load_dwordx4 v[236:239], v11, s[36:39], s60 offen offset:1024\n\t"
    "v_mov_b32 v131, v128\n\t"
    "buffer_load_dwordx4 v[232:235], v12, s[36:39], s60 offen offset:2048\n\t"
    "buffer_load_dwordx4 v[104:107], v13, s[36:39], s60 offen offset:3072\n\t"
    "buffer_load_dwordx4 v[108:111], v14, s[36:39], s60 offen\n\t"
    "buffer_load_dwordx4 v[112:115], v15, s[36:39], s60 offen offset:1024\n\t"
    "v_mov_b32 v9, 160536\n\t"
    "v_mov_b32 v10, 1\n\t"
    "s_mov_b64 exec, 1\n\t"
    "ds_add_u32 v9, v10\n\t"
    "s_mov_b64 exec, -1\n\t"
    "s_waitcnt vmcnt(12)\n\t"
    "ds_read_b128 v[12:15], v244 offset:0\n\t"
    "ds_read_b128 v[32:35], v245 offset:0\n\t"
    "ds_read_b128 v[36:39], v246 offset:0\n\t"
    "ds_read_b128 v[228:231], v244 offset:64\n\t"
    "ds_read_b128 v[40:43], v245 offset:64\n\t"
    "ds_read_b128 v[44:47], v246 offset:64\n\t"
    "ds_read_b128 v[48:51], v244 offset:128\n\t"
    "ds_read_b128 v[224:227], v245 offset:128\n\t"
    "ds_read_b128 v[84:87], v246 offset:128\n\t"
    "ds_read_b128 v[88:91], v244 offset:672\n\t"
    "s_waitcnt lgkmcnt(9)\n\t"
    "v_mfma_f32_16x16x32_bf16 v[80:83], v[0:3], v[12:15], 0\n\t"
    "ds_read_b128 v[12:15], v245 offset:672\n\t"
    "s_waitcnt lgkmcnt(9)\n\t"
    "v_mfma_f32_16x16x32_bf16 v[60:63], v[0:3], v[32:35], 0\n\t"
    "ds_read_b128 v[32:35], v246 offset:672\n\t"
    "s_waitcnt lgkmcnt(9)\n\t"
    "v_mfma_f32_16x16x32_bf16 v[56:59], v[0:3], v[36:39], 0\n\t"
    "ds_read_b128 v[0:3], v250 offset:2176\n\t"
    "ds_read_b128 v[220:223], v247 offset:0\n\t"
    "s_waitcnt lgkmcnt(10)\n\t"
    "v_mfma_f32_16x16x32_bf16 v[80:83], v[4:7], v[228:231], v[80:83]\n\t"
    "ds_read_b128 v[36:39], v248 offset:0\n\t"
    "s_waitcnt lgkmcnt(10)\n\t"
    "v_mfma_f32_16x16x32_bf16 v[60:63], v[4:7], v[40:43], v[60:63]\n\t"
    "ds_read_b128 v[228:231], v249 offset:0\n\t"
    "s_waitcnt lgkmcnt(10)\n\t"
    "v_mfma_f32_16x16x32_bf16 v[56:59], v[4:7], v[44:47], v[56:59]\n\t"
    "ds_read_b128 v[216:219], v250 offset:2720\n\t"
    "ds_read_b128 v[4:7], v244 offset:32\n\t"
    "s_waitcnt lgkmcnt(11)\n\t"
    "v_mfma_f32_16x16x32_bf16 v[80:83], v[16:19], v[48:51], v[80:83]\n\t"
    "ds_read_b128 v[40:43], v245 offset:32\n\t"
    "s_waitcnt lgkmcnt(11)\n\t"
    "v_mfma_f32_16x16x32_bf16 v[60:63], v[16:19], v[224:227], v[60:63]\n\t"
    "ds_read_b128 v[224:227], v246 offset:32\n\t"
    "s_waitcnt lgkmcnt(11)\n\t"
    "v_mfma_f32_16x16x32_bf16 v[56:59], v[16:19], v[84:87], v[56:59]\n\t"
    "ds_read_b128 v[16:19], v250 offset:3264\n\t"
    "ds_read_b128 v[212:215], v244 offset:96\n\t"
    "s_waitcnt lgkmcnt(12)\n\t"
    "v_mfma_f32_16x16x32_bf16 v[80:83], v[28:31], v[88:91], v[80:83]\n\t"
    "ds_read_b128 v[84:87], v245 offset:96\n\t"
    "s_waitcnt lgkmcnt(12)\n\t"
    "v_mfma_f32_16x16x32_bf16 v[60:63], v[28:31], v[12:15], v[60:63]\n\t"
    "ds_read_b128 v[12:15], v246 offset:96\n\t"
    "s_waitcnt lgkmcnt(12)\n\t"
    "v_mfma_f32_16x16x32_bf16 v[56:59], v[28:31], v[32:35], v[56:59]\n\t"
    "ds_read_b128 v[208:211], v250 offset:3808\n\t"
    "ds_read_b128 v[88:91], v244 offset:160\n\t"
    "s_waitcnt lgkmcnt(12)\n\t"
    "v_mfma_f32_16x16x32_bf16 v[80:83], v[0:3], v[220:223], v[80:83]\n\t"
    "ds_read_b128 v[92:95], v245 offset:160\n\t"
    "s_waitcnt lgkmcnt(12)\n\t"
    "v_mfma_f32_16x16x32_bf16 v[60:63], v[0:3], v[36:39], v[60:63]\n\t"
    "ds_read_b128 v[96:99], v246 offset:160\n\t"
    "s_waitcnt lgkmcnt(12)\n\t"
    "v_mfma_f32_16x16x32_bf16 v[56:59], v[0:3], v[228:231], v[56:59]\n\t"
    "ds_read_b128 v[228:231], v250 offset:4352\n\t"
    "ds_read_b128 v[220:223], v244 offset:704\n\t"
    "s_waitcnt lgkmcnt(12)\n\t"
    "v_mfma_f32_16x16x32_bf16 v[48:51], v[216:219], v[4:7], 0\n\t"
    "ds_read_b128 v[204:207], v245 offset:704\n\t"
    "v_cvt_pk_bf16_f32 v36, v80, v81\n\t"
    "v_cvt_pk_bf16_f32 v37, v82, v83\n\t"
    "s_waitcnt lgkmcnt(12)\n\t"
    "v_mfma_f32_16x16x32_bf16 v[44:47], v[216:219], v[40:43], 0\n\t"
    "ds_read_b128 v[200:203], v246 offset:704\n\t"
    "v_cvt_pk_bf16_f32 v32, v60, v61\n\t"
    "v_cvt_pk_bf16_f32 v33, v62, v63\n\t"
    "s_waitcnt lgkmcnt(12)\n\t"
    "v_mfma_f32_16x16x32_bf16 v[40:43], v[216:219], v[224:227], 0\n\t"
    "ds_read_b128 v[224:227], v250 offset:4896\n\t"
    "ds_read_b128 v[0:3], v247 offset:32\n\t"
    "v_cvt_pk_bf16_f32 v28, v56, v57\n\t"
    "s_waitcnt lgkmcnt(12)\n\t"
    "v_mfma_f32_16x16x32_bf16 v[48:51], v[16:19], v[212:215], v[48:51]\n\t"
    "ds_read_b128 v[4:7], v248 offset:32\n\t"
    "v_cvt_pk_bf16_f32 v29, v58, v59\n\t"
    "s_waitcnt lgkmcnt(12)\n\t"
    "v_mfma_f32_16x16x32_bf16 v[44:47], v[16:19], v[84:87], v[44:47]\n\t"
    "ds_read_b128 v[216:219], v249 offset:32\n\t"
    "s_waitcnt vmcnt(11)\n\t"
    "ds_write_b128 v8, v[20:23] offset:0\n\t"
    "s_waitcnt vmcnt(10)\n\t"
    "ds_write_b128 v8, v[24:27] offset:1024\n\t"
    "s_waitcnt vmcnt(9)\n\t"
    "ds_write_b128 v8, v[52:55] offset:2048\n\t"
    "s_waitcnt vmcnt(8)\n\t"
    "ds_write_b128 v8, v[240:243] offset:3072\n\t"
    "s_waitcnt vmcnt(7)\n\t"
    "ds_write_b128 v8, v[64:67] offset:4096\n\t"
    "s_waitcnt vmcnt(6)\n\t"
    "s_mov_b64 exec, 0xfffff\n\t"
    "ds_write_b128 v8, v[68:71] offset:5120\n\t"
    "s_mov_b64 exec, -1\n\t"
    "s_waitcnt lgkmcnt(15)\n\t"
    "v_mfma_f32_16x16x32_bf16 v[40:43], v[16:19], v[12:15], v[40:43]\n\t"
    "v_mfma_f32_16x16x32_bf16 v[48:51], v[208:211], v[88:91], v[48:51]\n\t"
    "v_mfma_f32_16x16x32_bf16 v[44:47], v[208:211], v[92:95], v[44:47]\n\t"
    "s_waitcnt lgkmcnt(14)\n\t"
    "v_mfma_f32_16x16x32_bf16 v[40:43], v[208:211], v[96:99], v[40:43]\n\t"
    "s_waitcnt lgkmcnt(12)\n\t"
    "v_mfma_f32_16x16x32_bf16 v[48:51], v[228:231], v[220:223], v[48:51]\n\t"
    "s_waitcnt lgkmcnt(11)\n\t"
    "v_mfma_f32_16x16x32_bf16 v[44:47], v[228:231], v[204:207], v[44:47]\n\t"
    "s_waitcnt lgkmcnt(10)\n\t"
    "v_mfma_f32_16x16x32_bf16 v[40:43], v[228:231], v[200:203], v[40:43]\n\t"
    "ds_read_b128 v[16:19], v250 offset:5440\n\t"
    "ds_read_b128 v[12:15], v244 offset:0\n\t"
    "s_waitcnt lgkmcnt(10)\n\t"
    "v_mfma_f32_16x16x32_bf16 v[48:51], v[224:227], v[0:3], v[48:51]\n\t"
    "ds_read_b128 v[0:3], v245 offset:0\n\t"
    "s_waitcnt lgkmcnt(10)\n\t"
    "v_mfma_f32_16x16x32_bf16 v[44:47], v[224:227], v[4:7], v[44:47]\n\t"
    "ds_read_b128 v[4:7], v246 offset:0\n\t"
    "s_waitcnt lgkmcnt(10)\n\t"
    "v_mfma_f32_16x16x32_bf16 v[40:43], v[224:227], v[216:219], v[40:43]\n\t"
    "ds_read_b128 v[20:23], v250 offset:5984\n\t"
    "ds_read_b128 v[240:243], v244 offset:64\n\t"
    "v_cvt_pk_bf16_f32 v38, v48, v49\n\t"
    "v_cvt_pk_bf16_f32 v39, v50, v51\n\t"
    "s_nop 0\n\t"
    "v_cvt_pk_bf16_f32 v34, v44, v45\n\t"
    "v_cvt_pk_bf16_f32 v35, v46, v47\n\t"
    "v_mfma_f32_16x16x32_bf16 v[88:91], v[36:39], v[36:39], 0\n\t"
    "ds_read_b128 v[24:27], v245 offset:64\n\t"
    "v_cvt_pk_bf16_f32 v30, v40, v41\n\t"
    "v_cvt_pk_bf16_f32 v31, v42, v43\n\t"
    "v_mfma_f32_16x16x32_bf16 v[152:155], v[128:131], v[36:39], 0\n\t"
    "ds_read_b128 v[68:71], v246 offset:64\n\t"
    "v_mfma_f32_16x16x32_bf16 v[140:143], v[32:35], v[32:35], 0\n\t"
    "ds_read_b128 v[84:87], v250 offset:6528\n\t"
    "ds_read_b128 v[116:119], v244 offset:128\n\t"
    "v_mfma_f32_16x16x32_bf16 v[100:103], v[128:131], v[32:35], 0\n\t"
    "ds_read_b128 v[120:123], v245 offset:128\n\t"
    "v_mfma_f32_16x16x32_bf16 v[96:99], v[28:31], v[28:31], 0\n\t"
    "ds_read_b128 v[228:231], v246 offset:128\n\t"
    "v_mfma_f32_16x16x32_bf16 v[92:95], v[128:131], v[28:31], 0\n\t"
    "ds_read_b128 v[124:127], v250 offset:7072\n\t"
    "ds_read_b128 v[132:135], v244 offset:672\n\t"
    "s_waitcnt lgkmcnt(12)\n\t"
    "v_mfma_f32_16x16x32_bf16 v[64:67], v[16:19], v[12:15], 0\n\t"
    "ds_read_b128 v[224:227], v245 offset:672\n\t"
    "s_waitcnt lgkmcnt(12)\n\t"
    "v_mfma_f32_16x16x32_bf16 v[52:55], v[16:19], v[0:3], 0\n\t"
    "ds_read_b128 v[220:223], v246 offset:672\n\t"
    "s_waitcnt lgkmcnt(12)\n\t"
    "v_mfma_f32_16x16x32_bf16 v[12:15], v[16:19], v[4:7], 0\n\t"
    "ds_read_b128 v[0:3], v250 offset:7616\n\t"
    "ds_read_b128 v[216:219], v247 offset:0\n\t"
    "s_waitcnt lgkmcnt(12)\n\t"
    "v_mfma_f32_16x16x32_bf16 v[64:67], v[20:23], v[240:243], v[64:67]\n\t"
    "ds_read_b128 v[4:7], v248 offset:0\n\t"
    "s_waitcnt lgkmcnt(12)\n\t"
    "v_mfma_f32_16x16x32_bf16 v[52:55], v[20:23], v[24:27], v[52:55]\n\t"
    "ds_read_b128 v[240:243], v249 offset:0\n\t"
    "s_waitcnt lgkmcnt(12)\n\t"
    "v_mfma_f32_16x16x32_bf16 v[12:15], v[20:23], v[68:71], v[12:15]\n\t"
    "ds_read_b128 v[212:215], v250 offset:8160\n\t"
    "ds_read_b128 v[16:19], v244 offset:32\n\t"
    "s_waitcnt lgkmcnt(12)\n\t"
    "v_mfma_f32_16x16x32_bf16 v[64:67], v[84:87], v[116:119], v[64:67]\n\t"
    "ds_read_b128 v[68:71], v245 offset:32\n\t"
    "s_waitcnt lgkmcnt(12)\n\t"
    "v_mfma_f32_16x16x32_bf16 v[52:55], v[84:87], v[120:123], v[52:55]\n\t"
    "ds_read_b128 v[116:119], v246 offset:32\n\t"
    "s_waitcnt lgkmcnt(12)\n\t"
    "v_mfma_f32_16x16x32_bf16 v[12:15], v[84:87], v[228:231], v[12:15]\n\t"
    "ds_read_b128 v[120:123], v250 offset:8704\n\t"
    "ds_read_b128 v[144:147], v244 offset:96\n\t"
    "s_waitcnt lgkmcnt(12)\n\t"
    "v_mfma_f32_16x16x32_bf16 v[64:67], v[124:127], v[132:135], v[64:67]\n\t"
    "ds_read_b128 v[148:151], v245 offset:96\n\t"
    "s_waitcnt lgkmcnt(12)\n\t"
    "v_mfma_f32_16x16x32_bf16 v[52:55], v[124:127], v[224:227], v[52:55]\n\t"
    "ds_read_b128 v[156:159], v246 offset:96\n\t"
    "s_waitcnt lgkmcnt(12)\n\t"
    "v_mfma_f32_16x16x32_bf16 v[12:15], v[124:127], v[220:223], v[12:15]\n\t"
    "ds_read_b128 v[124:127], v250 offset:9248\n\t"
    "ds_read_b128 v[160:163], v244 offset:160\n\t"
    "s_waitcnt lgkmcnt(12)\n\t"
    "v_mfma_f32_16x16x32_bf16 v[64:67], v[0:3], v[216:219], v[64:67]\n\t"
    "ds_read_b128 v[228:231], v245 offset:160\n\t"
    "s_waitcnt lgkmcnt(12)\n\t"
    "v_mfma_f32_16x16x32_bf16 v[52:55], v[0:3], v[4:7], v[52:55]\n\t"
    "ds_read_b128 v[224:227], v246 offset:160\n\t"
    "s_waitcnt lgkmcnt(12)\n\t"
    "v_mfma_f32_16x16x32_bf16 v[12:15], v[0:3], v[240:243], v[12:15]\n\t"
    "ds_read_b128 v[0:3], v250 offset:9792\n\t"
    "ds_read_b128 v[4:7], v244 offset:704\n\t"
    "v_cvt_pk_bf16_f32 v24, v64, v65\n\t"
    "s_waitcnt lgkmcnt(12)\n\t"
    "v_mfma_f32_16x16x32_bf16 v[84:87], v[212:215], v[16:19], 0\n\t"
    "ds_read_b128 v[164:167], v245 offset:704\n\t"
    "v_cvt_pk_bf16_f32 v25, v66, v67\n\t"
    "v_cvt_pk_bf16_f32 v20, v52, v53\n\t"
    "s_waitcnt lgkmcnt(12)\n\t"
    "v_mfma_f32_16x16x32_bf16 v[136:139], v[212:215], v[68:71], 0\n\t"
    "ds_read_b128 v[240:243], v246 offset:704\n\t"
    "v_cvt_pk_bf16_f32 v21, v54, v55\n\t"
    "v_cvt_pk_bf16_f32 v16, v12, v13\n\t"
    "s_waitcnt lgkmcnt(12)\n\t"
    "v_mfma_f32_16x16x32_bf16 v[132:135], v[212:215], v[116:119], 0\n\t"
    "ds_read_b128 v[68:71], v250 offset:10336\n\t"
    "ds_read_b128 v[220:223], v247 offset:32\n\t"
    "v_cvt_pk_bf16_f32 v17, v14, v15\n\t"
    "s_waitcnt lgkmcnt(12)\n\t"
    "v_mfma_f32_16x16x32_bf16 v[84:87], v[120:123], v[144:147], v[84:87]\n\t"
    "ds_read_b128 v[116:119], v248 offset:32\n\t"
    "s_waitcnt lgkmcnt(12)\n\t"
    "v_mfma_f32_16x16x32_bf16 v[136:139], v[120:123], v[148:151], v[136:139]\n\t"
    "ds_read_b128 v[216:219], v249 offset:32\n\t"
    "s_waitcnt vmcnt(5)\n\t"
    "ds_write_b128 v8, v[76:79] offset:0\n\t"
    "s_waitcnt vmcnt(4)\n\t"
    "ds_write_b128 v8, v[236:239] offset:1024\n\t"
    "s_waitcnt lgkmcnt(14)\n\t"
    "v_mfma_f32_16x16x32_bf16 v[132:135], v[120:123], v[156:159], v[132:135]\n\t"
    "s_waitcnt vmcnt(3)\n\t"
    "ds_write_b128 v8, v[232:235] offset:2048\n\t"
    "s_waitcnt vmcnt(2)\n\t"
    "ds_write_b128 v8, v[104:107] offset:3072\n\t"
    "s_waitcnt vmcnt(1)\n\t"
    "ds_write_b128 v8, v[108:111] offset:4096\n\t"
    "s_waitcnt lgkmcnt(15)\n\t"
    "v_mfma_f32_16x16x32_bf16 v[84:87], v[124:127], v[160:163], v[84:87]\n\t"
    "s_waitcnt vmcnt(0)\n\t"
    "s_mov_b64 exec, 0xfffff\n\t"
    "ds_write_b128 v8, v[112:115] offset:5120\n\t"
    "s_mov_b64 exec, -1\n\t"
    "s_waitcnt lgkmcnt(15)\n\t"
    "v_mfma_f32_16x16x32_bf16 v[136:139], v[124:127], v[228:231], v[136:139]\n\t"
    "s_waitcnt lgkmcnt(14)\n\t"
    "v_mfma_f32_16x16x32_bf16 v[132:135], v[124:127], v[224:227], v[132:135]\n\t"
    "s_waitcnt lgkmcnt(12)\n\t"
    "v_mfma_f32_16x16x32_bf16 v[84:87], v[0:3], v[4:7], v[84:87]\n\t"
    "s_waitcnt lgkmcnt(11)\n\t"
    "v_mfma_f32_16x16x32_bf16 v[136:139], v[0:3], v[164:167], v[136:139]\n\t"
    "s_waitcnt lgkmcnt(10)\n\t"
    "v_mfma_f32_16x16x32_bf16 v[132:135], v[0:3], v[240:243], v[132:135]\n\t"
    "ds_read_b128 v[240:243], v250 offset:10880\n\t"
    "ds_read_b128 v[0:3], v244 offset:0\n\t"
    "s_waitcnt lgkmcnt(10)\n\t"
    "v_mfma_f32_16x16x32_bf16 v[84:87], v[68:71], v[220:223], v[84:87]\n\t"
    "ds_read_b128 v[4:7], v245 offset:0\n\t"
    "s_waitcnt lgkmcnt(10)\n\t"
    "v_mfma_f32_16x16x32_bf16 v[136:139], v[68:71], v[116:119], v[136:139]\n\t"
    "ds_read_b128 v[8:11], v246 offset:0\n\t"
    "s_waitcnt lgkmcnt(10)\n\t"
    "v_mfma_f32_16x16x32_bf16 v[132:135], v[68:71], v[216:219], v[132:135]\n\t"
    "ds_read_b128 v[68:71], v250 offset:11424\n\t"
    "ds_read_b128 v[76:79], v244 offset:64\n\t"
    "v_cvt_pk_bf16_f32 v26, v84, v85\n\t"
    "v_cvt_pk_bf16_f32 v27, v86, v87\n\t"
    "s_nop 0\n\t"
    "v_cvt_pk_bf16_f32 v22, v136, v137\n\t"
    "v_cvt_pk_bf16_f32 v23, v138, v139\n\t"
    "v_mfma_f32_16x16x32_bf16 v[88:91], v[24:27], v[24:27], v[88:91]\n\t"
    "ds_read_b128 v[104:107], v245 offset:64\n\t"
    "v_cvt_pk_bf16_f32 v18, v132, v133\n\t"
    "v_cvt_pk_bf16_f32 v19, v134, v135\n\t"
    "v_mfma_f32_16x16x32_bf16 v[152:155], v[128:131], v[24:27], v[152:155]\n\t"
    "ds_read_b128 v[108:111], v246 offset:64\n\t"
    "v_mfma_f32_16x16x32_bf16 v[140:143], v[20:23], v[20:23], v[140:143]\n\t"
    "ds_read_b128 v[112:115], v250 offset:11968\n\t"
    "ds_read_b128 v[236:239], v244 offset:128\n\t"
    "v_mfma_f32_16x16x32_bf16 v[100:103], v[128:131], v[20:23], v[100:103]\n\t"
    "ds_read_b128 v[232:235], v245 offset:128\n\t"
    "v_mfma_f32_16x16x32_bf16 v[96:99], v[16:19], v[16:19], v[96:99]\n\t"
    "ds_read_b128 v[116:119], v246 offset:128\n\t"
    "v_mfma_f32_16x16x32_bf16 v[92:95], v[128:131], v[16:19], v[92:95]\n\t"
    "ds_read_b128 v[120:123], v250 offset:12512\n\t"
    "ds_read_b128 v[124:127], v244 offset:672\n\t"
    "s_waitcnt lgkmcnt(12)\n\t"
    "v_mfma_f32_16x16x32_bf16 v[160:163], v[240:243], v[0:3], 0\n\t"
    "ds_read_b128 v[0:3], v245 offset:672\n\t"
    "s_waitcnt lgkmcnt(12)\n\t"
    "v_mfma_f32_16x16x32_bf16 v[156:159], v[240:243], v[4:7], 0\n\t"
    "ds_read_b128 v[4:7], v246 offset:672\n\t"
    "s_waitcnt lgkmcnt(12)\n\t"
    "v_mfma_f32_16x16x32_bf16 v[148:151], v[240:243], v[8:11], 0\n\t"
    "ds_read_b128 v[8:11], v250 offset:13056\n\t"
    "ds_read_b128 v[144:147], v247 offset:0\n\t"
    "s_waitcnt lgkmcnt(12)\n\t"
    "v_mfma_f32_16x16x32_bf16 v[160:163], v[68:71], v[76:79], v[160:163]\n\t"
    "ds_read_b128 v[240:243], v248 offset:0\n\t"
    "s_waitcnt lgkmcnt(12)\n\t"
    "v_mfma_f32_16x16x32_bf16 v[156:159], v[68:71], v[104:107], v[156:159]\n\t"
    "ds_read_b128 v[76:79], v249 offset:0\n\t"
    "s_waitcnt lgkmcnt(12)\n\t"
    "v_mfma_f32_16x16x32_bf16 v[148:151], v[68:71], v[108:111], v[148:151]\n\t"
    "ds_read_b128 v[68:71], v250 offset:13600\n\t"
    "ds_read_b128 v[104:107], v244 offset:32\n\t"
    "s_waitcnt lgkmcnt(12)\n\t"
    "v_mfma_f32_16x16x32_bf16 v[160:163], v[112:115], v[236:239], v[160:163]\n\t"
    "ds_read_b128 v[108:111], v245 offset:32\n\t"
    "s_waitcnt lgkmcnt(12)\n\t"
    "v_mfma_f32_16x16x32_bf16 v[156:159], v[112:115], v[232:235], v[156:159]\n\t"
    "ds_read_b128 v[176:179], v246 offset:32\n\t"
    "s_waitcnt lgkmcnt(12)\n\t"
    "v_mfma_f32_16x16x32_bf16 v[148:151], v[112:115], v[116:119], v[148:151]\n\t"
    "ds_read_b128 v[112:115], v250 offset:14144\n\t"
    "ds_read_b128 v[116:119], v244 offset:96\n\t"
    "s_waitcnt lgkmcnt(12)\n\t"
    "v_mfma_f32_16x16x32_bf16 v[160:163], v[120:123], v[124:127], v[160:163]\n\t"
    "ds_read_b128 v[124:127], v245 offset:96\n\t"
    "s_waitcnt lgkmcnt(12)\n\t"
    "v_mfma_f32_16x16x32_bf16 v[156:159], v[120:123], v[0:3], v[156:159]\n\t"
    "ds_read_b128 v[236:239], v246 offset:96\n\t"
    "s_waitcnt lgkmcnt(12)\n\t"
    "v_mfma_f32_16x16x32_bf16 v[148:151], v[120:123], v[4:7], v[148:151]\n\t"
    "ds_read_b128 v[120:123], v250 offset:14688\n\t"
    "ds_read_b128 v[180:183], v244 offset:160\n\t"
    "s_waitcnt lgkmcnt(12)\n\t"
    "v_mfma_f32_16x16x32_bf16 v[160:163], v[8:11], v[144:147], v[160:163]\n\t"
    "ds_read_b128 v[232:235], v245 offset:160\n\t"
    "s_waitcnt lgkmcnt(12)\n\t"
    "v_mfma_f32_16x16x32_bf16 v[156:159], v[8:11], v[240:243], v[156:159]\n\t"
    "ds_read_b128 v[240:243], v246 offset:160\n\t"
    "s_waitcnt lgkmcnt(12)\n\t"
    "v_mfma_f32_16x16x32_bf16 v[148:151], v[8:11], v[76:79], v[148:151]\n\t"
    "ds_read_b128 v[228:231], v250 offset:15232\n\t"
    "ds_read_b128 v[76:79], v244 offset:704\n\t"
    "v_cvt_pk_bf16_f32 v8, v160, v161\n\t"
    "v_cvt_pk_bf16_f32 v9, v162, v163\n\t"
    "s_waitcnt lgkmcnt(12)\n\t"
    "v_mfma_f32_16x16x32_bf16 v[172:175], v[68:71], v[104:107], 0\n\t"
    "ds_read_b128 v[224:227], v245 offset:704\n\t"
    "v_cvt_pk_bf16_f32 v4, v156, v157\n\t"
    "v_cvt_pk_bf16_f32 v5, v158, v159\n\t"
    "s_waitcnt lgkmcnt(12)\n\t"
    "v_mfma_f32_16x16x32_bf16 v[168:171], v[68:71], v[108:111], 0\n\t"
    "ds_read_b128 v[104:107], v252 offset:18432\n\t"
    "ds_read_b128 v[220:223], v252 offset:19456\n\t"
    "ds_read_b128 v[108:111], v246 offset:704\n\t"
    "s_waitcnt lgkmcnt(14)\n\t"
    "v_mfma_f32_16x16x32_bf16 v[164:167], v[68:71], v[176:179], 0\n\t"
    "ds_read_b128 v[68:71], v250 offset:15776\n\t"
    "ds_read_b128 v[144:147], v247 offset:32\n\t"
    "v_cvt_pk_bf16_f32 v0, v148, v149\n\t"
    "v_cvt_pk_bf16_f32 v1, v150, v151\n\t"
    "s_waitcnt lgkmcnt(14)\n\t"
    "v_mfma_f32_16x16x32_bf16 v[172:175], v[112:115], v[116:119], v[172:175]\n\t"
    "ds_read_b128 v[116:119], v248 offset:32\n\t"
    "s_waitcnt lgkmcnt(14)\n\t"
    "v_mfma_f32_16x16x32_bf16 v[168:171], v[112:115], v[124:127], v[168:171]\n\t"
    "ds_read_b128 v[216:219], v249 offset:32\n\t"
    "s_branch .Lw_sp7_%=\n\t"
    ".Lw_spb7_%=:\n\t"
    "s_waitcnt lgkmcnt(14)\n\t"
    "v_mfma_f32_16x16x32_bf16 v[164:167], v[112:115], v[236:239], v[164:167]\n\t"
    "s_waitcnt lgkmcnt(12)\n\t"
    "v_mfma_f32_16x16x32_bf16 v[172:175], v[120:123], v[180:183], v[172:175]\n\t"
    "s_waitcnt lgkmcnt(11)\n\t"
    "v_mfma_f32_16x16x32_bf16 v[168:171], v[120:123], v[232:235], v[168:171]\n\t"
    "s_waitcnt lgkmcnt(10)\n\t"
    "v_mfma_f32_16x16x32_bf16 v[164:167], v[120:123], v[240:243], v[164:167]\n\t"
    "ds_read_b128 v[112:115], v251 offset:0\n\t"
    "s_waitcnt lgkmcnt(9)\n\t"
    "v_mfma_f32_16x16x32_bf16 v[172:175], v[228:231], v[76:79], v[172:175]\n\t"
    "s_waitcnt lgkmcnt(8)\n\t"
    "v_mfma_f32_16x16x32_bf16 v[168:171], v[228:231], v[224:227], v[168:171]\n\t"
    "s_waitcnt lgkmcnt(5)\n\t"
    "v_mfma_f32_16x16x32_bf16 v[164:167], v[228:231], v[108:111], v[164:167]\n\t"
    "ds_read_b128 v[240:243], v251 offset:1024\n\t"
    "s_waitcnt lgkmcnt(4)\n\t"
    "v_mfma_f32_16x16x32_bf16 v[172:175], v[68:71], v[144:147], v[172:175]\n\t"
    "s_waitcnt lgkmcnt(3)\n\t"
    "v_mfma_f32_16x16x32_bf16 v[168:171], v[68:71], v[116:119], v[168:171]\n\t"
    "s_waitcnt lgkmcnt(2)\n\t"
    "s_cmp_lg_u32 s49, 0\n\t"
    "s_cbranch_scc1 .Lw_stub6_%=\n\t"
    ".Lw_back6_%=:\n\t"
    "v_mfma_f32_16x16x32_bf16 v[164:167], v[68:71], v[216:219], v[164:167]\n\t"
    "s_mov_b32 m0, s25\n\t"
    "s_add_u32 s60, s46, 0\n\t"
    "buffer_load_dwordx4 v104, s[36:39], s60 offen lds\n\t"
    "buffer_load_dwordx4 v105, s[36:39], s60 offen offset:1024 lds\n\t"
    "v_cvt_pk_bf16_f32 v10, v172, v173\n\t"
    "buffer_load_dwordx4 v106, s[36:39], s60 offen offset:2048 lds\n\t"
    "v_cvt_pk_bf16_f32 v11, v174, v175\n\t"
    "buffer_load_dwordx4 v107, s[36:39], s60 offen offset:3072 lds\n\t"
    "v_cvt_pk_bf16_f32 v6, v168, v169\n\t"
    "v_mfma_f32_16x16x32_bf16 v[88:91], v[8:11], v[8:11], v[88:91]\n\t"
    "s_add_u32 m0, s25, 4096\n\t"
    "s_nop 0\n\t"
    "buffer_load_dwordx4 v220, s[36:39], s60 offen lds\n\t"
    "v_mfma_f32_16x16x32_bf16 v[152:155], v[128:131], v[8:11], v[152:155]\n\t"
    "ds_read_b128 v[104:107], v251 offset:2048\n\t"
    "v_cvt_pk_bf16_f32 v7, v170, v171\n\t"
    "v_cvt_pk_bf16_f32 v2, v164, v165\n\t"
    "v_cvt_pk_bf16_f32 v3, v166, v167\n\t"
    "v_mfma_f32_16x16x32_bf16 v[140:143], v[4:7], v[4:7], v[140:143]\n\t"
    "s_mov_b64 exec, 0xfffff\n\t"
    "buffer_load_dwordx4 v221, s[36:39], s60 offen offset:1024 lds\n\t"
    "s_mov_b64 exec, -1\n\t"
    "v_mfma_f32_16x16x32_bf16 v[100:103], v[128:131], v[4:7], v[100:103]\n\t"
    "s_mov_b32 m0, 160768\n\t"
    "s_nop 0\n\t"
    "buffer_load_dword v223, s[36:39], s60 offen lds\n\t"
    "v_mfma_f32_16x16x32_bf16 v[96:99], v[0:3], v[0:3], v[96:99]\n\t"
    "ds_read_b128 v[116:119], v251 offset:3072\n\t"
    "v_cndmask_b32_e64 v90, v91, v90, s[80:81]\n\t"
    "v_cndmask_b32_e64 v91, v143, v142, s[80:81]\n\t"
    "v_mfma_f32_16x16x32_bf16 v[92:95], v[128:131], v[0:3], v[92:95]\n\t"
    "buffer_load_dword v72, s[36:39], s60 offen lds\n\t"
    "v_cndmask_b32_e64 v90, v90, v89, s[78:79]\n\t"
    "v_cndmask_b32_e64 v91, v91, v141, s[78:79]\n\t"
    "v_cndmask_b32_e64 v90, v90, v88, s[76:77]\n\t"
    "v_cndmask_b32_e64 v91, v91, v140, s[76:77]\n\t"
    "ds_bpermute_b32 v88, v253, v90\n\t"
    "ds_bpermute_b32 v89, v253, v91\n\t"
    "v_cndmask_b32_e64 v90, v99, v98, s[80:81]\n\t"
    "v_mul_f32 v91, 0x3c2aaaab, v152\n\t"
    "v_cndmask_b32_e64 v90, v90, v97, s[78:79]\n\t"
    "v_mul_f32_e64 v93, -v91, v91\n\t"
    "v_cndmask_b32_e64 v90, v90, v96, s[76:77]\n\t"
    "v_mul_f32 v94, 0x3c2aaaab, v100\n\t"
    "ds_bpermute_b32 v95, v253, v90\n\t"
    "v_mul_f32_e64 v90, -v94, v94\n\t"
    "v_mul_f32 v92, 0x3c2aaaab, v92\n\t"
    "ds_read_b128 v[236:239], v251 offset:4096\n\t"
    "v_mul_f32_e64 v129, -v92, v92\n\t"
    "s_waitcnt lgkmcnt(3)\n\t"
    "v_fmamk_f32 v88, v88, 0x3c2aaaab, v93\n\t"
    "s_waitcnt lgkmcnt(2)\n\t"
    "v_fmamk_f32 v93, v89, 0x3c2aaaab, v90\n\t"
    "v_max_f32 v88, 0, v88\n\t"
    "v_max_f32 v93, 0, v93\n\t"
    "v_add_f32 v88, 0x3727c5ac, v88\n\t"
    "v_add_f32 v93, 0x3727c5ac, v93\n\t"
    "v_rsq_f32 v130, v88\n\t"
    "s_waitcnt lgkmcnt(1)\n\t"
    "v_fmamk_f32 v95, v95, 0x3c2aaaab, v129\n\t"
    "v_mul_f32 v129, 0x3f0aec44, v130\n\t"
    "v_rsq_f32 v93, v93\n\t"
    "v_max_f32 v95, 0, v95\n\t"
    "v_mul_f32_e64 v130, -v91, v129\n\t"
    "v_add_f32 v95, 0x3727c5ac, v95\n\t"
    "v_fma_f32 v80, v80, v129, v130\n\t"
    "v_fma_f32 v81, v81, v129, v130\n\t"
    "v_fma_f32 v82, v82, v129, v130\n\t"
    "v_fma_f32 v83, v83, v129, v130\n\t"
    "v_fma_f32 v48, v48, v129, v130\n\t"
    "v_fma_f32 v49, v49, v129, v130\n\t"
    "v_fma_f32 v50, v50, v129, v130\n\t"
    "v_fma_f32 v51, v51, v129, v130\n\t"
    "v_mul_f32 v131, 0x3f0aec44, v93\n\t"
    "v_rsq_f32 v93, v95\n\t"
    "v_cvt_pk_bf16_f32 v100, v80, v81\n\t"
    "v_cvt_pk_bf16_f32 v101, v82, v83\n\t"
    "v_cvt_pk_bf16_f32 v102, v48, v49\n\t"
    "v_cvt_pk_bf16_f32 v103, v50, v51\n\t"
    "v_mul_f32_e64 v68, -v94, v131\n\t"
    "v_mul_f32 v69, 0x3f0aec44, v93\n\t"
    "v_mfma_f32_16x16x32_bf16 v[48:51], v[112:115], v[100:103], 0\n\t"
    "v_fma_f32 v60, v60, v131, v68\n\t"
    "v_fma_f32 v61, v61, v131, v68\n\t"
    "v_fma_f32 v62, v62, v131, v68\n\t"
    "v_fma_f32 v63, v63, v131, v68\n\t"
    "v_fma_f32 v44, v44, v131, v68\n\t"
    "v_fma_f32 v45, v45, v131, v68\n\t"
    "v_fma_f32 v46, v46, v131, v68\n\t"
    "v_fma_f32 v47, v47, v131, v68\n\t"
    "v_cvt_pk_bf16_f32 v96, v60, v61\n\t"
    "v_cvt_pk_bf16_f32 v97, v62, v63\n\t"
    "v_cvt_pk_bf16_f32 v98, v44, v45\n\t"
    "v_cvt_pk_bf16_f32 v99, v46, v47\n\t"
    "v_mul_f32_e64 v70, -v92, v69\n\t"
    "v_fma_f32 v64, v64, v129, v130\n\t"
    "v_mfma_f32_16x16x32_bf16 v[60:63], v[112:115], v[96:99], 0\n\t"
    "v_fma_f32 v56, v56, v69, v70\n\t"
    "v_fma_f32 v57, v57, v69, v70\n\t"
    "v_fma_f32 v58, v58, v69, v70\n\t"
    "v_fma_f32 v59, v59, v69, v70\n\t"
    "v_fma_f32 v40, v40, v69, v70\n\t"
    "v_fma_f32 v41, v41, v69, v70\n\t"
    "v_fma_f32 v42, v42, v69, v70\n\t"
    "v_fma_f32 v43, v43, v69, v70\n\t"
    "v_cvt_pk_bf16_f32 v92, v56, v57\n\t"
    "v_cvt_pk_bf16_f32 v93, v58, v59\n\t"
    "v_cvt_pk_bf16_f32 v94, v40, v41\n\t"
    "v_cvt_pk_bf16_f32 v95, v42, v43\n\t"
    "v_fma_f32 v65, v65, v129, v130\n\t"
    "v_fma_f32 v66, v66, v129, v130\n\t"
    "v_mfma_f32_16x16x32_bf16 v[56:59], v[112:115], v[92:95], 0\n\t"
    "ds_read_b128 v[112:115], v251 offset:5120\n\t"
    "v_fma_f32 v67, v67, v129, v130\n\t"
    "v_fma_f32 v84, v84, v129, v130\n\t"
    "v_fma_f32 v85, v85, v129, v130\n\t"
    "v_fma_f32 v86, v86, v129, v130\n\t"
    "v_fma_f32 v87, v87, v129, v130\n\t"
    "v_cvt_pk_bf16_f32 v88, v64, v65\n\t"
    "v_cvt_pk_bf16_f32 v89, v66, v67\n\t"
    "v_cvt_pk_bf16_f32 v90, v84, v85\n\t"
    "v_cvt_pk_bf16_f32 v91, v86, v87\n\t"
    "v_fma_f32 v52, v52, v131, v68\n\t"
    "v_fma_f32 v53, v53, v131, v68\n\t"
    "v_mfma_f32_16x16x32_bf16 v[48:51], v[240:243], v[88:91], v[48:51]\n\t"
    "v_fma_f32 v54, v54, v131, v68\n\t"
    "v_fma_f32 v55, v55, v131, v68\n\t"
    "v_fma_f32 v136, v136, v131, v68\n\t"
    "v_fma_f32 v137, v137, v131, v68\n\t"
    "v_fma_f32 v138, v138, v131, v68\n\t"
    "v_fma_f32 v139, v139, v131, v68\n\t"
    "v_cvt_pk_bf16_f32 v84, v52, v53\n\t"
    "v_cvt_pk_bf16_f32 v85, v54, v55\n\t"
    "v_cvt_pk_bf16_f32 v86, v136, v137\n\t"
    "v_cvt_pk_bf16_f32 v87, v138, v139\n\t"
    "v_fma_f32 v12, v12, v69, v70\n\t"
    "v_fma_f32 v13, v13, v69, v70\n\t"
    "v_mfma_f32_16x16x32_bf16 v[60:63], v[240:243], v[84:87], v[60:63]\n\t"
    "v_fma_f32 v14, v14, v69, v70\n\t"
    "v_fma_f32 v15, v15, v69, v70\n\t"
    "v_fma_f32 v132, v132, v69, v70\n\t"
    "v_fma_f32 v133, v133, v69, v70\n\t"
    "v_fma_f32 v134, v134, v69, v70\n\t"
    "v_fma_f32 v135, v135, v69, v70\n\t"
    "v_cvt_pk_bf16_f32 v80, v12, v13\n\t"
    "v_cvt_pk_bf16_f32 v81, v14, v15\n\t"
    "v_cvt_pk_bf16_f32 v82, v132, v133\n\t"
    "v_cvt_pk_bf16_f32 v83, v134, v135\n\t"
    "v_fma_f32 v160, v160, v129, v130\n\t"
    "v_fma_f32 v161, v161, v129, v130\n\t"
    "v_mfma_f32_16x16x32_bf16 v[56:59], v[240:243], v[80:83], v[56:59]\n\t"
    "ds_read_b128 v[120:123], v251 offset:6144\n\t"
    "v_fma_f32 v162, v162, v129, v130\n\t"
    "v_fma_f32 v163, v163, v129, v130\n\t"
    "v_fma_f32 v172, v172, v129, v130\n\t"
    "v_fma_f32 v173, v173, v129, v130\n\t"
    "v_fma_f32 v174, v174, v129, v130\n\t"
    "v_fma_f32 v175, v175, v129, v130\n\t"
    "v_cvt_pk_bf16_f32 v76, v160, v161\n\t"
    "v_cvt_pk_bf16_f32 v77, v162, v163\n\t"
    "v_cvt_pk_bf16_f32 v78, v172, v173\n\t"
    "v_cvt_pk_bf16_f32 v79, v174, v175\n\t"
    "v_fma_f32 v156, v156, v131, v68\n\t"
    "v_fma_f32 v157, v157, v131, v68\n\t"
    "v_mfma_f32_16x16x32_bf16 v[48:51], v[104:107], v[76:79], v[48:51]\n\t"
    "v_fma_f32 v158, v158, v131, v68\n\t"
    "v_fma_f32 v159, v159, v131, v68\n\t"
    "v_fma_f32 v168, v168, v131, v68\n\t"
    "v_fma_f32 v169, v169, v131, v68\n\t"
    "v_fma_f32 v170, v170, v131, v68\n\t"
    "v_fma_f32 v171, v171, v131, v68\n\t"
    "v_cvt_pk_bf16_f32 v72, v156, v157\n\t"
    "v_cvt_pk_bf16_f32 v73, v158, v159\n\t"
    "v_cvt_pk_bf16_f32 v74, v168, v169\n\t"
    "v_cvt_pk_bf16_f32 v75, v170, v171\n\t"
    "v_fma_f32 v148, v148, v69, v70\n\t"
    "v_fma_f32 v149, v149, v69, v70\n\t"
    "v_mfma_f32_16x16x32_bf16 v[60:63], v[104:107], v[72:75], v[60:63]\n\t"
    "v_fma_f32 v150, v150, v69, v70\n\t"
    "v_fma_f32 v151, v151, v69, v70\n\t"
    "v_fma_f32 v164, v164, v69, v70\n\t"
    "v_fma_f32 v165, v165, v69, v70\n\t"
    "v_fma_f32 v166, v166, v69, v70\n\t"
    "v_fma_f32 v167, v167, v69, v70\n\t"
    "v_cvt_pk_bf16_f32 v68, v148, v149\n\t"
    "v_cvt_pk_bf16_f32 v69, v150, v151\n\t"
    "v_cvt_pk_bf16_f32 v70, v164, v165\n\t"
    "v_cvt_pk_bf16_f32 v71, v166, v167\n\t"
    "v_cvt_pk_bf16_f32 v44, v48, v49\n\t"
    "v_cvt_pk_bf16_f32 v45, v50, v51\n\t"
    "v_mfma_f32_16x16x32_bf16 v[56:59], v[104:107], v[68:71], v[56:59]\n\t"
    "ds_read_b128 v[124:127], v251 offset:7168\n\t"
    "v_cvt_pk_bf16_f32 v48, v60, v61\n\t"
    "v_cvt_pk_bf16_f32 v49, v62, v63\n\t"
    "v_mfma_f32_16x16x32_bf16 v[108:111], v[116:119], v[100:103], 0\n\t"
    "v_mov_b32 v47, 0\n\t"
    "v_mov_b32 v51, 0\n\t"
    "v_mfma_f32_16x16x32_bf16 v[104:107], v[116:119], v[96:99], 0\n\t"
    "v_mov_b32 v43, 0\n\t"
    "v_cvt_pk_bf16_f32 v40, v56, v57\n\t"
    "v_mfma_f32_16x16x32_bf16 v[12:15], v[116:119], v[92:95], 0\n\t"
    "ds_read_b128 v[240:243], v251 offset:8192\n\t"
    "v_cvt_pk_bf16_f32 v41, v58, v59\n\t"
    "v_mov_b32 v59, 0\n\t"
    "s_waitcnt lgkmcnt(4)\n\t"
    "v_mfma_f32_16x16x32_bf16 v[108:111], v[236:239], v[88:91], v[108:111]\n\t"
    "v_mov_b32 v63, 0\n\t"
    "v_mov_b32 v55, 0\n\t"
    "v_mfma_f32_16x16x32_bf16 v[104:107], v[236:239], v[84:87], v[104:107]\n\t"
    "v_mov_b32 v67, 0\n\t"
    "v_mov_b32 v135, 0\n\t"
    "v_mfma_f32_16x16x32_bf16 v[12:15], v[236:239], v[80:83], v[12:15]\n\t"
    "ds_read_b128 v[144:147], v251 offset:9216\n\t"
    "v_mov_b32 v143, 0\n\t"
    "v_mov_b32 v139, 0\n\t"
    "s_waitcnt lgkmcnt(4)\n\t"
    "v_mfma_f32_16x16x32_bf16 v[108:111], v[112:115], v[76:79], v[108:111]\n\t"
    "v_mfma_f32_16x16x32_bf16 v[104:107], v[112:115], v[72:75], v[104:107]\n\t"
    "v_mfma_f32_16x16x32_bf16 v[12:15], v[112:115], v[68:71], v[12:15]\n\t"
    "ds_read_b128 v[148:151], v251 offset:10240\n\t"
    "s_waitcnt lgkmcnt(4)\n\t"
    "v_mfma_f32_16x16x32_bf16 v[112:115], v[120:123], v[100:103], 0\n\t"
    "s_nop 2\n\t"
    "v_cvt_pk_bf16_f32 v46, v110, v111\n\t"
    "v_mfma_f32_16x16x32_bf16 v[116:119], v[120:123], v[96:99], 0\n\t"
    "v_cvt_pk_bf16_f32 v58, v108, v109\n\t"
    "v_cvt_pk_bf16_f32 v50, v106, v107\n\t"
    "v_mfma_f32_16x16x32_bf16 v[108:111], v[120:123], v[92:95], 0\n\t"
    "ds_read_b128 v[152:155], v251 offset:11264\n\t"
    "v_cvt_pk_bf16_f32 v62, v104, v105\n\t"
    "v_cvt_pk_bf16_f32 v42, v14, v15\n\t"
    "s_waitcnt lgkmcnt(4)\n\t"
    "v_mfma_f32_16x16x32_bf16 v[112:115], v[124:127], v[88:91], v[112:115]\n\t"
    "v_cvt_pk_bf16_f32 v54, v12, v13\n\t"
    "v_mfma_f32_16x16x32_bf16 v[116:119], v[124:127], v[84:87], v[116:119]\n\t"
    "v_mfma_f32_16x16x32_bf16 v[108:111], v[124:127], v[80:83], v[108:111]\n\t"
    "ds_read_b128 v[124:127], v251 offset:49152\n\t"
    "s_waitcnt lgkmcnt(4)\n\t"
    "v_mfma_f32_16x16x32_bf16 v[112:115], v[240:243], v[76:79], v[112:115]\n\t"
    "v_mfma_f32_16x16x32_bf16 v[116:119], v[240:243], v[72:75], v[116:119]\n\t"
    "v_mfma_f32_16x16x32_bf16 v[108:111], v[240:243], v[68:71], v[108:111]\n\t"
    "ds_read_b128 v[156:159], v251 offset:50176\n\t"
    "s_waitcnt lgkmcnt(4)\n\t"
    "v_mfma_f32_16x16x32_bf16 v[12:15], v[100:103], v[144:147], 0\n\t"
    "s_nop 2\n\t"
    "v_cvt_pk_bf16_f32 v56, v112, v113\n\t"
    "v_mfma_f32_16x16x32_bf16 v[120:123], v[96:99], v[144:147], 0\n\t"
    "v_cvt_pk_bf16_f32 v57, v114, v115\n\t"
    "v_cvt_pk_bf16_f32 v60, v116, v117\n\t"
    "v_mfma_f32_16x16x32_bf16 v[112:115], v[92:95], v[144:147], 0\n\t"
    "ds_read_b128 v[144:147], v251 offset:51200\n\t"
    "v_cvt_pk_bf16_f32 v61, v118, v119\n\t"
    "v_cvt_pk_bf16_f32 v52, v108, v109\n\t"
    "s_waitcnt lgkmcnt(4)\n\t"
    "v_mfma_f32_16x16x32_bf16 v[12:15], v[88:91], v[148:151], v[12:15]\n\t"
    "v_cvt_pk_bf16_f32 v53, v110, v111\n\t"
    "v_mfma_f32_16x16x32_bf16 v[120:123], v[84:87], v[148:151], v[120:123]\n\t"
    "v_mfma_f32_16x16x32_bf16 v[112:115], v[80:83], v[148:151], v[112:115]\n\t"
    "s_waitcnt lgkmcnt(3)\n\t"
    "v_mfma_f32_16x16x32_bf16 v[12:15], v[76:79], v[152:155], v[12:15]\n\t"
    "v_mfma_f32_16x16x32_bf16 v[120:123], v[72:75], v[152:155], v[120:123]\n\t"
    "v_mfma_f32_16x16x32_bf16 v[112:115], v[68:71], v[152:155], v[112:115]\n\t"
    "s_waitcnt lgkmcnt(2)\n\t"
    "v_mfma_f32_16x16x32_bf16 v[104:107], v[100:103], v[124:127], 0\n\t"
    "v_mfma_f32_16x16x32_bf16 v[108:111], v[96:99], v[124:127], 0\n\t"
    "s_nop 2\n\t"
    "v_cvt_pk_bf16_f32 v12, v12, v13\n\t"
    "v_cvt_pk_bf16_f32 v13, v14, v15\n\t"
    "v_mfma_f32_16x16x32_bf16 v[116:119], v[92:95], v[124:127], 0\n\t"
    "v_cvt_pk_bf16_f32 v14, v120, v121\n\t"
    "v_cvt_pk_bf16_f32 v15, v122, v123\n\t"
    "s_waitcnt lgkmcnt(1)\n\t"
    "v_mfma_f32_16x16x32_bf16 v[104:107], v[88:91], v[156:159], v[104:107]\n\t"
    "v_cvt_pk_bf16_f32 v64, v112, v113\n\t"
    "v_cvt_pk_bf16_f32 v65, v114, v115\n\t"
    "v_mfma_f32_16x16x32_bf16 v[108:111], v[84:87], v[156:159], v[108:111]\n\t"
    "v_mfma_f32_16x16x32_bf16 v[116:119], v[80:83], v[156:159], v[116:119]\n\t"
    "s_waitcnt lgkmcnt(0)\n\t"
    "v_mfma_f32_16x16x32_bf16 v[104:107], v[76:79], v[144:147], v[104:107]\n\t"
    "v_mfma_f32_16x16x32_bf16 v[108:111], v[72:75], v[144:147], v[108:111]\n\t"
    "v_mfma_f32_16x16x32_bf16 v[116:119], v[68:71], v[144:147], v[116:119]\n\t"
    "s_nop 5\n\t"
    "v_cvt_pk_bf16_f32 v172, v104, v105\n\t"
    "v_cvt_pk_bf16_f32 v173, v106, v107\n\t"
    "v_cvt_pk_bf16_f32 v146, v108, v109\n\t"
    "v_cvt_pk_bf16_f32 v147, v110, v111\n\t"
    "v_cvt_pk_bf16_f32 v130, v116, v117\n\t"
    "v_cvt_pk_bf16_f32 v131, v118, v119\n\t"
    "s_mov_b32 s44, s45\n\t"
    "s_mov_b32 s45, s46\n\t"
    "s_mov_b32 s46, s47\n\t"
    "s_mov_b32 s48, s49\n\t"
    "s_mov_b32 s49, s50\n\t"
    "s_mov_b32 s50, 16\n\t"
    ".Lw_r1_%=:\n\t"
    "s_sub_u32 s28, s28, 0x10000000\n\t"
    "s_cbranch_scc0 .Lw_drop0_%=\n\t"
    "s_setprio 0\n\t"
    ".Lw_drop0_%=:\n\t"
    "v_mfma_f32_16x16x32_bf16 v[104:107], v[44:47], v[56:59], 0\n\t"
    "s_sub_u32 s61, s53, s27\n\t"
    "s_add_u32 s66, s53, 128\n\t"
    "v_add_u32 v175, s61, v255\n\t"
    "v_mfma_f32_16x16x32_bf16 v[108:111], v[48:51], v[56:59], 0\n\t"
    "ds_read_b128 v[112:115], v251 offset:12288\n\t"
    "v_mov_b32 v66, 159808\n\t"
    "v_lshrrev_b32 v129, 3, v251\n\t"
    "v_mfma_f32_16x16x32_bf16 v[240:243], v[40:43], v[56:59], 0\n\t"
    "ds_read_b128 v[236:239], v251 offset:13312\n\t"
    "v_cndmask_b32_e64 v170, v66, v175, s[84:85]\n\t"
    "v_add_u32 v129, s66, v129\n\t"
    "v_mfma_f32_16x16x32_bf16 v[116:119], v[44:47], v[60:63], 0\n\t"
    "s_cmp_eq_u32 s54, 2\n\t"
    "s_cbranch_scc0 .Lw_spin_%=\n\t"
    ".Lw_spinback_%=:\n\t"
    "v_mfma_f32_16x16x32_bf16 v[232:235], v[48:51], v[60:63], 0\n\t"
    "ds_read_b128 v[120:123], v170 offset:64\n\t"
    "ds_read_b128 v[228:231], v175 offset:0\n\t"
    "v_cndmask_b32_e64 v169, v66, v175, s[86:87]\n\t"
    "v_mfma_f32_16x16x32_bf16 v[124:127], v[40:43], v[60:63], 0\n\t"
    "ds_read_b128 v[152:155], v169 offset:64\n\t"
    "v_cndmask_b32_e64 v168, v66, v175, s[88:89]\n\t"
    "v_cndmask_b32_e64 v174, v66, v175, s[90:91]\n\t"
    "ds_read_b128 v[224:227], v168 offset:64\n\t"
    "ds_read_b128 v[220:223], v174 offset:64\n\t"
    "v_mov_b32 v66, 159872\n\t"
    "v_exp_f32 v104, v104\n\t"
    "ds_read_b128 v[160:163], v251 offset:14336\n\t"
    "v_cndmask_b32_e64 v145, v66, v129, s[92:93]\n\t"
    "v_exp_f32 v105, v105\n\t"
    "v_exp_f32 v106, v106\n\t"
    "v_exp_f32 v107, v107\n\t"
    "v_exp_f32 v108, v108\n\t"
    "v_exp_f32 v109, v109\n\t"
    "v_exp_f32 v110, v110\n\t"
    "v_exp_f32 v111, v111\n\t"
    "v_cvt_pk_bf16_f32 v156, v104, v105\n\t"
    "v_cvt_pk_bf16_f32 v157, v106, v107\n\t"
    "v_cvt_pk_bf16_f32 v158, v108, v109\n\t"
    "v_cvt_pk_bf16_f32 v159, v110, v111\n\t"
    "v_cndmask_b32_e64 v148, v128, v172, s[82:83]\n\t"
    "v_cndmask_b32_e64 v149, v128, v173, s[82:83]\n\t"
    "v_cndmask_b32_e64 v150, v128, v146, s[82:83]\n\t"
    "v_cndmask_b32_e64 v151, v128, v147, s[82:83]\n\t"
    "v_exp_f32 v240, v240\n\t"
    "v_exp_f32 v241, v241\n\t"
    "v_exp_f32 v242, v242\n\t"
    "v_exp_f32 v243, v243\n\t"
    "v_cvt_pk_bf16_f32 v140, v240, v241\n\t"
    "s_waitcnt lgkmcnt(5)\n\t"
    "v_mfma_f32_16x16x32_bf16 v[104:107], v[120:123], v[44:47], 0\n\t"
    "v_cvt_pk_bf16_f32 v141, v242, v243\n\t"
    "v_cndmask_b32_e64 v132, v128, v130, s[82:83]\n\t"
    "s_waitcnt lgkmcnt(3)\n\t"
    "v_mfma_f32_16x16x32_bf16 v[104:107], v[152:155], v[48:51], v[104:107]\n\t"
    "v_cndmask_b32_e64 v133, v128, v131, s[82:83]\n\t"
    "v_exp_f32 v116, v116\n\t"
    "s_waitcnt lgkmcnt(2)\n\t"
    "v_mfma_f32_16x16x32_bf16 v[104:107], v[224:227], v[40:43], v[104:107]\n\t"
    "v_exp_f32 v117, v117\n\t"
    "v_mfma_f32_16x16x32_bf16 v[240:243], v[228:231], v[56:59], 0\n\t"
    "v_exp_f32 v118, v118\n\t"
    "v_mfma_f32_16x16x32_bf16 v[224:227], v[228:231], v[60:63], 0\n\t"
    "v_exp_f32 v119, v119\n\t"
    "s_waitcnt lgkmcnt(1)\n\t"
    "v_mfma_f32_16x16x32_bf16 v[104:107], v[220:223], v[228:231], v[104:107]\n\t"
    "v_exp_f32 v232, v232\n\t"
    "v_mfma_f32_16x16x32_bf16 v[108:111], v[112:115], v[100:103], 0\n\t"
    "s_nop 0\n\t"
    "v_exp_f32 v240, v240\n\t"
    "v_mfma_f32_16x16x32_bf16 v[120:123], v[112:115], v[96:99], 0\n\t"
    "v_cvt_pk_bf16_f32 v142, v240, v240\n\t"
    "v_cvt_pk_bf16_f32 v152, v116, v117\n\t"
    "v_mfma_f32_16x16x32_bf16 v[164:167], v[112:115], v[92:95], 0\n\t"
    "ds_read_u16 v66, v145 offset:0\n\t"
    "v_exp_f32 v233, v233\n\t"
    "v_mfma_f32_16x16x32_bf16 v[108:111], v[236:239], v[88:91], v[108:111]\n\t"
    "ds_read_u16 v134, v145 offset:32\n\t"
    "v_exp_f32 v234, v234\n\t"
    "v_mfma_f32_16x16x32_bf16 v[120:123], v[236:239], v[84:87], v[120:123]\n\t"
    "ds_read_b128 v[112:115], v251 offset:15360\n\t"
    "v_exp_f32 v235, v235\n\t"
    "v_mfma_f32_16x16x32_bf16 v[164:167], v[236:239], v[80:83], v[164:167]\n\t"
    "v_cvt_pk_bf16_f32 v153, v118, v119\n\t"
    "v_cvt_pk_bf16_f32 v154, v232, v233\n\t"
    "s_waitcnt lgkmcnt(3)\n\t"
    "v_mfma_f32_16x16x32_bf16 v[108:111], v[160:163], v[76:79], v[108:111]\n\t"
    "v_cvt_pk_bf16_f32 v155, v234, v235\n\t"
    "v_exp_f32 v124, v124\n\t"
    "v_mfma_f32_16x16x32_bf16 v[120:123], v[160:163], v[72:75], v[120:123]\n\t"
    "ds_read_b128 v[116:119], v251 offset:16384\n\t"
    "v_exp_f32 v125, v125\n\t"
    "v_mfma_f32_16x16x32_bf16 v[164:167], v[160:163], v[68:71], v[164:167]\n\t"
    "v_exp_f32 v126, v126\n\t"
    "v_mfma_f32_16x16x32_bf16 v[240:243], v[44:47], v[52:55], 0\n\t"
    "v_exp_f32 v127, v127\n\t"
    "v_mfma_f32_16x16x32_bf16 v[160:163], v[48:51], v[52:55], 0\n\t"
    "ds_read_b128 v[236:239], v251 offset:17408\n\t"
    "v_exp_f32 v224, v224\n\t"
    "v_mfma_f32_16x16x32_bf16 v[232:235], v[40:43], v[52:55], 0\n\t"
    "v_exp_f32 v104, v104\n\t"
    "v_mfma_f32_16x16x32_bf16 v[220:223], v[228:231], v[52:55], 0\n\t"
    "v_cvt_pk_bf16_f32 v136, v124, v125\n\t"
    "v_cvt_pk_bf16_f32 v137, v126, v127\n\t"
    "v_mfma_f32_16x16x32_bf16 v[124:127], v[12:15], v[156:159], 0\n\t"
    "v_cvt_pk_bf16_f32 v138, v224, v224\n\t"
    "v_cvt_pk_bf16_f32 v129, v104, v104\n\t"
    "v_mfma_f32_16x16x32_bf16 v[104:107], v[148:151], v[156:159], 0\n\t"
    "ds_write_b16 v254, v129\n\t"
    "v_exp_f32 v240, v240\n\t"
    "ds_read_b128 v[176:179], v255 offset:0\n\t"
    "s_waitcnt lgkmcnt(6)\n\t"
    "v_mfma_f32_16x16x32_bf16 v[124:127], v[64:67], v[140:143], v[124:127]\n\t"
    "ds_read_b128 v[180:183], v255 offset:64\n\t"
    "v_exp_f32 v241, v241\n\t"
    "s_waitcnt lgkmcnt(6)\n\t"
    "v_mfma_f32_16x16x32_bf16 v[104:107], v[132:135], v[140:143], v[104:107]\n\t"
    "v_exp_f32 v242, v242\n\t"
    "s_waitcnt lgkmcnt(5)\n\t"
    "v_mfma_f32_16x16x32_bf16 v[184:187], v[112:115], v[100:103], 0\n\t"
    "ds_read_b128 v[188:191], v251 offset:18432\n\t"
    "v_exp_f32 v243, v243\n\t"
    "v_mfma_f32_16x16x32_bf16 v[192:195], v[112:115], v[96:99], 0\n\t"
    "v_exp_f32 v160, v160\n\t"
    "v_mfma_f32_16x16x32_bf16 v[196:199], v[112:115], v[92:95], 0\n\t"
    "v_rcp_f32 v129, v106\n\t"
    "s_waitcnt lgkmcnt(5)\n\t"
    "v_mfma_f32_16x16x32_bf16 v[184:187], v[116:119], v[88:91], v[184:187]\n\t"
    "ds_read_b128 v[112:115], v251 offset:19456\n\t"
    "v_mul_f32 v129, 0x3febdf2f, v129\n\t"
    "v_cvt_pk_bf16_f32 v156, v240, v241\n\t"
    "v_mfma_f32_16x16x32_bf16 v[192:195], v[116:119], v[84:87], v[192:195]\n\t"
    "v_mul_f32 v124, v124, v129\n\t"
    "v_mul_f32 v125, v125, v129\n\t"
    "v_mfma_f32_16x16x32_bf16 v[196:199], v[116:119], v[80:83], v[196:199]\n\t"
    "v_mul_f32 v126, v126, v129\n\t"
    "v_mul_f32 v127, v127, v129\n\t"
    "s_waitcnt lgkmcnt(5)\n\t"
    "v_mfma_f32_16x16x32_bf16 v[184:187], v[236:239], v[76:79], v[184:187]\n\t"
    "ds_read_b128 v[228:231], v251 offset:20480\n\t"
    "v_mul_f32 v104, v104, v129\n\t"
    "v_mul_f32 v105, v105, v129\n\t"
    "v_mfma_f32_16x16x32_bf16 v[192:195], v[236:239], v[72:75], v[192:195]\n\t"
    "v_exp_f32 v161, v161\n\t"
    "v_mfma_f32_16x16x32_bf16 v[196:199], v[236:239], v[68:71], v[196:199]\n\t"
    "v_exp_f32 v162, v162\n\t"
    "v_mfma_f32_16x16x32_bf16 v[116:119], v[12:15], v[152:155], 0\n\t"
    "v_exp_f32 v163, v163\n\t"
    "v_mfma_f32_16x16x32_bf16 v[236:239], v[148:151], v[152:155], 0\n\t"
    "v_cvt_pk_bf16_f32 v157, v242, v243\n\t"
    "v_cvt_pk_bf16_f32 v158, v160, v161\n\t"
    "v_mfma_f32_16x16x32_bf16 v[116:119], v[64:67], v[136:139], v[116:119]\n\t"
    "v_cvt_pk_bf16_f32 v159, v162, v163\n\t"
    "v_cvt_pk_bf16_f32 v124, v124, v125\n\t"
    "v_mfma_f32_16x16x32_bf16 v[236:239], v[132:135], v[136:139], v[236:239]\n\t"
    "v_exp_f32 v232, v232\n\t"
    "s_waitcnt lgkmcnt(2)\n\t"
    "v_mfma_f32_16x16x32_bf16 v[240:243], v[188:191], v[100:103], 0\n\t"
    "v_exp_f32 v233, v233\n\t"
    "v_mfma_f32_16x16x32_bf16 v[152:155], v[188:191], v[96:99], 0\n\t"
    "v_exp_f32 v234, v234\n\t"
    "v_mfma_f32_16x16x32_bf16 v[160:163], v[188:191], v[92:95], 0\n\t"
    "s_nop 0\n\t"
    "v_rcp_f32 v107, v238\n\t"
    "s_waitcnt lgkmcnt(1)\n\t"
    "v_mfma_f32_16x16x32_bf16 v[240:243], v[112:115], v[88:91], v[240:243]\n\t"
    "v_min_u32 v129, v106, v238\n\t"
    "v_max_u32 v191, v106, v238\n\t"
    "v_mfma_f32_16x16x32_bf16 v[152:155], v[112:115], v[84:87], v[152:155]\n\t"
    "ds_read_b128 v[200:203], v251 offset:21504\n\t"
    "v_exp_f32 v235, v235\n\t"
    "v_mfma_f32_16x16x32_bf16 v[160:163], v[112:115], v[80:83], v[160:163]\n\t"
    "v_exp_f32 v220, v220\n\t"
    "s_waitcnt lgkmcnt(1)\n\t"
    "v_mfma_f32_16x16x32_bf16 v[240:243], v[228:231], v[76:79], v[240:243]\n\t"
    "v_cvt_pk_bf16_f32 v140, v232, v233\n\t"
    "v_cvt_pk_bf16_f32 v141, v234, v235\n\t"
    "v_mfma_f32_16x16x32_bf16 v[152:155], v[228:231], v[72:75], v[152:155]\n\t"
    "ds_read_b128 v[112:115], v251 offset:22528\n\t"
    "v_cvt_pk_bf16_f32 v142, v220, v220\n\t"
    "v_mul_f32 v107, 0x3febdf2f, v107\n\t"
    "v_mfma_f32_16x16x32_bf16 v[160:163], v[228:231], v[68:71], v[160:163]\n\t"
    "v_cvt_pk_bf16_f32 v125, v126, v127\n\t"
    "v_cvt_pk_bf16_f32 v126, v104, v105\n\t"
    "v_mfma_f32_16x16x32_bf16 v[232:235], v[176:179], v[12:15], 0\n\t"
    "v_mul_f32 v116, v116, v107\n\t"
    "v_mul_f32 v117, v117, v107\n\t"
    "v_mfma_f32_16x16x32_bf16 v[204:207], v[176:179], v[148:151], 0\n\t"
    "ds_read_b128 v[176:179], v251 offset:23552\n\t"
    "v_mul_f32 v118, v118, v107\n\t"
    "v_mul_f32 v119, v119, v107\n\t"
    "v_mfma_f32_16x16x32_bf16 v[232:235], v[180:183], v[64:67], v[232:235]\n\t"
    "v_mul_f32 v236, v236, v107\n\t"
    "v_mul_f32 v237, v237, v107\n\t"
    "v_mfma_f32_16x16x32_bf16 v[204:207], v[180:183], v[132:135], v[204:207]\n\t"
    "v_cvt_pk_bf16_f32 v44, v108, v109\n\t"
    "v_cvt_pk_bf16_f32 v45, v110, v111\n\t"
    "v_mfma_f32_16x16x32_bf16 v[104:107], v[12:15], v[156:159], 0\n\t"
    "v_cvt_pk_bf16_f32 v46, v186, v187\n\t"
    "v_cvt_pk_bf16_f32 v58, v184, v185\n\t"
    "v_mfma_f32_16x16x32_bf16 v[12:15], v[148:151], v[156:159], 0\n\t"
    "v_cvt_pk_bf16_f32 v56, v240, v241\n\t"
    "v_cvt_pk_bf16_f32 v57, v242, v243\n\t"
    "v_mfma_f32_16x16x32_bf16 v[104:107], v[64:67], v[140:143], v[104:107]\n\t"
    "v_mov_b32_dpp v127, v204 quad_perm:[2,2,2,2] row_mask:0xf bank_mask:0xf bound_ctrl:1\n\t"
    "v_cvt_pk_bf16_f32 v48, v120, v121\n\t"
    "v_mfma_f32_16x16x32_bf16 v[12:15], v[132:135], v[140:143], v[12:15]\n\t"
    "v_cvt_pk_bf16_f32 v49, v122, v123\n\t"
    "v_cvt_pk_bf16_f32 v50, v194, v195\n\t"
    "s_waitcnt lgkmcnt(2)\n\t"
    "v_mfma_f32_16x16x32_bf16 v[240:243], v[100:103], v[200:203], 0\n\t"
    "v_cvt_pk_bf16_f32 v40, v164, v165\n\t"
    "v_cvt_pk_bf16_f32 v41, v166, v167\n\t"
    "v_mfma_f32_16x16x32_bf16 v[228:231], v[96:99], v[200:203], 0\n\t"
    "v_cvt_pk_bf16_f32 v42, v198, v199\n\t"
    "v_cvt_pk_bf16_f32 v62, v192, v193\n\t"
    "v_mfma_f32_16x16x32_bf16 v[108:111], v[92:95], v[200:203], 0\n\t"
    "ds_read_b128 v[148:151], v170 offset:256\n\t"
    "v_rcp_f32 v15, v14\n\t"
    "s_waitcnt lgkmcnt(2)\n\t"
    "v_mfma_f32_16x16x32_bf16 v[240:243], v[88:91], v[112:115], v[240:243]\n\t"
    "ds_read_b128 v[224:227], v169 offset:256\n\t"
    "v_rcp_f32 v123, v127\n\t"
    "v_mfma_f32_16x16x32_bf16 v[228:231], v[84:87], v[112:115], v[228:231]\n\t"
    "ds_read_b128 v[156:159], v168 offset:256\n\t"
    "v_cvt_pk_bf16_f32 v60, v152, v153\n\t"
    "v_cvt_pk_bf16_f32 v61, v154, v155\n\t"
    "v_mfma_f32_16x16x32_bf16 v[108:111], v[80:83], v[112:115], v[108:111]\n\t"
    "ds_read_b128 v[112:115], v175 offset:192\n\t"
    "v_min3_u32 v129, v129, v14, v127\n\t"
    "v_max3_u32 v191, v191, v14, v127\n\t"
    "s_waitcnt lgkmcnt(4)\n\t"
    "v_mfma_f32_16x16x32_bf16 v[240:243], v[76:79], v[176:179], v[240:243]\n\t"
    "v_cvt_pk_bf16_f32 v120, v116, v117\n\t"
    "v_cvt_pk_bf16_f32 v121, v118, v119\n\t"
    "v_mfma_f32_16x16x32_bf16 v[228:231], v[72:75], v[176:179], v[228:231]\n\t"
    "ds_read_b128 v[152:155], v174 offset:256\n\t"
    "v_cvt_pk_bf16_f32 v122, v236, v237\n\t"
    "v_mul_f32 v15, 0x3febdf2f, v15\n\t"
    "v_mfma_f32_16x16x32_bf16 v[108:111], v[68:71], v[176:179], v[108:111]\n\t"
    "ds_read_b128 v[164:167], v251 offset:24576\n\t"
    "v_mul_f32 v123, 0x3febdf2f, v123\n\t"
    "v_mul_f32 v104, v104, v15\n\t"
    "v_mfma_f32_16x16x32_bf16 v[236:239], v[44:47], v[56:59], 0\n\t"
    "v_mul_f32 v105, v105, v15\n\t"
    "v_mul_f32 v106, v106, v15\n\t"
    "v_mfma_f32_16x16x32_bf16 v[176:179], v[48:51], v[56:59], 0\n\t"
    "v_mul_f32 v107, v107, v15\n\t"
    "v_mul_f32 v12, v12, v15\n\t"
    "v_mfma_f32_16x16x32_bf16 v[180:183], v[40:43], v[56:59], 0\n\t"
    "ds_read_b128 v[184:187], v251 offset:25600\n\t"
    "v_mul_f32 v13, v13, v15\n\t"
    "v_mul_f32 v232, v232, v123\n\t"
    "v_mfma_f32_16x16x32_bf16 v[192:195], v[44:47], v[60:63], 0\n\t"
    "v_mul_f32 v204, v204, v123\n\t"
    "v_cvt_pk_bf16_f32 v54, v196, v197\n\t"
    "v_mfma_f32_16x16x32_bf16 v[220:223], v[48:51], v[60:63], 0\n\t"
    "v_cvt_pk_bf16_f32 v52, v160, v161\n\t"
    "v_cvt_pk_bf16_f32 v53, v162, v163\n\t"
    "v_mfma_f32_16x16x32_bf16 v[160:163], v[40:43], v[60:63], 0\n\t"
    "ds_read_b128 v[196:199], v251 offset:26624\n\t"
    "v_cvt_pk_bf16_f32 v116, v104, v105\n\t"
    "v_cvt_pk_bf16_f32 v117, v106, v107\n\t"
    "s_waitcnt lgkmcnt(7)\n\t"
    "v_mfma_f32_16x16x32_bf16 v[104:107], v[148:151], v[44:47], 0\n\t"
    "v_cvt_pk_bf16_f32 v118, v12, v13\n\t"
    "v_exp_f32 v236, v236\n\t"
    "s_waitcnt lgkmcnt(6)\n\t"
    "v_mfma_f32_16x16x32_bf16 v[104:107], v[224:227], v[48:51], v[104:107]\n\t"
    "v_exp_f32 v237, v237\n\t"
    "s_waitcnt lgkmcnt(5)\n\t"
    "v_mfma_f32_16x16x32_bf16 v[104:107], v[156:159], v[40:43], v[104:107]\n\t"
    "v_exp_f32 v238, v238\n\t"
    "s_waitcnt lgkmcnt(4)\n\t"
    "v_mfma_f32_16x16x32_bf16 v[224:227], v[112:115], v[56:59], 0\n\t"
    "v_exp_f32 v239, v239\n\t"
    "v_mfma_f32_16x16x32_bf16 v[200:203], v[112:115], v[60:63], 0\n\t"
    "v_exp_f32 v176, v176\n\t"
    "s_waitcnt lgkmcnt(3)\n\t"
    "v_mfma_f32_16x16x32_bf16 v[104:107], v[152:155], v[112:115], v[104:107]\n\t"
    "v_exp_f32 v177, v177\n\t"
    "s_waitcnt lgkmcnt(2)\n\t"
    "v_mfma_f32_16x16x32_bf16 v[216:219], v[164:167], v[100:103], 0\n\t"
    "v_exp_f32 v178, v178\n\t"
    "v_mfma_f32_16x16x32_bf16 v[212:215], v[164:167], v[96:99], 0\n\t"
    "v_exp_f32 v179, v179\n\t"
    "v_mfma_f32_16x16x32_bf16 v[208:211], v[164:167], v[92:95], 0\n\t"
    "ds_read_u16 v66, v145 offset:192\n\t"
    "v_cvt_pk_bf16_f32 v12, v240, v241\n\t"
    "v_cvt_pk_bf16_f32 v13, v242, v243\n\t"
    "s_waitcnt lgkmcnt(2)\n\t"
    "v_mfma_f32_16x16x32_bf16 v[216:219], v[184:187], v[88:91], v[216:219]\n\t"
    "ds_read_u16 v134, v145 offset:224\n\t"
    "v_cvt_pk_bf16_f32 v14, v228, v229\n\t"
    "v_cvt_pk_bf16_f32 v15, v230, v231\n\t"
    "v_mfma_f32_16x16x32_bf16 v[212:215], v[184:187], v[84:87], v[212:215]\n\t"
    "ds_read_b128 v[156:159], v251 offset:27648\n\t"
    "v_cvt_pk_bf16_f32 v152, v236, v237\n\t"
    "v_cvt_pk_bf16_f32 v153, v238, v239\n\t"
    "v_mfma_f32_16x16x32_bf16 v[208:211], v[184:187], v[80:83], v[208:211]\n\t"
    "v_cvt_pk_bf16_f32 v154, v176, v177\n\t"
    "v_cvt_pk_bf16_f32 v155, v178, v179\n\t"
    "s_waitcnt lgkmcnt(3)\n\t"
    "v_mfma_f32_16x16x32_bf16 v[216:219], v[196:199], v[76:79], v[216:219]\n\t"
    "v_cndmask_b32_e64 v148, v172, v128, s[82:83]\n\t"
    "v_cndmask_b32_e64 v149, v173, v128, s[82:83]\n\t"
    "v_mfma_f32_16x16x32_bf16 v[212:215], v[196:199], v[72:75], v[212:215]\n\t"
    "ds_read_b128 v[164:167], v251 offset:28672\n\t"
    "v_cndmask_b32_e64 v150, v146, v128, s[82:83]\n\t"
    "v_cndmask_b32_e64 v151, v147, v128, s[82:83]\n\t"
    "v_mfma_f32_16x16x32_bf16 v[208:211], v[196:199], v[68:71], v[208:211]\n\t"
    "v_exp_f32 v180, v180\n\t"
    "v_mfma_f32_16x16x32_bf16 v[176:179], v[44:47], v[52:55], 0\n\t"
    "v_exp_f32 v181, v181\n\t"
    "v_mfma_f32_16x16x32_bf16 v[184:187], v[48:51], v[52:55], 0\n\t"
    "ds_read_b128 v[196:199], v251 offset:29696\n\t"
    "v_exp_f32 v182, v182\n\t"
    "v_mfma_f32_16x16x32_bf16 v[228:231], v[40:43], v[52:55], 0\n\t"
    "v_exp_f32 v183, v183\n\t"
    "v_mfma_f32_16x16x32_bf16 v[236:239], v[112:115], v[52:55], 0\n\t"
    "v_exp_f32 v224, v224\n\t"
    "v_mfma_f32_16x16x32_bf16 v[240:243], v[12:15], v[152:155], 0\n\t"
    "v_cvt_pk_bf16_f32 v190, v232, v204\n\t"
    "v_cvt_pk_bf16_f32 v64, v108, v109\n\t"
    "v_mfma_f32_16x16x32_bf16 v[112:115], v[148:151], v[152:155], 0\n\t"
    "v_cvt_pk_bf16_f32 v65, v110, v111\n\t"
    "v_cvt_pk_bf16_f32 v136, v180, v181\n\t"
    "v_cvt_pk_bf16_f32 v137, v182, v183\n\t"
    "v_cvt_pk_bf16_f32 v138, v224, v224\n\t"
    "v_cndmask_b32_e64 v132, v130, v128, s[82:83]\n\t"
    "v_cndmask_b32_e64 v133, v131, v128, s[82:83]\n\t"
    "s_waitcnt lgkmcnt(4)\n\t"
    "v_mfma_f32_16x16x32_bf16 v[240:243], v[64:67], v[136:139], v[240:243]\n\t"
    "v_exp_f32 v192, v192\n\t"
    "s_waitcnt lgkmcnt(3)\n\t"
    "v_mfma_f32_16x16x32_bf16 v[112:115], v[132:135], v[136:139], v[112:115]\n\t"
    "v_exp_f32 v193, v193\n\t"
    "s_waitcnt lgkmcnt(2)\n\t"
    "v_mfma_f32_16x16x32_bf16 v[180:183], v[156:159], v[100:103], 0\n\t"
    "ds_read_b128 v[232:235], v251 offset:30720\n\t"
    "v_exp_f32 v194, v194\n\t"
    "v_mfma_f32_16x16x32_bf16 v[204:207], v[156:159], v[96:99], 0\n\t"
    "v_exp_f32 v195, v195\n\t"
    "v_mfma_f32_16x16x32_bf16 v[224:227], v[156:159], v[92:95], 0\n\t"
    "v_rcp_f32 v119, v112\n\t"
    "s_waitcnt lgkmcnt(2)\n\t"
    "v_mfma_f32_16x16x32_bf16 v[180:183], v[164:167], v[88:91], v[180:183]\n\t"
    "ds_read_b128 v[108:111], v251 offset:31744\n\t"
    "v_exp_f32 v220, v220\n\t"
    "v_mfma_f32_16x16x32_bf16 v[204:207], v[164:167], v[84:87], v[204:207]\n\t"
    "v_exp_f32 v221, v221\n\t"
    "v_mfma_f32_16x16x32_bf16 v[224:227], v[164:167], v[80:83], v[224:227]\n\t"
    "v_exp_f32 v222, v222\n\t"
    "s_waitcnt lgkmcnt(2)\n\t"
    "v_mfma_f32_16x16x32_bf16 v[180:183], v[196:199], v[76:79], v[180:183]\n\t"
    "ds_read_b128 v[152:155], v251 offset:32768\n\t"
    "v_exp_f32 v223, v223\n\t"
    "v_mfma_f32_16x16x32_bf16 v[204:207], v[196:199], v[72:75], v[204:207]\n\t"
    "v_cvt_pk_bf16_f32 v156, v192, v193\n\t"
    "v_cvt_pk_bf16_f32 v157, v194, v195\n\t"
    "v_mfma_f32_16x16x32_bf16 v[224:227], v[196:199], v[68:71], v[224:227]\n\t"
    "v_cvt_pk_bf16_f32 v158, v220, v221\n\t"
    "v_cvt_pk_bf16_f32 v159, v222, v223\n\t"
    "v_exp_f32 v160, v160\n\t"
    "v_exp_f32 v161, v161\n\t"
    "v_mfma_f32_16x16x32_bf16 v[220:223], v[12:15], v[156:159], 0\n\t"
    "v_exp_f32 v162, v162\n\t"
    "v_mfma_f32_16x16x32_bf16 v[196:199], v[148:151], v[156:159], 0\n\t"
    "v_exp_f32 v163, v163\n\t"
    "v_exp_f32 v200, v200\n\t"
    "v_exp_f32 v104, v104\n\t"
    "v_cvt_pk_bf16_f32 v140, v160, v161\n\t"
    "v_cvt_pk_bf16_f32 v141, v162, v163\n\t"
    "v_cvt_pk_bf16_f32 v142, v200, v200\n\t"
    "v_cvt_pk_bf16_f32 v113, v104, v104\n\t"
    "v_mul_f32 v119, 0x3febdf2f, v119\n\t"
    "v_mfma_f32_16x16x32_bf16 v[220:223], v[64:67], v[140:143], v[220:223]\n\t"
    "ds_write_b16 v254, v113\n\t"
    "v_mul_f32 v240, v240, v119\n\t"
    "v_mul_f32 v241, v241, v119\n\t"
    "v_mfma_f32_16x16x32_bf16 v[196:199], v[132:135], v[140:143], v[196:199]\n\t"
    "ds_read_b128 v[104:107], v255 offset:0\n\t"
    "v_mul_f32 v242, v242, v119\n\t"
    "v_mul_f32 v243, v243, v119\n\t"
    "s_waitcnt lgkmcnt(4)\n\t"
    "v_mfma_f32_16x16x32_bf16 v[156:159], v[232:235], v[100:103], 0\n\t"
    "ds_read_b128 v[200:203], v255 offset:64\n\t"
    "v_mul_f32 v114, v114, v119\n\t"
    "v_mul_f32 v115, v115, v119\n\t"
    "v_mfma_f32_16x16x32_bf16 v[160:163], v[232:235], v[96:99], 0\n\t"
    "v_rcp_f32 v119, v196\n\t"
    "v_mfma_f32_16x16x32_bf16 v[164:167], v[232:235], v[92:95], 0\n\t"
    "v_min3_u32 v129, v129, v112, v196\n\t"
    "v_max3_u32 v191, v191, v112, v196\n\t"
    "s_waitcnt lgkmcnt(4)\n\t"
    "v_mfma_f32_16x16x32_bf16 v[156:159], v[108:111], v[88:91], v[156:159]\n\t"
    "v_exp_f32 v176, v176\n\t"
    "v_mfma_f32_16x16x32_bf16 v[160:163], v[108:111], v[84:87], v[160:163]\n\t"
    "ds_read_b128 v[232:235], v251 offset:33792\n\t"
    "v_exp_f32 v177, v177\n\t"
    "v_mfma_f32_16x16x32_bf16 v[164:167], v[108:111], v[80:83], v[164:167]\n\t"
    "v_exp_f32 v178, v178\n\t"
    "s_waitcnt lgkmcnt(4)\n\t"
    "v_mfma_f32_16x16x32_bf16 v[156:159], v[152:155], v[76:79], v[156:159]\n\t"
    "v_exp_f32 v179, v179\n\t"
    "v_mfma_f32_16x16x32_bf16 v[160:163], v[152:155], v[72:75], v[160:163]\n\t"
    "ds_read_b128 v[108:111], v251 offset:34816\n\t"
    "v_exp_f32 v184, v184\n\t"
    "v_mfma_f32_16x16x32_bf16 v[164:167], v[152:155], v[68:71], v[164:167]\n\t"
    "v_exp_f32 v185, v185\n\t"
    "v_exp_f32 v186, v186\n\t"
    "v_exp_f32 v187, v187\n\t"
    "v_cvt_pk_bf16_f32 v152, v176, v177\n\t"
    "s_waitcnt lgkmcnt(3)\n\t"
    "v_mfma_f32_16x16x32_bf16 v[192:195], v[104:107], v[12:15], 0\n\t"
    "v_cvt_pk_bf16_f32 v153, v178, v179\n\t"
    "v_cvt_pk_bf16_f32 v154, v184, v185\n\t"
    "v_mfma_f32_16x16x32_bf16 v[176:179], v[104:107], v[148:151], 0\n\t"
    "ds_read_b128 v[104:107], v251 offset:35840\n\t"
    "v_cvt_pk_bf16_f32 v155, v186, v187\n\t"
    "v_mul_f32 v119, 0x3febdf2f, v119\n\t"
    "s_waitcnt lgkmcnt(3)\n\t"
    "v_mfma_f32_16x16x32_bf16 v[192:195], v[200:203], v[64:67], v[192:195]\n\t"
    "v_exp_f32 v228, v228\n\t"
    "v_mfma_f32_16x16x32_bf16 v[176:179], v[200:203], v[132:135], v[176:179]\n\t"
    "v_exp_f32 v229, v229\n\t"
    "v_mfma_f32_16x16x32_bf16 v[184:187], v[12:15], v[152:155], 0\n\t"
    "ds_read_b128 v[12:15], v251 offset:52224\n\t"
    "v_exp_f32 v230, v230\n\t"
    "v_mfma_f32_16x16x32_bf16 v[200:203], v[148:151], v[152:155], 0\n\t"
    "v_exp_f32 v231, v231\n\t"
    "v_exp_f32 v236, v236\n\t"
    "v_cvt_pk_bf16_f32 v136, v228, v229\n\t"
    "v_cvt_pk_bf16_f32 v137, v230, v231\n\t"
    "v_cvt_pk_bf16_f32 v138, v236, v236\n\t"
    "v_cvt_pk_bf16_f32 v127, v240, v241\n\t"
    "v_cvt_pk_bf16_f32 v112, v242, v243\n\t"
    "v_mfma_f32_16x16x32_bf16 v[184:187], v[64:67], v[136:139], v[184:187]\n\t"
    "v_cvt_pk_bf16_f32 v113, v114, v115\n\t"
    "v_mov_b32_dpp v123, v176 quad_perm:[0,0,0,0] row_mask:0xf bank_mask:0xf bound_ctrl:1\n\t"
    "v_mfma_f32_16x16x32_bf16 v[200:203], v[132:135], v[136:139], v[200:203]\n\t"
    "ds_read_b128 v[148:151], v251 offset:53248\n\t"
    "v_mul_f32 v220, v220, v119\n\t"
    "v_mul_f32 v221, v221, v119\n\t"
    "s_waitcnt lgkmcnt(4)\n\t"
    "v_mfma_f32_16x16x32_bf16 v[152:155], v[100:103], v[232:235], 0\n\t"
    "v_mul_f32 v222, v222, v119\n\t"
    "v_mul_f32 v223, v223, v119\n\t"
    "v_mfma_f32_16x16x32_bf16 v[240:243], v[96:99], v[232:235], 0\n\t"
    "v_mul_f32 v198, v198, v119\n\t"
    "v_mul_f32 v199, v199, v119\n\t"
    "v_mfma_f32_16x16x32_bf16 v[236:239], v[92:95], v[232:235], 0\n\t"
    "ds_read_b128 v[228:231], v251 offset:54272\n\t"
    "v_rcp_f32 v119, v200\n\t"
    "s_waitcnt lgkmcnt(4)\n\t"
    "v_mfma_f32_16x16x32_bf16 v[152:155], v[88:91], v[108:111], v[152:155]\n\t"
    "v_rcp_f32 v171, v123\n\t"
    "v_mfma_f32_16x16x32_bf16 v[240:243], v[84:87], v[108:111], v[240:243]\n\t"
    "v_min3_u32 v129, v129, v200, v123\n\t"
    "v_max3_u32 v191, v191, v200, v123\n\t"
    "v_mfma_f32_16x16x32_bf16 v[236:239], v[80:83], v[108:111], v[236:239]\n\t"
    "v_cvt_pk_bf16_f32 v123, v220, v221\n\t"
    "v_cvt_pk_bf16_f32 v108, v222, v223\n\t"
    "s_waitcnt lgkmcnt(3)\n\t"
    "v_mfma_f32_16x16x32_bf16 v[152:155], v[76:79], v[104:107], v[152:155]\n\t"
    "v_cvt_pk_bf16_f32 v109, v198, v199\n\t"
    "v_mul_f32 v119, 0x3febdf2f, v119\n\t"
    "v_mfma_f32_16x16x32_bf16 v[240:243], v[72:75], v[104:107], v[240:243]\n\t"
    "v_cvt_pk_bf16_f32 v44, v216, v217\n\t"
    "v_cvt_pk_bf16_f32 v45, v218, v219\n\t"
    "v_mfma_f32_16x16x32_bf16 v[236:239], v[68:71], v[104:107], v[236:239]\n\t"
    "v_cvt_pk_bf16_f32 v46, v182, v183\n\t"
    "v_cvt_pk_bf16_f32 v58, v180, v181\n\t"
    "s_waitcnt lgkmcnt(2)\n\t"
    "v_mfma_f32_16x16x32_bf16 v[180:183], v[100:103], v[12:15], 0\n\t"
    "v_cvt_pk_bf16_f32 v56, v156, v157\n\t"
    "v_cvt_pk_bf16_f32 v57, v158, v159\n\t"
    "v_mfma_f32_16x16x32_bf16 v[196:199], v[96:99], v[12:15], 0\n\t"
    "v_cvt_pk_bf16_f32 v48, v212, v213\n\t"
    "v_cvt_pk_bf16_f32 v49, v214, v215\n\t"
    "v_mfma_f32_16x16x32_bf16 v[212:215], v[92:95], v[12:15], 0\n\t"
    "ds_read_b128 v[232:235], v170 offset:448\n\t"
    "v_cvt_pk_bf16_f32 v50, v206, v207\n\t"
    "v_mul_f32 v171, 0x3febdf2f, v171\n\t"
    "s_waitcnt lgkmcnt(2)\n\t"
    "v_mfma_f32_16x16x32_bf16 v[180:183], v[88:91], v[148:151], v[180:183]\n\t"
    "ds_read_b128 v[156:159], v169 offset:448\n\t"
    "v_cvt_pk_bf16_f32 v40, v208, v209\n\t"
    "v_cvt_pk_bf16_f32 v41, v210, v211\n\t"
    "v_mfma_f32_16x16x32_bf16 v[196:199], v[84:87], v[148:151], v[196:199]\n\t"
    "ds_read_b128 v[208:211], v168 offset:448\n\t"
    "v_cvt_pk_bf16_f32 v42, v226, v227\n\t"
    "v_cvt_pk_bf16_f32 v62, v204, v205\n\t"
    "v_mfma_f32_16x16x32_bf16 v[212:215], v[80:83], v[148:151], v[212:215]\n\t"
    "ds_read_b128 v[220:223], v175 offset:384\n\t"
    "v_cvt_pk_bf16_f32 v60, v160, v161\n\t"
    "v_cvt_pk_bf16_f32 v61, v162, v163\n\t"
    "s_waitcnt lgkmcnt(4)\n\t"
    "v_mfma_f32_16x16x32_bf16 v[180:183], v[76:79], v[228:231], v[180:183]\n\t"
    "v_mul_f32 v184, v184, v119\n\t"
    "v_mul_f32 v185, v185, v119\n\t"
    "v_mfma_f32_16x16x32_bf16 v[196:199], v[72:75], v[228:231], v[196:199]\n\t"
    "ds_read_b128 v[216:219], v174 offset:448\n\t"
    "v_mul_f32 v186, v186, v119\n\t"
    "v_mul_f32 v187, v187, v119\n\t"
    "v_mfma_f32_16x16x32_bf16 v[212:215], v[68:71], v[228:231], v[212:215]\n\t"
    "ds_read_b128 v[148:151], v251 offset:36864\n\t"
    "v_mul_f32 v202, v202, v119\n\t"
    "v_mul_f32 v203, v203, v119\n\t"
    "v_mfma_f32_16x16x32_bf16 v[160:163], v[44:47], v[56:59], 0\n\t"
    "v_mul_f32 v192, v192, v171\n\t"
    "v_mul_f32 v176, v176, v171\n\t"
    "v_mfma_f32_16x16x32_bf16 v[204:207], v[48:51], v[56:59], 0\n\t"
    "v_cvt_pk_bf16_f32 v119, v184, v185\n\t"
    "v_cvt_pk_bf16_f32 v104, v186, v187\n\t"
    "v_mfma_f32_16x16x32_bf16 v[228:231], v[40:43], v[56:59], 0\n\t"
    "ds_read_b128 v[184:187], v251 offset:37888\n\t"
    "v_cvt_pk_bf16_f32 v105, v202, v203\n\t"
    "v_cvt_pk_bf16_f32 v189, v192, v176\n\t"
    "v_mfma_f32_16x16x32_bf16 v[200:203], v[44:47], v[60:63], 0\n\t"
    "v_cvt_pk_bf16_f32 v54, v224, v225\n\t"
    "v_cvt_pk_bf16_f32 v52, v164, v165\n\t"
    "v_mfma_f32_16x16x32_bf16 v[176:179], v[48:51], v[60:63], 0\n\t"
    "v_cvt_pk_bf16_f32 v53, v166, v167\n\t"
    "v_cvt_pk_bf16_f32 v12, v152, v153\n\t"
    "v_mfma_f32_16x16x32_bf16 v[164:167], v[40:43], v[60:63], 0\n\t"
    "ds_read_b128 v[224:227], v251 offset:38912\n\t"
    "v_exp_f32 v160, v160\n\t"
    "s_waitcnt lgkmcnt(7)\n\t"
    "v_mfma_f32_16x16x32_bf16 v[192:195], v[232:235], v[44:47], 0\n\t"
    "v_exp_f32 v161, v161\n\t"
    "s_waitcnt lgkmcnt(6)\n\t"
    "v_mfma_f32_16x16x32_bf16 v[192:195], v[156:159], v[48:51], v[192:195]\n\t"
    "v_exp_f32 v162, v162\n\t"
    "s_waitcnt lgkmcnt(5)\n\t"
    "v_mfma_f32_16x16x32_bf16 v[192:195], v[208:211], v[40:43], v[192:195]\n\t"
    "v_exp_f32 v163, v163\n\t"
    "s_waitcnt lgkmcnt(4)\n\t"
    "v_mfma_f32_16x16x32_bf16 v[208:211], v[220:223], v[56:59], 0\n\t"
    "v_exp_f32 v204, v204\n\t"
    "v_mfma_f32_16x16x32_bf16 v[232:235], v[220:223], v[60:63], 0\n\t"
    "v_exp_f32 v205, v205\n\t"
    "s_waitcnt lgkmcnt(3)\n\t"
    "v_mfma_f32_16x16x32_bf16 v[192:195], v[216:219], v[220:223], v[192:195]\n\t"
    "v_exp_f32 v206, v206\n\t"
    "s_waitcnt lgkmcnt(2)\n\t"
    "v_mfma_f32_16x16x32_bf16 v[216:219], v[148:151], v[100:103], 0\n\t"
    "v_exp_f32 v207, v207\n\t"
    "v_mfma_f32_16x16x32_bf16 v[130:133], v[148:151], v[96:99], 0\n\t"
    "v_cvt_pk_bf16_f32 v13, v154, v155\n\t"
    "v_cvt_pk_bf16_f32 v14, v240, v241\n\t"
    "v_mfma_f32_16x16x32_bf16 v[152:155], v[148:151], v[92:95], 0\n\t"
    "ds_read_u16 v66, v145 offset:384\n\t"
    "v_cvt_pk_bf16_f32 v15, v242, v243\n\t"
    "v_cvt_pk_bf16_f32 v156, v160, v161\n\t"
    "s_waitcnt lgkmcnt(2)\n\t"
    "v_mfma_f32_16x16x32_bf16 v[216:219], v[184:187], v[88:91], v[216:219]\n\t"
    "ds_read_u16 v134, v145 offset:416\n\t"
    "v_cvt_pk_bf16_f32 v157, v162, v163\n\t"
    "v_cvt_pk_bf16_f32 v158, v204, v205\n\t"
    "v_mfma_f32_16x16x32_bf16 v[130:133], v[184:187], v[84:87], v[130:133]\n\t"
    "ds_read_b128 v[160:163], v251 offset:39936\n\t"
    "v_cvt_pk_bf16_f32 v159, v206, v207\n\t"
    "v_cvt_pk_bf16_f32 v172, v180, v181\n\t"
    "v_mfma_f32_16x16x32_bf16 v[152:155], v[184:187], v[80:83], v[152:155]\n\t"
    "v_cvt_pk_bf16_f32 v173, v182, v183\n\t"
    "v_cvt_pk_bf16_f32 v146, v196, v197\n\t"
    "s_waitcnt lgkmcnt(3)\n\t"
    "v_mfma_f32_16x16x32_bf16 v[216:219], v[224:227], v[76:79], v[216:219]\n\t"
    "v_cvt_pk_bf16_f32 v147, v198, v199\n\t"
    "v_cndmask_b32_e64 v148, v128, v172, s[82:83]\n\t"
    "v_mfma_f32_16x16x32_bf16 v[130:133], v[224:227], v[72:75], v[130:133]\n\t"
    "ds_read_b128 v[180:183], v251 offset:40960\n\t"
    "v_cndmask_b32_e64 v149, v128, v173, s[82:83]\n\t"
    "v_cndmask_b32_e64 v150, v128, v146, s[82:83]\n\t"
    "v_mfma_f32_16x16x32_bf16 v[152:155], v[224:227], v[68:71], v[152:155]\n\t"
    "v_cndmask_b32_e64 v151, v128, v147, s[82:83]\n\t"
    "v_cvt_pk_bf16_f32 v64, v236, v237\n\t"
    "v_mfma_f32_16x16x32_bf16 v[240:243], v[44:47], v[52:55], 0\n\t"
    "v_cvt_pk_bf16_f32 v44, v216, v217\n\t"
    "v_cvt_pk_bf16_f32 v45, v218, v219\n\t"
    "v_mfma_f32_16x16x32_bf16 v[224:227], v[48:51], v[52:55], 0\n\t"
    "ds_read_b128 v[216:219], v251 offset:41984\n\t"
    "v_cvt_pk_bf16_f32 v48, v130, v131\n\t"
    "v_cvt_pk_bf16_f32 v49, v132, v133\n\t"
    "v_mfma_f32_16x16x32_bf16 v[204:207], v[40:43], v[52:55], 0\n\t"
    "v_cvt_pk_bf16_f32 v40, v152, v153\n\t"
    "v_cvt_pk_bf16_f32 v41, v154, v155\n\t"
    "v_mfma_f32_16x16x32_bf16 v[196:199], v[220:223], v[52:55], 0\n\t"
    "v_exp_f32 v228, v228\n\t"
    "v_mfma_f32_16x16x32_bf16 v[184:187], v[12:15], v[156:159], 0\n\t"
    "v_exp_f32 v229, v229\n\t"
    "v_mfma_f32_16x16x32_bf16 v[220:223], v[148:151], v[156:159], 0\n\t"
    "v_exp_f32 v230, v230\n\t"
    "v_exp_f32 v231, v231\n\t"
    "v_exp_f32 v208, v208\n\t"
    "v_cvt_pk_bf16_f32 v65, v238, v239\n\t"
    "v_cvt_pk_bf16_f32 v140, v228, v229\n\t"
    "v_cvt_pk_bf16_f32 v141, v230, v231\n\t"
    "v_cvt_pk_bf16_f32 v142, v208, v208\n\t"
    "v_cvt_pk_bf16_f32 v130, v212, v213\n\t"
    "v_cvt_pk_bf16_f32 v131, v214, v215\n\t"
    "s_waitcnt lgkmcnt(4)\n\t"
    "v_mfma_f32_16x16x32_bf16 v[184:187], v[64:67], v[140:143], v[184:187]\n\t"
    "v_cndmask_b32_e64 v132, v128, v130, s[82:83]\n\t"
    "v_cndmask_b32_e64 v133, v128, v131, s[82:83]\n\t"
    "v_exp_f32 v200, v200\n\t"
    "v_exp_f32 v201, v201\n\t"
    "s_waitcnt lgkmcnt(3)\n\t"
    "v_mfma_f32_16x16x32_bf16 v[220:223], v[132:135], v[140:143], v[220:223]\n\t"
    "v_exp_f32 v202, v202\n\t"
    "s_waitcnt lgkmcnt(2)\n\t"
    "v_mfma_f32_16x16x32_bf16 v[156:159], v[160:163], v[100:103], 0\n\t"
    "ds_read_b128 v[208:211], v251 offset:43008\n\t"
    "v_exp_f32 v203, v203\n\t"
    "v_mfma_f32_16x16x32_bf16 v[212:215], v[160:163], v[96:99], 0\n\t"
    "v_exp_f32 v176, v176\n\t"
    "v_mfma_f32_16x16x32_bf16 v[228:231], v[160:163], v[92:95], 0\n\t"
    "v_rcp_f32 v144, v222\n\t"
    "s_waitcnt lgkmcnt(2)\n\t"
    "v_mfma_f32_16x16x32_bf16 v[156:159], v[180:183], v[88:91], v[156:159]\n\t"
    "ds_read_b128 v[160:163], v251 offset:44032\n\t"
    "v_exp_f32 v177, v177\n\t"
    "v_mfma_f32_16x16x32_bf16 v[212:215], v[180:183], v[84:87], v[212:215]\n\t"
    "v_exp_f32 v178, v178\n\t"
    "v_mfma_f32_16x16x32_bf16 v[228:231], v[180:183], v[80:83], v[228:231]\n\t"
    "v_exp_f32 v179, v179\n\t"
    "s_waitcnt lgkmcnt(2)\n\t"
    "v_mfma_f32_16x16x32_bf16 v[156:159], v[216:219], v[76:79], v[156:159]\n\t"
    "ds_read_b128 v[180:183], v251 offset:45056\n\t"
    "v_cvt_pk_bf16_f32 v152, v200, v201\n\t"
    "v_cvt_pk_bf16_f32 v153, v202, v203\n\t"
    "v_mfma_f32_16x16x32_bf16 v[212:215], v[216:219], v[72:75], v[212:215]\n\t"
    "v_cvt_pk_bf16_f32 v154, v176, v177\n\t"
    "v_cvt_pk_bf16_f32 v155, v178, v179\n\t"
    "v_mfma_f32_16x16x32_bf16 v[228:231], v[216:219], v[68:71], v[228:231]\n\t"
    "v_exp_f32 v164, v164\n\t"
    "v_mfma_f32_16x16x32_bf16 v[236:239], v[12:15], v[152:155], 0\n\t"
    "v_cvt_pk_bf16_f32 v46, v158, v159\n\t"
    "v_cvt_pk_bf16_f32 v58, v156, v157\n\t"
    "v_mfma_f32_16x16x32_bf16 v[176:179], v[148:151], v[152:155], 0\n\t"
    "v_cvt_pk_bf16_f32 v50, v214, v215\n\t"
    "v_cvt_pk_bf16_f32 v62, v212, v213\n\t"
    "v_exp_f32 v165, v165\n\t"
    "v_exp_f32 v166, v166\n\t"
    "v_exp_f32 v167, v167\n\t"
    "v_exp_f32 v232, v232\n\t"
    "v_cvt_pk_bf16_f32 v42, v230, v231\n\t"
    "v_cvt_pk_bf16_f32 v54, v228, v229\n\t"
    "v_exp_f32 v192, v192\n\t"
    "v_cvt_pk_bf16_f32 v136, v164, v165\n\t"
    "v_cvt_pk_bf16_f32 v137, v166, v167\n\t"
    "v_cvt_pk_bf16_f32 v138, v232, v232\n\t"
    "v_cvt_pk_bf16_f32 v171, v192, v192\n\t"
    "v_mul_f32 v144, 0x3febdf2f, v144\n\t"
    "v_mfma_f32_16x16x32_bf16 v[236:239], v[64:67], v[136:139], v[236:239]\n\t"
    "ds_write_b16 v254, v171\n\t"
    "v_mul_f32 v184, v184, v144\n\t"
    "v_mul_f32 v185, v185, v144\n\t"
    "v_mfma_f32_16x16x32_bf16 v[176:179], v[132:135], v[136:139], v[176:179]\n\t"
    "ds_read_b128 v[232:235], v255 offset:0\n\t"
    "v_mul_f32 v186, v186, v144\n\t"
    "v_mul_f32 v187, v187, v144\n\t"
    "s_waitcnt lgkmcnt(4)\n\t"
    "v_mfma_f32_16x16x32_bf16 v[152:155], v[208:211], v[100:103], 0\n\t"
    "ds_read_b128 v[164:167], v255 offset:64\n\t"
    "v_mul_f32 v220, v220, v144\n\t"
    "v_mul_f32 v221, v221, v144\n\t"
    "v_mfma_f32_16x16x32_bf16 v[156:159], v[208:211], v[96:99], 0\n\t"
    "v_rcp_f32 v171, v178\n\t"
    "v_mfma_f32_16x16x32_bf16 v[192:195], v[208:211], v[92:95], 0\n\t"
    "v_min3_u32 v129, v129, v222, v178\n\t"
    "v_max3_u32 v191, v191, v222, v178\n\t"
    "s_waitcnt lgkmcnt(4)\n\t"
    "v_mfma_f32_16x16x32_bf16 v[152:155], v[160:163], v[88:91], v[152:155]\n\t"
    "v_exp_f32 v240, v240\n\t"
    "v_mfma_f32_16x16x32_bf16 v[156:159], v[160:163], v[84:87], v[156:159]\n\t"
    "ds_read_b128 v[228:231], v251 offset:46080\n\t"
    "v_exp_f32 v241, v241\n\t"
    "v_mfma_f32_16x16x32_bf16 v[192:195], v[160:163], v[80:83], v[192:195]\n\t"
    "v_exp_f32 v242, v242\n\t"
    "s_waitcnt lgkmcnt(4)\n\t"
    "v_mfma_f32_16x16x32_bf16 v[152:155], v[180:183], v[76:79], v[152:155]\n\t"
    "v_exp_f32 v243, v243\n\t"
    "v_mfma_f32_16x16x32_bf16 v[156:159], v[180:183], v[72:75], v[156:159]\n\t"
    "ds_read_b128 v[216:219], v251 offset:47104\n\t"
    "v_exp_f32 v224, v224\n\t"
    "v_mfma_f32_16x16x32_bf16 v[192:195], v[180:183], v[68:71], v[192:195]\n\t"
    "v_exp_f32 v225, v225\n\t"
    "v_exp_f32 v226, v226\n\t"
    "s_nop 0\n\t"
    "v_cvt_pk_bf16_f32 v56, v152, v153\n\t"
    "v_cvt_pk_bf16_f32 v57, v154, v155\n\t"
    "v_cvt_pk_bf16_f32 v60, v156, v157\n\t"
    "s_waitcnt lgkmcnt(3)\n\t"
    "v_mfma_f32_16x16x32_bf16 v[152:155], v[232:235], v[12:15], 0\n\t"
    "v_cvt_pk_bf16_f32 v61, v158, v159\n\t"
    "v_cvt_pk_bf16_f32 v52, v192, v193\n\t"
    "v_mfma_f32_16x16x32_bf16 v[160:163], v[232:235], v[148:151], 0\n\t"
    "ds_read_b128 v[180:183], v251 offset:48128\n\t"
    "v_cvt_pk_bf16_f32 v53, v194, v195\n\t"
    "v_cvt_pk_bf16_f32 v156, v240, v241\n\t"
    "s_waitcnt lgkmcnt(3)\n\t"
    "v_mfma_f32_16x16x32_bf16 v[152:155], v[164:167], v[64:67], v[152:155]\n\t"
    "v_exp_f32 v227, v227\n\t"
    "v_mfma_f32_16x16x32_bf16 v[160:163], v[164:167], v[132:135], v[160:163]\n\t"
    "v_cvt_pk_bf16_f32 v157, v242, v243\n\t"
    "v_cvt_pk_bf16_f32 v158, v224, v225\n\t"
    "v_cvt_pk_bf16_f32 v159, v226, v227\n\t"
    "v_exp_f32 v204, v204\n\t"
    "v_exp_f32 v205, v205\n\t"
    "v_mfma_f32_16x16x32_bf16 v[240:243], v[12:15], v[156:159], 0\n\t"
    "v_exp_f32 v206, v206\n\t"
    "v_mfma_f32_16x16x32_bf16 v[164:167], v[148:151], v[156:159], 0\n\t"
    "v_exp_f32 v207, v207\n\t"
    "v_exp_f32 v196, v196\n\t"
    "v_cvt_pk_bf16_f32 v140, v204, v205\n\t"
    "v_cvt_pk_bf16_f32 v141, v206, v207\n\t"
    "v_cvt_pk_bf16_f32 v142, v196, v196\n\t"
    "v_mul_f32 v171, 0x3febdf2f, v171\n\t"
    "v_cvt_pk_bf16_f32 v114, v184, v185\n\t"
    "v_mfma_f32_16x16x32_bf16 v[240:243], v[64:67], v[140:143], v[240:243]\n\t"
    "v_cvt_pk_bf16_f32 v115, v186, v187\n\t"
    "v_cvt_pk_bf16_f32 v144, v220, v221\n\t"
    "v_mfma_f32_16x16x32_bf16 v[164:167], v[132:135], v[140:143], v[164:167]\n\t"
    "v_mov_b32_dpp v188, v160 quad_perm:[2,2,2,2] row_mask:0xf bank_mask:0xf bound_ctrl:1\n\t"
    "v_mul_f32 v236, v236, v171\n\t"
    "s_waitcnt lgkmcnt(2)\n\t"
    "v_mfma_f32_16x16x32_bf16 v[148:151], v[100:103], v[228:231], 0\n\t"
    "v_mul_f32 v237, v237, v171\n\t"
    "v_mul_f32 v238, v238, v171\n\t"
    "v_mfma_f32_16x16x32_bf16 v[100:103], v[96:99], v[228:231], 0\n\t"
    "v_mul_f32 v239, v239, v171\n\t"
    "v_mul_f32 v176, v176, v171\n\t"
    "v_mfma_f32_16x16x32_bf16 v[96:99], v[92:95], v[228:231], 0\n\t"
    "v_mul_f32 v177, v177, v171\n\t"
    "v_min3_u32 v129, v129, v166, v188\n\t"
    "s_waitcnt lgkmcnt(1)\n\t"
    "v_mfma_f32_16x16x32_bf16 v[148:151], v[88:91], v[216:219], v[148:151]\n\t"
    "v_rcp_f32 v66, v166\n\t"
    "v_mfma_f32_16x16x32_bf16 v[100:103], v[84:87], v[216:219], v[100:103]\n\t"
    "v_rcp_f32 v167, v188\n\t"
    "v_mfma_f32_16x16x32_bf16 v[96:99], v[80:83], v[216:219], v[96:99]\n\t"
    "v_max3_u32 v191, v191, v166, v188\n\t"
    "v_cvt_pk_bf16_f32 v110, v236, v237\n\t"
    "s_waitcnt lgkmcnt(0)\n\t"
    "v_mfma_f32_16x16x32_bf16 v[148:151], v[76:79], v[180:183], v[148:151]\n\t"
    "v_cvt_pk_bf16_f32 v111, v238, v239\n\t"
    "v_cvt_pk_bf16_f32 v76, v176, v177\n\t"
    "v_mfma_f32_16x16x32_bf16 v[100:103], v[72:75], v[180:183], v[100:103]\n\t"
    "v_mul_f32 v66, 0x3febdf2f, v66\n\t"
    "v_mul_f32 v167, 0x3febdf2f, v167\n\t"
    "v_mfma_f32_16x16x32_bf16 v[96:99], v[68:71], v[180:183], v[96:99]\n\t"
    "v_mul_f32 v240, v240, v66\n\t"
    "v_mul_f32 v241, v241, v66\n\t"
    "v_cvt_pk_bf16_f32 v12, v148, v149\n\t"
    "v_cvt_pk_bf16_f32 v13, v150, v151\n\t"
    "v_mul_f32 v242, v242, v66\n\t"
    "v_cvt_pk_bf16_f32 v14, v100, v101\n\t"
    "v_cvt_pk_bf16_f32 v15, v102, v103\n\t"
    "v_mul_f32 v243, v243, v66\n\t"
    "v_cvt_pk_bf16_f32 v64, v96, v97\n\t"
    "v_cvt_pk_bf16_f32 v65, v98, v99\n\t"
    "v_mul_f32 v164, v164, v66\n\t"
    "v_mul_f32 v165, v165, v66\n\t"
    "v_mul_f32 v152, v152, v167\n\t"
    "v_mul_f32 v160, v160, v167\n\t"
    "v_cvt_pk_bf16_f32 v106, v240, v241\n\t"
    "v_cvt_pk_bf16_f32 v107, v242, v243\n\t"
    "v_cvt_pk_bf16_f32 v68, v164, v165\n\t"
    "v_cvt_pk_bf16_f32 v188, v152, v160\n\t"
    "s_sub_u32 s28, s28, 0x10000000\n\t"
    "s_cbranch_scc0 .Lw_drop1_%=\n\t"
    "s_setprio 0\n\t"
    ".Lw_drop1_%=:\n\t"
    "s_cmp_eq_u32 s51, 0\n\t"
    "s_cbranch_scc1 .Lw_r2b_%=\n\t"
    "v_mfma_f32_16x16x32_bf16 v[152:155], v[44:47], v[56:59], 0\n\t"
    "ds_read_b128 v[164:167], v170 offset:640\n\t"
    "ds_read_b128 v[160:163], v169 offset:640\n\t"
    "ds_read_b128 v[156:159], v168 offset:640\n\t"
    "v_mfma_f32_16x16x32_bf16 v[168:171], v[48:51], v[56:59], 0\n\t"
    "ds_read_b128 v[88:91], v175 offset:576\n\t"
    "ds_read_b128 v[80:83], v174 offset:640\n\t"
    "v_lshrrev_b32 v69, 2, v251\n\t"
    "v_mfma_f32_16x16x32_bf16 v[100:103], v[40:43], v[56:59], 0\n\t"
    "s_waitcnt vmcnt(0)\n\t"
    "ds_read_b128 v[240:243], v250 offset:0\n\t"
    "ds_read_b128 v[236:239], v244 offset:0\n\t"
    "v_mfma_f32_16x16x32_bf16 v[96:99], v[44:47], v[60:63], 0\n\t"
    "ds_read_b128 v[176:179], v245 offset:0\n\t"
    "v_and_b32 v69, 48, v69\n\t"
    "v_bfe_u32 v70, v251, 3, 3\n\t"
    "v_mfma_f32_16x16x32_bf16 v[92:95], v[48:51], v[60:63], 0\n\t"
    "ds_read_b128 v[180:183], v246 offset:0\n\t"
    "v_or_b32 v69, v69, v70\n\t"
    "v_cndmask_b32_e64 v148, v172, v128, s[82:83]\n\t"
    "v_mfma_f32_16x16x32_bf16 v[84:87], v[40:43], v[60:63], 0\n\t"
    "ds_read_b128 v[192:195], v250 offset:544\n\t"
    "ds_read_b128 v[196:199], v244 offset:64\n\t"
    "v_and_or_b32 v70, v69, 50, 8\n\t"
    "v_exp_f32 v152, v152\n\t"
    "ds_read_b128 v[72:75], v252 offset:20480\n\t"
    "v_exp_f32 v153, v153\n\t"
    "v_exp_f32 v154, v154\n\t"
    "v_exp_f32 v155, v155\n\t"
    "v_exp_f32 v168, v168\n\t"
    "v_exp_f32 v169, v169\n\t"
    "v_exp_f32 v170, v170\n\t"
    "v_exp_f32 v171, v171\n\t"
    "v_cvt_pk_bf16_f32 v152, v152, v153\n\t"
    "v_cvt_pk_bf16_f32 v153, v154, v155\n\t"
    "v_cvt_pk_bf16_f32 v154, v168, v169\n\t"
    "v_cvt_pk_bf16_f32 v155, v170, v171\n\t"
    "v_cndmask_b32_e64 v149, v173, v128, s[82:83]\n\t"
    "s_waitcnt lgkmcnt(11)\n\t"
    "v_mfma_f32_16x16x32_bf16 v[168:171], v[164:167], v[44:47], 0\n\t"
    "ds_read_b128 v[164:167], v245 offset:64\n\t"
    "v_cndmask_b32_e64 v150, v146, v128, s[82:83]\n\t"
    "v_cndmask_b32_e64 v151, v147, v128, s[82:83]\n\t"
    "s_waitcnt lgkmcnt(11)\n\t"
    "v_mfma_f32_16x16x32_bf16 v[168:171], v[160:163], v[48:51], v[168:171]\n\t"
    "ds_read_b128 v[160:163], v246 offset:64\n\t"
    "v_exp_f32 v100, v100\n\t"
    "s_waitcnt lgkmcnt(11)\n\t"
    "v_mfma_f32_16x16x32_bf16 v[168:171], v[156:159], v[40:43], v[168:171]\n\t"
    "ds_read_b128 v[172:175], v250 offset:1088\n\t"
    "ds_read_b128 v[200:203], v244 offset:128\n\t"
    "v_cndmask_b32_e64 v132, v130, v128, s[82:83]\n\t"
    "s_waitcnt lgkmcnt(12)\n\t"
    "v_mfma_f32_16x16x32_bf16 v[156:159], v[88:91], v[56:59], 0\n\t"
    "ds_read_b128 v[232:235], v245 offset:128\n\t"
    "v_exp_f32 v101, v101\n\t"
    "v_mfma_f32_16x16x32_bf16 v[184:187], v[88:91], v[60:63], 0\n\t"
    "ds_read_b128 v[228:231], v246 offset:128\n\t"
    "v_exp_f32 v102, v102\n\t"
    "s_waitcnt lgkmcnt(13)\n\t"
    "v_mfma_f32_16x16x32_bf16 v[168:171], v[80:83], v[88:91], v[168:171]\n\t"
    "v_exp_f32 v103, v103\n\t"
    "s_waitcnt lgkmcnt(11)\n\t"
    "v_mfma_f32_16x16x32_bf16 v[80:83], v[240:243], v[236:239], 0\n\t"
    "v_exp_f32 v156, v156\n\t"
    "s_waitcnt lgkmcnt(10)\n\t"
    "v_mfma_f32_16x16x32_bf16 v[60:63], v[240:243], v[176:179], 0\n\t"
    "v_cvt_pk_bf16_f32 v136, v100, v101\n\t"
    "v_cvt_pk_bf16_f32 v137, v102, v103\n\t"
    "s_waitcnt lgkmcnt(9)\n\t"
    "v_mfma_f32_16x16x32_bf16 v[56:59], v[240:243], v[180:183], 0\n\t"
    "ds_read_u16 v66, v145 offset:576\n\t"
    "v_cvt_pk_bf16_f32 v138, v156, v156\n\t"
    "v_cndmask_b32_e64 v133, v131, v128, s[82:83]\n\t"
    "s_waitcnt lgkmcnt(8)\n\t"
    "v_mfma_f32_16x16x32_bf16 v[80:83], v[192:195], v[196:199], v[80:83]\n\t"
    "ds_read_u16 v134, v145 offset:608\n\t"
    "v_exp_f32 v168, v168\n\t"
    "v_exp_f32 v96, v96\n\t"
    "v_exp_f32 v97, v97\n\t"
    "v_exp_f32 v98, v98\n\t"
    "s_waitcnt lgkmcnt(7)\n\t"
    "v_mfma_f32_16x16x32_bf16 v[60:63], v[192:195], v[164:167], v[60:63]\n\t"
    "v_exp_f32 v99, v99\n\t"
    "s_waitcnt lgkmcnt(6)\n\t"
    "v_mfma_f32_16x16x32_bf16 v[56:59], v[192:195], v[160:163], v[56:59]\n\t"
    "v_exp_f32 v92, v92\n\t"
    "s_waitcnt lgkmcnt(4)\n\t"
    "v_mfma_f32_16x16x32_bf16 v[80:83], v[172:175], v[200:203], v[80:83]\n\t"
    "v_exp_f32 v93, v93\n\t"
    "s_waitcnt lgkmcnt(3)\n\t"
    "v_mfma_f32_16x16x32_bf16 v[60:63], v[172:175], v[232:235], v[60:63]\n\t"
    "ds_read_b128 v[192:195], v250 offset:1632\n\t"
    "ds_read_b128 v[240:243], v244 offset:672\n\t"
    "v_cvt_pk_bf16_f32 v71, v168, v168\n\t"
    "s_waitcnt lgkmcnt(4)\n\t"
    "v_mfma_f32_16x16x32_bf16 v[56:59], v[172:175], v[228:231], v[56:59]\n\t"
    "ds_read_b128 v[196:199], v245 offset:672\n\t"
    "v_exp_f32 v94, v94\n\t"
    "ds_write_b16 v254, v71\n\t"
    "v_mfma_f32_16x16x32_bf16 v[176:179], v[44:47], v[52:55], 0\n\t"
    "ds_read_b128 v[44:47], v246 offset:672\n\t"
    "v_exp_f32 v95, v95\n\t"
    "ds_read_b128 v[168:171], v255 offset:0\n\t"
    "v_mfma_f32_16x16x32_bf16 v[172:175], v[48:51], v[52:55], 0\n\t"
    "ds_read_b128 v[48:51], v250 offset:2176\n\t"
    "ds_read_b128 v[236:239], v247 offset:0\n\t"
    "v_cvt_pk_bf16_f32 v156, v96, v97\n\t"
    "v_mfma_f32_16x16x32_bf16 v[100:103], v[40:43], v[52:55], 0\n\t"
    "ds_read_b128 v[40:43], v248 offset:0\n\t"
    "v_cvt_pk_bf16_f32 v157, v98, v99\n\t"
    "v_cvt_pk_bf16_f32 v158, v92, v93\n\t"
    "v_mfma_f32_16x16x32_bf16 v[180:183], v[88:91], v[52:55], 0\n\t"
    "ds_read_b128 v[232:235], v249 offset:0\n\t"
    "v_cvt_pk_bf16_f32 v159, v94, v95\n\t"
    "v_exp_f32 v84, v84\n\t"
    "ds_read_b128 v[164:167], v255 offset:64\n\t"
    "v_mfma_f32_16x16x32_bf16 v[52:55], v[12:15], v[152:155], 0\n\t"
    "ds_read_b128 v[88:91], v250 offset:2720\n\t"
    "ds_read_b128 v[92:95], v244 offset:32\n\t"
    "v_exp_f32 v85, v85\n\t"
    "v_mfma_f32_16x16x32_bf16 v[160:163], v[148:151], v[152:155], 0\n\t"
    "ds_read_b128 v[96:99], v245 offset:32\n\t"
    "v_exp_f32 v86, v86\n\t"
    "s_waitcnt lgkmcnt(15)\n\t"
    "v_mfma_f32_16x16x32_bf16 v[52:55], v[64:67], v[136:139], v[52:55]\n\t"
    "ds_read_b128 v[200:203], v246 offset:32\n\t"
    "v_exp_f32 v87, v87\n\t"
    "s_waitcnt lgkmcnt(15)\n\t"
    "v_mfma_f32_16x16x32_bf16 v[160:163], v[132:135], v[136:139], v[160:163]\n\t"
    "v_exp_f32 v184, v184\n\t"
    "v_cvt_pk_bf16_f32 v140, v84, v85\n\t"
    "v_cvt_pk_bf16_f32 v141, v86, v87\n\t"
    "v_cvt_pk_bf16_f32 v142, v184, v184\n\t"
    "v_exp_f32 v176, v176\n\t"
    "s_waitcnt lgkmcnt(13)\n\t"
    "v_mfma_f32_16x16x32_bf16 v[80:83], v[192:195], v[240:243], v[80:83]\n\t"
    "v_exp_f32 v177, v177\n\t"
    "s_waitcnt lgkmcnt(12)\n\t"
    "v_mfma_f32_16x16x32_bf16 v[60:63], v[192:195], v[196:199], v[60:63]\n\t"
    "v_exp_f32 v178, v178\n\t"
    "s_waitcnt lgkmcnt(10)\n\t"
    "v_mfma_f32_16x16x32_bf16 v[56:59], v[192:195], v[44:47], v[56:59]\n\t"
    "v_exp_f32 v179, v179\n\t"
    "s_waitcnt lgkmcnt(7)\n\t"
    "v_mfma_f32_16x16x32_bf16 v[80:83], v[48:51], v[236:239], v[80:83]\n\t"
    "ds_read_b128 v[84:87], v250 offset:3264\n\t"
    "ds_read_b128 v[184:187], v244 offset:96\n\t"
    "v_cvt_pk_bf16_f32 v152, v176, v177\n\t"
    "s_waitcnt lgkmcnt(8)\n\t"
    "v_mfma_f32_16x16x32_bf16 v[60:63], v[48:51], v[40:43], v[60:63]\n\t"
    "ds_read_b128 v[192:195], v245 offset:96\n\t"
    "v_exp_f32 v172, v172\n\t"
    "s_waitcnt lgkmcnt(8)\n\t"
    "v_mfma_f32_16x16x32_bf16 v[56:59], v[48:51], v[232:235], v[56:59]\n\t"
    "ds_read_b128 v[196:199], v246 offset:96\n\t"
    "v_exp_f32 v173, v173\n\t"
    "s_waitcnt lgkmcnt(6)\n\t"
    "v_mfma_f32_16x16x32_bf16 v[48:51], v[88:91], v[92:95], 0\n\t"
    "ds_read_b128 v[204:207], v250 offset:3808\n\t"
    "ds_read_b128 v[208:211], v244 offset:160\n\t"
    "ds_read_b128 v[212:215], v252 offset:18432\n\t"
    "s_waitcnt lgkmcnt(8)\n\t"
    "v_mfma_f32_16x16x32_bf16 v[44:47], v[88:91], v[96:99], 0\n\t"
    "ds_read_b128 v[216:219], v252 offset:19456\n\t"
    "ds_read_b128 v[240:243], v245 offset:160\n\t"
    "v_exp_f32 v174, v174\n\t"
    "s_waitcnt lgkmcnt(9)\n\t"
    "v_mfma_f32_16x16x32_bf16 v[40:43], v[88:91], v[200:203], 0\n\t"
    "ds_read_b128 v[236:239], v246 offset:160\n\t"
    "v_exp_f32 v175, v175\n\t"
    "v_mfma_f32_16x16x32_bf16 v[88:91], v[12:15], v[156:159], 0\n\t"
    "v_cvt_pk_bf16_f32 v153, v178, v179\n\t"
    "v_cvt_pk_bf16_f32 v154, v172, v173\n\t"
    "v_mfma_f32_16x16x32_bf16 v[92:95], v[148:151], v[156:159], 0\n\t"
    "v_cvt_pk_bf16_f32 v155, v174, v175\n\t"
    "v_exp_f32 v100, v100\n\t"
    "v_mfma_f32_16x16x32_bf16 v[88:91], v[64:67], v[140:143], v[88:91]\n\t"
    "v_exp_f32 v101, v101\n\t"
    "v_mfma_f32_16x16x32_bf16 v[92:95], v[132:135], v[140:143], v[92:95]\n\t"
    "v_exp_f32 v102, v102\n\t"
    "v_exp_f32 v103, v103\n\t"
    "v_exp_f32 v180, v180\n\t"
    "s_waitcnt lgkmcnt(8)\n\t"
    "v_mfma_f32_16x16x32_bf16 v[48:51], v[84:87], v[184:187], v[48:51]\n\t"
    "v_cvt_pk_bf16_f32 v136, v100, v101\n\t"
    "v_cvt_pk_bf16_f32 v137, v102, v103\n\t"
    "s_waitcnt lgkmcnt(7)\n\t"
    "v_mfma_f32_16x16x32_bf16 v[44:47], v[84:87], v[192:195], v[44:47]\n\t"
    "v_min3_u32 v129, v129, v160, v92\n\t"
    "v_max3_u32 v191, v191, v160, v92\n\t"
    "s_waitcnt lgkmcnt(6)\n\t"
    "v_mfma_f32_16x16x32_bf16 v[40:43], v[84:87], v[196:199], v[40:43]\n\t"
    "v_cvt_pk_bf16_f32 v138, v180, v180\n\t"
    "v_rcp_f32 v71, v160\n\t"
    "s_waitcnt lgkmcnt(4)\n\t"
    "v_mfma_f32_16x16x32_bf16 v[48:51], v[204:207], v[208:211], v[48:51]\n\t"
    "v_mul_f32 v71, 0x3febdf2f, v71\n\t"
    "v_rcp_f32 v92, v92\n\t"
    "s_waitcnt lgkmcnt(1)\n\t"
    "v_mfma_f32_16x16x32_bf16 v[44:47], v[204:207], v[240:243], v[44:47]\n\t"
    "ds_read_b128 v[240:243], v250 offset:4352\n\t"
    "ds_read_b128 v[232:235], v244 offset:704\n\t"
    "v_mul_f32 v52, v52, v71\n\t"
    "s_waitcnt lgkmcnt(2)\n\t"
    "v_mfma_f32_16x16x32_bf16 v[40:43], v[204:207], v[236:239], v[40:43]\n\t"
    "ds_read_b128 v[100:103], v245 offset:704\n\t"
    "v_mul_f32 v53, v53, v71\n\t"
    "v_mul_f32 v54, v54, v71\n\t"
    "v_mfma_f32_16x16x32_bf16 v[96:99], v[168:171], v[12:15], 0\n\t"
    "ds_read_b128 v[156:159], v246 offset:704\n\t"
    "v_mul_f32 v55, v55, v71\n\t"
    "v_mul_f32 v162, v162, v71\n\t"
    "v_mfma_f32_16x16x32_bf16 v[140:143], v[168:171], v[148:151], 0\n\t"
    "ds_read_b128 v[168:171], v250 offset:4896\n\t"
    "ds_read_b128 v[236:239], v247 offset:32\n\t"
    "v_mul_f32 v163, v163, v71\n\t"
    "v_mfma_f32_16x16x32_bf16 v[96:99], v[164:167], v[64:67], v[96:99]\n\t"
    "ds_read_b128 v[172:175], v248 offset:32\n\t"
    "v_cvt_pk_bf16_f32 v145, v52, v53\n\t"
    "v_cvt_pk_bf16_f32 v146, v54, v55\n\t"
    "v_mfma_f32_16x16x32_bf16 v[140:143], v[164:167], v[132:135], v[140:143]\n\t"
    "ds_read_b128 v[228:231], v249 offset:32\n\t"
    "v_cvt_pk_bf16_f32 v147, v162, v163\n\t"
    "v_mul_f32 v92, 0x3febdf2f, v92\n\t"
    "v_mfma_f32_16x16x32_bf16 v[84:87], v[12:15], v[152:155], 0\n\t"
    "ds_read_b128 v[52:55], v252 offset:21504\n\t"
    "v_mul_f32 v88, v88, v92\n\t"
    "v_mul_f32 v89, v89, v92\n\t"
    "v_mfma_f32_16x16x32_bf16 v[12:15], v[148:151], v[152:155], 0\n\t"
    "ds_read_b128 v[224:227], v252 offset:0\n\t"
    "v_mov_b32_dpp v93, v140 quad_perm:[0,0,0,0] row_mask:0xf bank_mask:0xf bound_ctrl:1\n\t"
    "v_mul_f32 v90, v90, v92\n\t"
    "v_mfma_f32_16x16x32_bf16 v[84:87], v[64:67], v[136:139], v[84:87]\n\t"
    "v_rcp_f32 v130, v93\n\t"
    "v_mfma_f32_16x16x32_bf16 v[12:15], v[132:135], v[136:139], v[12:15]\n\t"
    "ds_read_b128 v[64:67], v252 offset:1024\n\t"
    "v_mul_f32 v130, 0x3febdf2f, v130\n\t"
    "v_mul_f32 v91, v91, v92\n\t"
    "v_mul_f32 v94, v94, v92\n\t"
    "v_mul_f32 v95, v95, v92\n\t"
    "v_cvt_pk_bf16_f32 v77, v88, v89\n\t"
    "v_cvt_pk_bf16_f32 v78, v90, v91\n\t"
    "v_cvt_pk_bf16_f32 v79, v94, v95\n\t"
    "v_mul_f32 v96, v96, v130\n\t"
    "s_waitcnt lgkmcnt(9)\n\t"
    "v_mfma_f32_16x16x32_bf16 v[48:51], v[240:243], v[232:235], v[48:51]\n\t"
    "v_min3_u32 v129, v129, v12, v93\n\t"
    "v_max3_u32 v191, v191, v12, v93\n\t"
    "s_waitcnt lgkmcnt(8)\n\t"
    "v_mfma_f32_16x16x32_bf16 v[44:47], v[240:243], v[100:103], v[44:47]\n\t"
    "v_cmp_le_u32 vcc, s67, v191\n\t"
    "v_cmp_ge_u32_e64 s[62:63], s68, v129\n\t"
    "s_nop 1\n\t"
    "s_or_b64 s[62:63], s[62:63], vcc\n\t"
    "s_cselect_b32 s64, 160768, s53\n\t"
    "s_cselect_b32 s65, 0, 1\n\t"
    "s_cselect_b32 s66, 1, 0\n\t"
    "s_lshl_b32 s66, s66, s54\n\t"
    "s_or_b32 s55, s55, s66\n\t"
    "s_waitcnt lgkmcnt(7)\n\t"
    "v_mfma_f32_16x16x32_bf16 v[40:43], v[240:243], v[156:159], v[40:43]\n\t"
    "ds_read_b128 v[88:91], v252 offset:2048\n\t"
    "v_add_u32 v13, s64, v69\n\t"
    "v_add_u32 v97, s64, v70\n\t"
    "s_waitcnt lgkmcnt(6)\n\t"
    "v_mfma_f32_16x16x32_bf16 v[48:51], v[168:171], v[236:239], v[48:51]\n\t"
    "s_waitcnt lgkmcnt(4)\n\t"
    "s_cmp_lg_u32 s48, 0\n\t"
    "s_cbranch_scc1 .Lw_stub1_%=\n\t"
    ".Lw_back1_%=:\n\t"
    "s_mov_b32 m0, s25\n\t"
    "s_add_u32 s60, s45, 64\n\t"
    "buffer_load_dwordx4 v212, s[36:39], s60 offen lds\n\t"
    "v_mfma_f32_16x16x32_bf16 v[44:47], v[168:171], v[172:175], v[44:47]\n\t"
    "buffer_load_dwordx4 v213, s[36:39], s60 offen offset:1024 lds\n\t"
    "v_rcp_f32 v12, v12\n\t"
    "buffer_load_dwordx4 v214, s[36:39], s60 offen offset:2048 lds\n\t"
    "v_mfma_f32_16x16x32_bf16 v[40:43], v[168:171], v[228:231], v[40:43]\n\t"
    "buffer_load_dwordx4 v215, s[36:39], s60 offen offset:3072 lds\n\t"
    "v_mul_f32 v12, 0x3febdf2f, v12\n\t"
    "s_add_u32 m0, s25, 4096\n\t"
    "s_nop 0\n\t"
    "buffer_load_dwordx4 v216, s[36:39], s60 offen lds\n\t"
    "s_waitcnt lgkmcnt(3)\n\t"
    "v_mfma_f32_16x16x32_bf16 v[92:95], v[52:55], v[36:39], 0\n\t"
    "s_mov_b64 exec, 0xfffff\n\t"
    "buffer_load_dwordx4 v217, s[36:39], s60 offen offset:1024 lds\n\t"
    "s_mov_b64 exec, -1\n\t"
    "v_mfma_f32_16x16x32_bf16 v[240:243], v[52:55], v[32:35], 0\n\t"
    "ds_read_b128 v[236:239], v252 offset:22528\n\t"
    "v_mul_f32 v84, v84, v12\n\t"
    "v_mul_f32 v85, v85, v12\n\t"
    "v_mfma_f32_16x16x32_bf16 v[100:103], v[52:55], v[28:31], 0\n\t"
    "ds_read_b128 v[52:55], v252 offset:3072\n\t"
    "v_mul_f32 v86, v86, v12\n\t"
    "v_mul_f32 v87, v87, v12\n\t"
    "s_waitcnt lgkmcnt(4)\n\t"
    "v_mfma_f32_16x16x32_bf16 v[92:95], v[224:227], v[124:127], v[92:95]\n\t"
    "ds_write_b16 v13, v190 offset:64\n\t"
    "v_mul_f32 v14, v14, v12\n\t"
    "v_mul_f32 v15, v15, v12\n\t"
    "v_mfma_f32_16x16x32_bf16 v[240:243], v[224:227], v[120:123], v[240:243]\n\t"
    "ds_write_b16 v13, v189 offset:256\n\t"
    "v_cvt_pk_bf16_f32 v69, v84, v85\n\t"
    "v_cvt_pk_bf16_f32 v70, v86, v87\n\t"
    "v_mfma_f32_16x16x32_bf16 v[100:103], v[224:227], v[116:119], v[100:103]\n\t"
    "ds_read_b128 v[84:87], v252 offset:4096\n\t"
    "v_cvt_pk_bf16_f32 v71, v14, v15\n\t"
    "v_mul_f32 v140, v140, v130\n\t"
    "s_waitcnt lgkmcnt(6)\n\t"
    "v_mfma_f32_16x16x32_bf16 v[92:95], v[64:67], v[112:115], v[92:95]\n\t"
    "ds_write_b16 v13, v188 offset:448\n\t"
    "v_mov_b32 v129, v128\n\t"
    "v_mov_b32 v130, v128\n\t"
    "v_mfma_f32_16x16x32_bf16 v[240:243], v[64:67], v[108:111], v[240:243]\n\t"
    "v_mov_b32 v131, v128\n\t"
    "v_cvt_pk_bf16_f32 v191, v96, v140\n\t"
    "v_mfma_f32_16x16x32_bf16 v[100:103], v[64:67], v[104:107], v[100:103]\n\t"
    "ds_read_b128 v[64:67], v252 offset:5120\n\t"
    "ds_write_b16 v13, v191 offset:640\n\t"
    "s_mov_b64 exec, s[82:83]\n\t"
    "ds_write_b16_d16_hi v97, v190 offset:64\n\t"
    "ds_write_b16_d16_hi v97, v188 offset:448\n\t"
    "s_not_b64 exec, s[82:83]\n\t"
    "ds_write_b16_d16_hi v97, v189 offset:256\n\t"
    "ds_write_b16_d16_hi v97, v191 offset:640\n\t"
    "s_mov_b64 exec, -1\n\t"
    "s_waitcnt lgkmcnt(12)\n\t"
    "v_mfma_f32_16x16x32_bf16 v[92:95], v[88:91], v[144:147], v[92:95]\n\t"
    "s_min_u32 s66, s54, 1\n\t"
    "s_lshl_b32 s66, s66, 2\n\t"
    "s_add_u32 s66, s66, 160520\n\t"
    "v_mov_b32 v12, s66\n\t"
    "v_mov_b32 v13, s65\n\t"
    "s_mov_b64 exec, 1\n\t"
    "ds_add_u32 v12, v13\n\t"
    "s_mov_b64 exec, -1\n\t"
    "v_mfma_f32_16x16x32_bf16 v[240:243], v[88:91], v[76:79], v[240:243]\n\t"
    "v_cvt_pk_bf16_f32 v12, v92, v93\n\t"
    "v_cvt_pk_bf16_f32 v13, v94, v95\n\t"
    "v_mfma_f32_16x16x32_bf16 v[100:103], v[88:91], v[68:71], v[100:103]\n\t"
    "s_waitcnt lgkmcnt(12)\n\t"
    "v_mfma_f32_16x16x32_bf16 v[232:235], v[236:239], v[36:39], 0\n\t"
    "v_cvt_pk_bf16_f32 v36, v80, v81\n\t"
    "v_cvt_pk_bf16_f32 v37, v82, v83\n\t"
    "v_mfma_f32_16x16x32_bf16 v[92:95], v[236:239], v[32:35], 0\n\t"
    "v_cvt_pk_bf16_f32 v132, v240, v241\n\t"
    "v_cvt_pk_bf16_f32 v133, v242, v243\n\t"
    "v_mfma_f32_16x16x32_bf16 v[240:243], v[236:239], v[28:31], 0\n\t"
    "v_cvt_pk_bf16_f32 v136, v100, v101\n\t"
    "v_cvt_pk_bf16_f32 v137, v102, v103\n\t"
    "s_waitcnt lgkmcnt(11)\n\t"
    "v_mfma_f32_16x16x32_bf16 v[232:235], v[52:55], v[124:127], v[232:235]\n\t"
    "v_cvt_pk_bf16_f32 v38, v48, v49\n\t"
    "v_cvt_pk_bf16_f32 v39, v50, v51\n\t"
    "v_mfma_f32_16x16x32_bf16 v[92:95], v[52:55], v[120:123], v[92:95]\n\t"
    "v_cvt_pk_bf16_f32 v32, v60, v61\n\t"
    "v_cvt_pk_bf16_f32 v33, v62, v63\n\t"
    "v_mfma_f32_16x16x32_bf16 v[240:243], v[52:55], v[116:119], v[240:243]\n\t"
    "v_cvt_pk_bf16_f32 v34, v44, v45\n\t"
    "v_cvt_pk_bf16_f32 v35, v46, v47\n\t"
    "s_waitcnt lgkmcnt(8)\n\t"
    "v_mfma_f32_16x16x32_bf16 v[232:235], v[84:87], v[112:115], v[232:235]\n\t"
    "v_cvt_pk_bf16_f32 v28, v56, v57\n\t"
    "v_cvt_pk_bf16_f32 v29, v58, v59\n\t"
    "v_mfma_f32_16x16x32_bf16 v[92:95], v[84:87], v[108:111], v[92:95]\n\t"
    "v_cvt_pk_bf16_f32 v30, v40, v41\n\t"
    "v_cvt_pk_bf16_f32 v31, v42, v43\n\t"
    "v_mfma_f32_16x16x32_bf16 v[240:243], v[84:87], v[104:107], v[240:243]\n\t"
    "ds_read_b128 v[84:87], v250 offset:5440\n\t"
    "s_waitcnt lgkmcnt(7)\n\t"
    "v_mfma_f32_16x16x32_bf16 v[232:235], v[64:67], v[144:147], v[232:235]\n\t"
    "v_mfma_f32_16x16x32_bf16 v[92:95], v[64:67], v[76:79], v[92:95]\n\t"
    "v_mfma_f32_16x16x32_bf16 v[240:243], v[64:67], v[68:71], v[240:243]\n\t"
    "ds_read_b128 v[236:239], v250 offset:5984\n\t"
    "v_mfma_f32_16x16x32_bf16 v[88:91], v[36:39], v[36:39], 0\n\t"
    "s_nop 3\n\t"
    "v_cvt_pk_bf16_f32 v14, v232, v233\n\t"
    "v_mfma_f32_16x16x32_bf16 v[152:155], v[128:131], v[36:39], 0\n\t"
    "v_cvt_pk_bf16_f32 v15, v234, v235\n\t"
    "v_cvt_pk_bf16_f32 v134, v92, v93\n\t"
    "v_mfma_f32_16x16x32_bf16 v[140:143], v[32:35], v[32:35], 0\n\t"
    "buffer_store_dwordx4 v[12:15], v73, s[40:43], s44 offen offset:0\n\t"
    "ds_read_b128 v[148:151], v250 offset:6528\n\t"
    "v_cvt_pk_bf16_f32 v135, v94, v95\n\t"
    "v_mfma_f32_16x16x32_bf16 v[100:103], v[128:131], v[32:35], 0\n\t"
    "buffer_store_dwordx4 v[132:135], v74, s[40:43], s44 offen offset:0\n\t"
    "v_cvt_pk_bf16_f32 v138, v240, v241\n\t"
    "v_cvt_pk_bf16_f32 v139, v242, v243\n\t"
    "v_mfma_f32_16x16x32_bf16 v[96:99], v[28:31], v[28:31], 0\n\t"
    "buffer_store_dwordx4 v[136:139], v75, s[40:43], s44 offen offset:0\n\t"
    "v_mfma_f32_16x16x32_bf16 v[92:95], v[128:131], v[28:31], 0\n\t"
    "ds_read_b128 v[240:243], v250 offset:7072\n\t"
    "s_waitcnt vmcnt(3)\n\t"
    "ds_read_b128 v[12:15], v244 offset:0\n\t"
    "ds_read_b128 v[232:235], v245 offset:0\n\t"
    "ds_read_b128 v[132:135], v246 offset:0\n\t"
    "ds_read_b128 v[136:139], v244 offset:64\n\t"
    "ds_read_b128 v[156:159], v245 offset:64\n\t"
    "ds_read_b128 v[160:163], v246 offset:64\n\t"
    "ds_read_b128 v[164:167], v244 offset:128\n\t"
    "ds_read_b128 v[228:231], v245 offset:128\n\t"
    "ds_read_b128 v[168:171], v246 offset:128\n\t"
    "ds_read_b128 v[172:175], v244 offset:672\n\t"
    "s_waitcnt lgkmcnt(9)\n\t"
    "v_mfma_f32_16x16x32_bf16 v[64:67], v[84:87], v[12:15], 0\n\t"
    "ds_read_b128 v[176:179], v245 offset:672\n\t"
    "s_waitcnt lgkmcnt(9)\n\t"
    "v_mfma_f32_16x16x32_bf16 v[52:55], v[84:87], v[232:235], 0\n\t"
    "ds_read_b128 v[180:183], v246 offset:672\n\t"
    "s_waitcnt lgkmcnt(9)\n\t"
    "v_mfma_f32_16x16x32_bf16 v[12:15], v[84:87], v[132:135], 0\n\t"
    "ds_read_b128 v[84:87], v250 offset:7616\n\t"
    "ds_read_b128 v[132:135], v247 offset:0\n\t"
    "s_waitcnt lgkmcnt(10)\n\t"
    "v_mfma_f32_16x16x32_bf16 v[64:67], v[236:239], v[136:139], v[64:67]\n\t"
    "ds_read_b128 v[136:139], v248 offset:0\n\t"
    "s_waitcnt lgkmcnt(10)\n\t"
    "v_mfma_f32_16x16x32_bf16 v[52:55], v[236:239], v[156:159], v[52:55]\n\t"
    "ds_read_b128 v[156:159], v249 offset:0\n\t"
    "s_waitcnt lgkmcnt(10)\n\t"
    "v_mfma_f32_16x16x32_bf16 v[12:15], v[236:239], v[160:163], v[12:15]\n\t"
    "ds_read_b128 v[160:163], v250 offset:8160\n\t"
    "ds_read_b128 v[236:239], v244 offset:32\n\t"
    "s_waitcnt lgkmcnt(11)\n\t"
    "v_mfma_f32_16x16x32_bf16 v[64:67], v[148:151], v[164:167], v[64:67]\n\t"
    "ds_read_b128 v[232:235], v245 offset:32\n\t"
    "s_waitcnt lgkmcnt(11)\n\t"
    "v_mfma_f32_16x16x32_bf16 v[52:55], v[148:151], v[228:231], v[52:55]\n\t"
    "ds_read_b128 v[228:231], v246 offset:32\n\t"
    "s_waitcnt lgkmcnt(11)\n\t"
    "v_mfma_f32_16x16x32_bf16 v[12:15], v[148:151], v[168:171], v[12:15]\n\t"
    "ds_read_b128 v[148:151], v250 offset:8704\n\t"
    "ds_read_b128 v[164:167], v244 offset:96\n\t"
    "s_waitcnt lgkmcnt(12)\n\t"
    "v_mfma_f32_16x16x32_bf16 v[64:67], v[240:243], v[172:175], v[64:67]\n\t"
    "ds_read_b128 v[168:171], v245 offset:96\n\t"
    "s_waitcnt lgkmcnt(12)\n\t"
    "v_mfma_f32_16x16x32_bf16 v[52:55], v[240:243], v[176:179], v[52:55]\n\t"
    "ds_read_b128 v[172:175], v246 offset:96\n\t"
    "s_waitcnt lgkmcnt(12)\n\t"
    "v_mfma_f32_16x16x32_bf16 v[12:15], v[240:243], v[180:183], v[12:15]\n\t"
    "ds_read_b128 v[240:243], v250 offset:9248\n\t"
    "ds_read_b128 v[224:227], v244 offset:160\n\t"
    "ds_read_b128 v[220:223], v252 offset:18432\n\t"
    "ds_read_b128 v[216:219], v252 offset:19456\n\t"
    "s_waitcnt lgkmcnt(14)\n\t"
    "v_mfma_f32_16x16x32_bf16 v[64:67], v[84:87], v[132:135], v[64:67]\n\t"
    "ds_read_b128 v[176:179], v245 offset:160\n\t"
    "s_waitcnt lgkmcnt(14)\n\t"
    "v_mfma_f32_16x16x32_bf16 v[52:55], v[84:87], v[136:139], v[52:55]\n\t"
    "ds_read_b128 v[180:183], v246 offset:160\n\t"
    "s_waitcnt lgkmcnt(14)\n\t"
    "v_mfma_f32_16x16x32_bf16 v[12:15], v[84:87], v[156:159], v[12:15]\n\t"
    "ds_read_b128 v[156:159], v250 offset:9792\n\t"
    "ds_read_b128 v[212:215], v244 offset:704\n\t"
    "s_waitcnt lgkmcnt(14)\n\t"
    "v_mfma_f32_16x16x32_bf16 v[84:87], v[160:163], v[236:239], 0\n\t"
    "ds_read_b128 v[184:187], v245 offset:704\n\t"
    "s_waitcnt lgkmcnt(14)\n\t"
    "v_mfma_f32_16x16x32_bf16 v[136:139], v[160:163], v[232:235], 0\n\t"
    "ds_read_b128 v[188:191], v246 offset:704\n\t"
    "s_waitcnt lgkmcnt(14)\n\t"
    "v_mfma_f32_16x16x32_bf16 v[132:135], v[160:163], v[228:231], 0\n\t"
    "ds_read_b128 v[160:163], v250 offset:10336\n\t"
    "ds_read_b128 v[192:195], v247 offset:32\n\t"
    "ds_read_b128 v[236:239], v252 offset:21504\n\t"
    "s_waitcnt lgkmcnt(15)\n\t"
    "v_mfma_f32_16x16x32_bf16 v[84:87], v[148:151], v[164:167], v[84:87]\n\t"
    "ds_read_b128 v[164:167], v248 offset:32\n\t"
    "s_waitcnt lgkmcnt(15)\n\t"
    "v_mfma_f32_16x16x32_bf16 v[136:139], v[148:151], v[168:171], v[136:139]\n\t"
    "ds_read_b128 v[168:171], v249 offset:32\n\t"
    "s_waitcnt lgkmcnt(15)\n\t"
    "v_mfma_f32_16x16x32_bf16 v[132:135], v[148:151], v[172:175], v[132:135]\n\t"
    "ds_read_b128 v[148:151], v252 offset:6144\n\t"
    "s_waitcnt lgkmcnt(14)\n\t"
    "v_mfma_f32_16x16x32_bf16 v[84:87], v[240:243], v[224:227], v[84:87]\n\t"
    "s_waitcnt lgkmcnt(11)\n\t"
    "v_mfma_f32_16x16x32_bf16 v[136:139], v[240:243], v[176:179], v[136:139]\n\t"
    "s_waitcnt lgkmcnt(10)\n\t"
    "v_mfma_f32_16x16x32_bf16 v[132:135], v[240:243], v[180:183], v[132:135]\n\t"
    "ds_read_b128 v[172:175], v252 offset:7168\n\t"
    "s_waitcnt lgkmcnt(9)\n\t"
    "v_mfma_f32_16x16x32_bf16 v[84:87], v[156:159], v[212:215], v[84:87]\n\t"
    "s_waitcnt lgkmcnt(8)\n\t"
    "v_mfma_f32_16x16x32_bf16 v[136:139], v[156:159], v[184:187], v[136:139]\n\t"
    "s_waitcnt lgkmcnt(7)\n\t"
    "v_mfma_f32_16x16x32_bf16 v[132:135], v[156:159], v[188:191], v[132:135]\n\t"
    "ds_read_b128 v[240:243], v252 offset:8192\n\t"
    "s_waitcnt lgkmcnt(6)\n\t"
    "v_mfma_f32_16x16x32_bf16 v[84:87], v[160:163], v[192:195], v[84:87]\n\t"
    "s_waitcnt lgkmcnt(4)\n\t"
    "v_mfma_f32_16x16x32_bf16 v[136:139], v[160:163], v[164:167], v[136:139]\n\t"
    "s_waitcnt lgkmcnt(3)\n\t"
    "s_cmp_lg_u32 s48, 0\n\t"
    "s_cbranch_scc1 .Lw_stub2_%=\n\t"
    ".Lw_back2_%=:\n\t"
    "v_mfma_f32_16x16x32_bf16 v[132:135], v[160:163], v[168:171], v[132:135]\n\t"
    "s_mov_b32 m0, s25\n\t"
    "s_add_u32 s60, s45, 128\n\t"
    "buffer_load_dwordx4 v220, s[36:39], s60 offen lds\n\t"
    "v_mfma_f32_16x16x32_bf16 v[232:235], v[236:239], v[24:27], 0\n\t"
    "buffer_load_dwordx4 v221, s[36:39], s60 offen offset:1024 lds\n\t"
    "ds_read_b128 v[228:231], v252 offset:22528\n\t"
    "buffer_load_dwordx4 v222, s[36:39], s60 offen offset:2048 lds\n\t"
    "v_mfma_f32_16x16x32_bf16 v[224:227], v[236:239], v[20:23], 0\n\t"
    "buffer_load_dwordx4 v223, s[36:39], s60 offen offset:3072 lds\n\t"
    "s_add_u32 m0, s25, 4096\n\t"
    "s_nop 0\n\t"
    "buffer_load_dwordx4 v216, s[36:39], s60 offen lds\n\t"
    "v_mfma_f32_16x16x32_bf16 v[220:223], v[236:239], v[16:19], 0\n\t"
    "s_mov_b64 exec, 0xfffff\n\t"
    "buffer_load_dwordx4 v217, s[36:39], s60 offen offset:1024 lds\n\t"
    "s_mov_b64 exec, -1\n\t"
    "s_waitcnt lgkmcnt(3)\n\t"
    "v_mfma_f32_16x16x32_bf16 v[232:235], v[148:151], v[124:127], v[232:235]\n\t"
    "ds_read_b128 v[156:159], v252 offset:9216\n\t"
    "v_mfma_f32_16x16x32_bf16 v[224:227], v[148:151], v[120:123], v[224:227]\n\t"
    "v_mfma_f32_16x16x32_bf16 v[220:223], v[148:151], v[116:119], v[220:223]\n\t"
    "ds_read_b128 v[148:151], v252 offset:10240\n\t"
    "s_waitcnt lgkmcnt(4)\n\t"
    "v_mfma_f32_16x16x32_bf16 v[232:235], v[172:175], v[112:115], v[232:235]\n\t"
    "v_mfma_f32_16x16x32_bf16 v[224:227], v[172:175], v[108:111], v[224:227]\n\t"
    "v_mfma_f32_16x16x32_bf16 v[220:223], v[172:175], v[104:107], v[220:223]\n\t"
    "ds_read_b128 v[236:239], v252 offset:11264\n\t"
    "s_waitcnt lgkmcnt(4)\n\t"
    "v_mfma_f32_16x16x32_bf16 v[232:235], v[240:243], v[144:147], v[232:235]\n\t"
    "v_mfma_f32_16x16x32_bf16 v[224:227], v[240:243], v[76:79], v[224:227]\n\t"
    "v_mfma_f32_16x16x32_bf16 v[220:223], v[240:243], v[68:71], v[220:223]\n\t"
    "s_waitcnt lgkmcnt(3)\n\t"
    "v_mfma_f32_16x16x32_bf16 v[160:163], v[228:231], v[24:27], 0\n\t"
    "v_cvt_pk_bf16_f32 v24, v64, v65\n\t"
    "v_cvt_pk_bf16_f32 v25, v66, v67\n\t"
    "v_mfma_f32_16x16x32_bf16 v[240:243], v[228:231], v[20:23], 0\n\t"
    "s_nop 0\n\t"
    "v_cvt_pk_bf16_f32 v164, v232, v233\n\t"
    "v_cvt_pk_bf16_f32 v165, v234, v235\n\t"
    "v_mfma_f32_16x16x32_bf16 v[168:171], v[228:231], v[16:19], 0\n\t"
    "v_cvt_pk_bf16_f32 v172, v224, v225\n\t"
    "v_cvt_pk_bf16_f32 v173, v226, v227\n\t"
    "s_waitcnt lgkmcnt(2)\n\t"
    "v_mfma_f32_16x16x32_bf16 v[160:163], v[156:159], v[124:127], v[160:163]\n\t"
    "v_cvt_pk_bf16_f32 v232, v220, v221\n\t"
    "v_cvt_pk_bf16_f32 v233, v222, v223\n\t"
    "v_mfma_f32_16x16x32_bf16 v[240:243], v[156:159], v[120:123], v[240:243]\n\t"
    "v_cvt_pk_bf16_f32 v26, v84, v85\n\t"
    "v_cvt_pk_bf16_f32 v27, v86, v87\n\t"
    "v_mfma_f32_16x16x32_bf16 v[168:171], v[156:159], v[116:119], v[168:171]\n\t"
    "v_cvt_pk_bf16_f32 v20, v52, v53\n\t"
    "v_cvt_pk_bf16_f32 v21, v54, v55\n\t"
    "s_waitcnt lgkmcnt(1)\n\t"
    "v_mfma_f32_16x16x32_bf16 v[160:163], v[148:151], v[112:115], v[160:163]\n\t"
    "v_cvt_pk_bf16_f32 v22, v136, v137\n\t"
    "v_cvt_pk_bf16_f32 v23, v138, v139\n\t"
    "v_mfma_f32_16x16x32_bf16 v[240:243], v[148:151], v[108:111], v[240:243]\n\t"
    "v_cvt_pk_bf16_f32 v16, v12, v13\n\t"
    "v_cvt_pk_bf16_f32 v17, v14, v15\n\t"
    "v_mfma_f32_16x16x32_bf16 v[168:171], v[148:151], v[104:107], v[168:171]\n\t"
    "ds_read_b128 v[176:179], v250 offset:10880\n\t"
    "v_cvt_pk_bf16_f32 v18, v132, v133\n\t"
    "v_cvt_pk_bf16_f32 v19, v134, v135\n\t"
    "s_waitcnt lgkmcnt(1)\n\t"
    "v_mfma_f32_16x16x32_bf16 v[160:163], v[236:239], v[144:147], v[160:163]\n\t"
    "v_mfma_f32_16x16x32_bf16 v[240:243], v[236:239], v[76:79], v[240:243]\n\t"
    "v_mfma_f32_16x16x32_bf16 v[168:171], v[236:239], v[68:71], v[168:171]\n\t"
    "ds_read_b128 v[180:183], v250 offset:11424\n\t"
    "v_mfma_f32_16x16x32_bf16 v[88:91], v[24:27], v[24:27], v[88:91]\n\t"
    "s_nop 3\n\t"
    "v_cvt_pk_bf16_f32 v166, v160, v161\n\t"
    "v_cvt_pk_bf16_f32 v167, v162, v163\n\t"
    "v_mfma_f32_16x16x32_bf16 v[152:155], v[128:131], v[24:27], v[152:155]\n\t"
    "buffer_store_dwordx4 v[164:167], v73, s[40:43], s44 offen offset:64\n\t"
    "v_cvt_pk_bf16_f32 v174, v240, v241\n\t"
    "v_cvt_pk_bf16_f32 v175, v242, v243\n\t"
    "v_mfma_f32_16x16x32_bf16 v[140:143], v[20:23], v[20:23], v[140:143]\n\t"
    "ds_read_b128 v[240:243], v250 offset:11968\n\t"
    "buffer_store_dwordx4 v[172:175], v74, s[40:43], s44 offen offset:64\n\t"
    "v_cvt_pk_bf16_f32 v234, v168, v169\n\t"
    "v_mfma_f32_16x16x32_bf16 v[100:103], v[128:131], v[20:23], v[100:103]\n\t"
    "v_cvt_pk_bf16_f32 v235, v170, v171\n\t"
    "v_mfma_f32_16x16x32_bf16 v[96:99], v[16:19], v[16:19], v[96:99]\n\t"
    "buffer_store_dwordx4 v[232:235], v75, s[40:43], s44 offen offset:64\n\t"
    "v_mfma_f32_16x16x32_bf16 v[92:95], v[128:131], v[16:19], v[92:95]\n\t"
    "ds_read_b128 v[236:239], v250 offset:12512\n\t"
    "s_waitcnt vmcnt(3)\n\t"
    "ds_read_b128 v[148:151], v244 offset:0\n\t"
    "ds_read_b128 v[232:235], v245 offset:0\n\t"
    "ds_read_b128 v[228:231], v246 offset:0\n\t"
    "ds_read_b128 v[164:167], v244 offset:64\n\t"
    "ds_read_b128 v[224:227], v245 offset:64\n\t"
    "ds_read_b128 v[220:223], v246 offset:64\n\t"
    "ds_read_b128 v[216:219], v244 offset:128\n\t"
    "ds_read_b128 v[212:215], v245 offset:128\n\t"
    "ds_read_b128 v[168:171], v246 offset:128\n\t"
    "ds_read_b128 v[172:175], v244 offset:672\n\t"
    "s_waitcnt lgkmcnt(9)\n\t"
    "v_mfma_f32_16x16x32_bf16 v[160:163], v[176:179], v[148:151], 0\n\t"
    "ds_read_b128 v[184:187], v245 offset:672\n\t"
    "s_waitcnt lgkmcnt(9)\n\t"
    "v_mfma_f32_16x16x32_bf16 v[156:159], v[176:179], v[232:235], 0\n\t"
    "ds_read_b128 v[232:235], v246 offset:672\n\t"
    "s_waitcnt lgkmcnt(9)\n\t"
    "v_mfma_f32_16x16x32_bf16 v[148:151], v[176:179], v[228:231], 0\n\t"
    "ds_read_b128 v[176:179], v250 offset:13056\n\t"
    "ds_read_b128 v[188:191], v247 offset:0\n\t"
    "s_waitcnt lgkmcnt(10)\n\t"
    "v_mfma_f32_16x16x32_bf16 v[160:163], v[180:183], v[164:167], v[160:163]\n\t"
    "ds_read_b128 v[164:167], v248 offset:0\n\t"
    "s_waitcnt lgkmcnt(10)\n\t"
    "v_mfma_f32_16x16x32_bf16 v[156:159], v[180:183], v[224:227], v[156:159]\n\t"
    "ds_read_b128 v[228:231], v249 offset:0\n\t"
    "s_waitcnt lgkmcnt(10)\n\t"
    "v_mfma_f32_16x16x32_bf16 v[148:151], v[180:183], v[220:223], v[148:151]\n\t"
    "ds_read_b128 v[180:183], v250 offset:13600\n\t"
    "ds_read_b128 v[224:227], v244 offset:32\n\t"
    "s_waitcnt lgkmcnt(11)\n\t"
    "v_mfma_f32_16x16x32_bf16 v[160:163], v[240:243], v[216:219], v[160:163]\n\t"
    "ds_read_b128 v[192:195], v245 offset:32\n\t"
    "s_waitcnt lgkmcnt(11)\n\t"
    "v_mfma_f32_16x16x32_bf16 v[156:159], v[240:243], v[212:215], v[156:159]\n\t"
    "ds_read_b128 v[196:199], v246 offset:32\n\t"
    "s_waitcnt lgkmcnt(11)\n\t"
    "v_mfma_f32_16x16x32_bf16 v[148:151], v[240:243], v[168:171], v[148:151]\n\t"
    "ds_read_b128 v[200:203], v250 offset:14144\n\t"
    "ds_read_b128 v[204:207], v244 offset:96\n\t"
    "s_waitcnt lgkmcnt(12)\n\t"
    "v_mfma_f32_16x16x32_bf16 v[160:163], v[236:239], v[172:175], v[160:163]\n\t"
    "ds_read_b128 v[208:211], v245 offset:96\n\t"
    "s_waitcnt lgkmcnt(12)\n\t"
    "v_mfma_f32_16x16x32_bf16 v[156:159], v[236:239], v[184:187], v[156:159]\n\t"
    "ds_read_b128 v[184:187], v246 offset:96\n\t"
    "s_waitcnt lgkmcnt(12)\n\t"
    "v_mfma_f32_16x16x32_bf16 v[148:151], v[236:239], v[232:235], v[148:151]\n\t"
    "ds_read_b128 v[212:215], v250 offset:14688\n\t"
    "ds_read_b128 v[216:219], v244 offset:160\n\t"
    "s_waitcnt lgkmcnt(12)\n\t"
    "v_mfma_f32_16x16x32_bf16 v[160:163], v[176:179], v[188:191], v[160:163]\n\t"
    "ds_read_b128 v[188:191], v245 offset:160\n\t"
    "s_waitcnt lgkmcnt(12)\n\t"
    "v_mfma_f32_16x16x32_bf16 v[156:159], v[176:179], v[164:167], v[156:159]\n\t"
    "ds_read_b128 v[240:243], v246 offset:160\n\t"
    "s_waitcnt lgkmcnt(12)\n\t"
    "v_mfma_f32_16x16x32_bf16 v[148:151], v[176:179], v[228:231], v[148:151]\n\t"
    "ds_read_b128 v[176:179], v250 offset:15232\n\t"
    "ds_read_b128 v[220:223], v244 offset:704\n\t"
    "s_waitcnt lgkmcnt(12)\n\t"
    "v_mfma_f32_16x16x32_bf16 v[172:175], v[180:183], v[224:227], 0\n\t"
    "ds_read_b128 v[236:239], v245 offset:704\n\t"
    "s_waitcnt lgkmcnt(12)\n\t"
    "v_mfma_f32_16x16x32_bf16 v[168:171], v[180:183], v[192:195], 0\n\t"
    "ds_read_b128 v[192:195], v252 offset:18432\n\t"
    "ds_read_b128 v[232:235], v252 offset:19456\n\t"
    "ds_read_b128 v[224:227], v246 offset:704\n\t"
    "s_waitcnt lgkmcnt(14)\n\t"
    "v_mfma_f32_16x16x32_bf16 v[164:167], v[180:183], v[196:199], 0\n\t"
    "ds_read_b128 v[180:183], v250 offset:15776\n\t"
    "ds_read_b128 v[196:199], v247 offset:32\n\t"
    "ds_read_b128 v[228:231], v252 offset:21504\n\t"
    "s_waitcnt lgkmcnt(15)\n\t"
    "v_mfma_f32_16x16x32_bf16 v[172:175], v[200:203], v[204:207], v[172:175]\n\t"
    "ds_read_b128 v[204:207], v248 offset:32\n\t"
    "s_waitcnt lgkmcnt(15)\n\t"
    "v_mfma_f32_16x16x32_bf16 v[168:171], v[200:203], v[208:211], v[168:171]\n\t"
    "ds_read_b128 v[208:211], v249 offset:32\n\t"
    "s_waitcnt lgkmcnt(15)\n\t"
    "v_mfma_f32_16x16x32_bf16 v[164:167], v[200:203], v[184:187], v[164:167]\n\t"
    "ds_read_b128 v[184:187], v252 offset:12288\n\t"
    "s_waitcnt lgkmcnt(14)\n\t"
    "v_mfma_f32_16x16x32_bf16 v[172:175], v[212:215], v[216:219], v[172:175]\n\t"
    "s_waitcnt lgkmcnt(13)\n\t"
    "v_mfma_f32_16x16x32_bf16 v[168:171], v[212:215], v[188:191], v[168:171]\n\t"
    "s_waitcnt lgkmcnt(12)\n\t"
    "v_mfma_f32_16x16x32_bf16 v[164:167], v[212:215], v[240:243], v[164:167]\n\t"
    "ds_read_b128 v[188:191], v252 offset:13312\n\t"
    "s_waitcnt lgkmcnt(11)\n\t"
    "v_mfma_f32_16x16x32_bf16 v[172:175], v[176:179], v[220:223], v[172:175]\n\t"
    "s_waitcnt lgkmcnt(10)\n\t"
    "v_mfma_f32_16x16x32_bf16 v[168:171], v[176:179], v[236:239], v[168:171]\n\t"
    "s_waitcnt lgkmcnt(7)\n\t"
    "v_mfma_f32_16x16x32_bf16 v[164:167], v[176:179], v[224:227], v[164:167]\n\t"
    "ds_read_b128 v[240:243], v252 offset:14336\n\t"
    "s_waitcnt lgkmcnt(6)\n\t"
    "v_mfma_f32_16x16x32_bf16 v[172:175], v[180:183], v[196:199], v[172:175]\n\t"
    "s_waitcnt lgkmcnt(4)\n\t"
    "v_mfma_f32_16x16x32_bf16 v[168:171], v[180:183], v[204:207], v[168:171]\n\t"
    "s_waitcnt lgkmcnt(3)\n\t"
    "s_cmp_lg_u32 s49, 0\n\t"
    "s_cbranch_scc1 .Lw_stub3_%=\n\t"
    ".Lw_back3_%=:\n\t"
    "v_mfma_f32_16x16x32_bf16 v[164:167], v[180:183], v[208:211], v[164:167]\n\t"
    "s_mov_b32 m0, s25\n\t"
    "s_add_u32 s60, s46, 0\n\t"
    "buffer_load_dwordx4 v192, s[36:39], s60 offen lds\n\t"
    "v_mfma_f32_16x16x32_bf16 v[176:179], v[228:231], v[8:11], 0\n\t"
    "buffer_load_dwordx4 v193, s[36:39], s60 offen offset:1024 lds\n\t"
    "ds_read_b128 v[180:183], v252 offset:22528\n\t"
    "buffer_load_dwordx4 v194, s[36:39], s60 offen offset:2048 lds\n\t"
    "v_mfma_f32_16x16x32_bf16 v[236:239], v[228:231], v[4:7], 0\n\t"
    "buffer_load_dwordx4 v195, s[36:39], s60 offen offset:3072 lds\n\t"
    "s_add_u32 m0, s25, 4096\n\t"
    "s_nop 0\n\t"
    "buffer_load_dwordx4 v232, s[36:39], s60 offen lds\n\t"
    "v_mfma_f32_16x16x32_bf16 v[192:195], v[228:231], v[0:3], 0\n\t"
    "s_mov_b64 exec, 0xfffff\n\t"
    "buffer_load_dwordx4 v233, s[36:39], s60 offen offset:1024 lds\n\t"
    "s_mov_b64 exec, -1\n\t"
    "s_waitcnt lgkmcnt(3)\n\t"
    "v_mfma_f32_16x16x32_bf16 v[176:179], v[184:187], v[124:127], v[176:179]\n\t"
    "s_mov_b32 m0, 160768\n\t"
    "s_nop 0\n\t"
    "buffer_load_dword v235, s[36:39], s60 offen lds\n\t"
    "v_mfma_f32_16x16x32_bf16 v[236:239], v[184:187], v[120:123], v[236:239]\n\t"
    "ds_read_b128 v[196:199], v252 offset:15360\n\t"
    "buffer_load_dword v72, s[36:39], s60 offen lds\n\t"
    "v_mfma_f32_16x16x32_bf16 v[192:195], v[184:187], v[116:119], v[192:195]\n\t"
    "ds_read_b128 v[184:187], v252 offset:16384\n\t"
    "s_waitcnt lgkmcnt(4)\n\t"
    "v_mfma_f32_16x16x32_bf16 v[176:179], v[188:191], v[112:115], v[176:179]\n\t"
    "v_mfma_f32_16x16x32_bf16 v[236:239], v[188:191], v[108:111], v[236:239]\n\t"
    "v_mfma_f32_16x16x32_bf16 v[192:195], v[188:191], v[104:107], v[192:195]\n\t"
    "ds_read_b128 v[232:235], v252 offset:17408\n\t"
    "s_waitcnt lgkmcnt(4)\n\t"
    "v_mfma_f32_16x16x32_bf16 v[176:179], v[240:243], v[144:147], v[176:179]\n\t"
    "v_mfma_f32_16x16x32_bf16 v[236:239], v[240:243], v[76:79], v[236:239]\n\t"
    "v_mfma_f32_16x16x32_bf16 v[192:195], v[240:243], v[68:71], v[192:195]\n\t"
    "s_waitcnt lgkmcnt(3)\n\t"
    "v_mfma_f32_16x16x32_bf16 v[188:191], v[180:183], v[8:11], 0\n\t"
    "v_cvt_pk_bf16_f32 v8, v160, v161\n\t"
    "v_cvt_pk_bf16_f32 v9, v162, v163\n\t"
    "v_mfma_f32_16x16x32_bf16 v[240:243], v[180:183], v[4:7], 0\n\t"
    "s_nop 0\n\t"
    "v_cvt_pk_bf16_f32 v228, v176, v177\n\t"
    "v_cvt_pk_bf16_f32 v229, v178, v179\n\t"
    "v_mfma_f32_16x16x32_bf16 v[224:227], v[180:183], v[0:3], 0\n\t"
    "v_cvt_pk_bf16_f32 v176, v236, v237\n\t"
    "v_cvt_pk_bf16_f32 v177, v238, v239\n\t"
    "v_cvt_pk_bf16_f32 v180, v192, v193\n\t"
    "s_waitcnt lgkmcnt(2)\n\t"
    "v_mfma_f32_16x16x32_bf16 v[188:191], v[196:199], v[124:127], v[188:191]\n\t"
    "v_cvt_pk_bf16_f32 v181, v194, v195\n\t"
    "v_cvt_pk_bf16_f32 v10, v172, v173\n\t"
    "v_mfma_f32_16x16x32_bf16 v[240:243], v[196:199], v[120:123], v[240:243]\n\t"
    "v_cvt_pk_bf16_f32 v11, v174, v175\n\t"
    "v_cvt_pk_bf16_f32 v4, v156, v157\n\t"
    "v_mfma_f32_16x16x32_bf16 v[224:227], v[196:199], v[116:119], v[224:227]\n\t"
    "ds_read_b128 v[116:119], v251 offset:0\n\t"
    "v_cvt_pk_bf16_f32 v5, v158, v159\n\t"
    "v_cvt_pk_bf16_f32 v6, v168, v169\n\t"
    "s_waitcnt lgkmcnt(2)\n\t"
    "v_mfma_f32_16x16x32_bf16 v[188:191], v[184:187], v[112:115], v[188:191]\n\t"
    "v_cvt_pk_bf16_f32 v7, v170, v171\n\t"
    "v_cvt_pk_bf16_f32 v0, v148, v149\n\t"
    "v_mfma_f32_16x16x32_bf16 v[240:243], v[184:187], v[108:111], v[240:243]\n\t"
    "v_cvt_pk_bf16_f32 v1, v150, v151\n\t"
    "v_cvt_pk_bf16_f32 v2, v164, v165\n\t"
    "v_mfma_f32_16x16x32_bf16 v[224:227], v[184:187], v[104:107], v[224:227]\n\t"
    "ds_read_b128 v[236:239], v251 offset:1024\n\t"
    "v_cvt_pk_bf16_f32 v3, v166, v167\n\t"
    "s_waitcnt lgkmcnt(2)\n\t"
    "v_mfma_f32_16x16x32_bf16 v[188:191], v[232:235], v[144:147], v[188:191]\n\t"
    "v_mfma_f32_16x16x32_bf16 v[240:243], v[232:235], v[76:79], v[240:243]\n\t"
    "v_mfma_f32_16x16x32_bf16 v[224:227], v[232:235], v[68:71], v[224:227]\n\t"
    "ds_read_b128 v[104:107], v251 offset:2048\n\t"
    "v_mfma_f32_16x16x32_bf16 v[88:91], v[8:11], v[8:11], v[88:91]\n\t"
    "s_nop 3\n\t"
    "v_cvt_pk_bf16_f32 v230, v188, v189\n\t"
    "v_cvt_pk_bf16_f32 v231, v190, v191\n\t"
    "v_mfma_f32_16x16x32_bf16 v[152:155], v[128:131], v[8:11], v[152:155]\n\t"
    "buffer_store_dwordx4 v[228:231], v73, s[40:43], s44 offen offset:128\n\t"
    "v_cvt_pk_bf16_f32 v178, v240, v241\n\t"
    "v_cvt_pk_bf16_f32 v179, v242, v243\n\t"
    "v_mfma_f32_16x16x32_bf16 v[140:143], v[4:7], v[4:7], v[140:143]\n\t"
    "buffer_store_dwordx4 v[176:179], v74, s[40:43], s44 offen offset:128\n\t"
    "v_cndmask_b32_e64 v183, v91, v90, s[80:81]\n\t"
    "v_cvt_pk_bf16_f32 v182, v224, v225\n\t"
    "v_mfma_f32_16x16x32_bf16 v[100:103], v[128:131], v[4:7], v[100:103]\n\t"
    "ds_read_b128 v[240:243], v251 offset:3072\n\t"
    "v_cndmask_b32_e64 v183, v183, v89, s[78:79]\n\t"
    "v_mul_f32 v224, 0x3c2aaaab, v152\n\t"
    "v_mfma_f32_16x16x32_bf16 v[96:99], v[0:3], v[0:3], v[96:99]\n\t"
    "v_cndmask_b32_e64 v183, v183, v88, s[76:77]\n\t"
    "v_cndmask_b32_e64 v225, v143, v142, s[80:81]\n\t"
    "v_mfma_f32_16x16x32_bf16 v[92:95], v[128:131], v[0:3], v[92:95]\n\t"
    "ds_bpermute_b32 v142, v253, v183\n\t"
    "v_cndmask_b32_e64 v225, v225, v141, s[78:79]\n\t"
    "v_cvt_pk_bf16_f32 v183, v226, v227\n\t"
    "v_cndmask_b32_e64 v225, v225, v140, s[76:77]\n\t"
    "buffer_store_dwordx4 v[180:183], v75, s[40:43], s44 offen offset:128\n\t"
    "ds_bpermute_b32 v226, v253, v225\n\t"
    "v_cndmask_b32_e64 v98, v99, v98, s[80:81]\n\t"
    "v_mul_f32_e64 v99, -v224, v224\n\t"
    "v_cndmask_b32_e64 v98, v98, v97, s[78:79]\n\t"
    "v_mul_f32 v97, 0x3c2aaaab, v100\n\t"
    "v_cndmask_b32_e64 v98, v98, v96, s[76:77]\n\t"
    "v_mul_f32_e64 v96, -v97, v97\n\t"
    "ds_bpermute_b32 v225, v253, v98\n\t"
    "v_mul_f32 v227, 0x3c2aaaab, v92\n\t"
    "ds_read_b128 v[232:235], v251 offset:4096\n\t"
    "v_mul_f32_e64 v98, -v227, v227\n\t"
    "s_waitcnt lgkmcnt(3)\n\t"
    "v_fmamk_f32 v99, v142, 0x3c2aaaab, v99\n\t"
    "v_max_f32 v99, 0, v99\n\t"
    "v_add_f32 v99, 0x3727c5ac, v99\n\t"
    "s_waitcnt lgkmcnt(2)\n\t"
    "v_fmamk_f32 v96, v226, 0x3c2aaaab, v96\n\t"
    "v_rsq_f32 v99, v99\n\t"
    "v_max_f32 v96, 0, v96\n\t"
    "v_mul_f32 v226, 0x3f0aec44, v99\n\t"
    "v_add_f32 v96, 0x3727c5ac, v96\n\t"
    "v_mul_f32_e64 v224, -v224, v226\n\t"
    "s_waitcnt lgkmcnt(1)\n\t"
    "v_fmamk_f32 v225, v225, 0x3c2aaaab, v98\n\t"
    "v_rsq_f32 v96, v96\n\t"
    "v_max_f32 v225, 0, v225\n\t"
    "v_fma_f32 v80, v80, v226, v224\n\t"
    "v_add_f32 v225, 0x3727c5ac, v225\n\t"
    "v_fma_f32 v81, v81, v226, v224\n\t"
    "v_fma_f32 v82, v82, v226, v224\n\t"
    "v_fma_f32 v83, v83, v226, v224\n\t"
    "v_fma_f32 v48, v48, v226, v224\n\t"
    "v_fma_f32 v49, v49, v226, v224\n\t"
    "v_fma_f32 v50, v50, v226, v224\n\t"
    "v_fma_f32 v51, v51, v226, v224\n\t"
    "v_mul_f32 v129, 0x3f0aec44, v96\n\t"
    "v_rsq_f32 v225, v225\n\t"
    "v_cvt_pk_bf16_f32 v100, v80, v81\n\t"
    "v_cvt_pk_bf16_f32 v101, v82, v83\n\t"
    "v_cvt_pk_bf16_f32 v102, v48, v49\n\t"
    "v_cvt_pk_bf16_f32 v103, v50, v51\n\t"
    "v_mul_f32_e64 v130, -v97, v129\n\t"
    "v_mul_f32 v131, 0x3f0aec44, v225\n\t"
    "v_mfma_f32_16x16x32_bf16 v[48:51], v[116:119], v[100:103], 0\n\t"
    "v_fma_f32 v60, v60, v129, v130\n\t"
    "v_fma_f32 v61, v61, v129, v130\n\t"
    "v_fma_f32 v62, v62, v129, v130\n\t"
    "v_fma_f32 v63, v63, v129, v130\n\t"
    "v_fma_f32 v44, v44, v129, v130\n\t"
    "v_fma_f32 v45, v45, v129, v130\n\t"
    "v_fma_f32 v46, v46, v129, v130\n\t"
    "v_fma_f32 v47, v47, v129, v130\n\t"
    "v_cvt_pk_bf16_f32 v96, v60, v61\n\t"
    "v_cvt_pk_bf16_f32 v97, v62, v63\n\t"
    "v_cvt_pk_bf16_f32 v98, v44, v45\n\t"
    "v_cvt_pk_bf16_f32 v99, v46, v47\n\t"
    "v_mul_f32_e64 v225, -v227, v131\n\t"
    "v_fma_f32 v64, v64, v226, v224\n\t"
    "v_mfma_f32_16x16x32_bf16 v[60:63], v[116:119], v[96:99], 0\n\t"
    "v_fma_f32 v56, v56, v131, v225\n\t"
    "v_fma_f32 v57, v57, v131, v225\n\t"
    "v_fma_f32 v58, v58, v131, v225\n\t"
    "v_fma_f32 v59, v59, v131, v225\n\t"
    "v_fma_f32 v40, v40, v131, v225\n\t"
    "v_fma_f32 v41, v41, v131, v225\n\t"
    "v_fma_f32 v42, v42, v131, v225\n\t"
    "v_fma_f32 v43, v43, v131, v225\n\t"
    "v_cvt_pk_bf16_f32 v92, v56, v57\n\t"
    "v_cvt_pk_bf16_f32 v93, v58, v59\n\t"
    "v_cvt_pk_bf16_f32 v94, v40, v41\n\t"
    "v_cvt_pk_bf16_f32 v95, v42, v43\n\t"
    "v_fma_f32 v65, v65, v226, v224\n\t"
    "v_fma_f32 v66, v66, v226, v224\n\t"
    "v_mfma_f32_16x16x32_bf16 v[56:59], v[116:119], v[92:95], 0\n\t"
    "ds_read_b128 v[228:231], v251 offset:5120\n\t"
    "v_fma_f32 v67, v67, v226, v224\n\t"
    "v_fma_f32 v84, v84, v226, v224\n\t"
    "v_fma_f32 v85, v85, v226, v224\n\t"
    "v_fma_f32 v86, v86, v226, v224\n\t"
    "v_fma_f32 v87, v87, v226, v224\n\t"
    "v_cvt_pk_bf16_f32 v88, v64, v65\n\t"
    "v_cvt_pk_bf16_f32 v89, v66, v67\n\t"
    "v_cvt_pk_bf16_f32 v90, v84, v85\n\t"
    "v_cvt_pk_bf16_f32 v91, v86, v87\n\t"
    "v_fma_f32 v52, v52, v129, v130\n\t"
    "v_fma_f32 v53, v53, v129, v130\n\t"
    "v_mfma_f32_16x16x32_bf16 v[48:51], v[236:239], v[88:91], v[48:51]\n\t"
    "v_fma_f32 v54, v54, v129, v130\n\t"
    "v_fma_f32 v55, v55, v129, v130\n\t"
    "v_fma_f32 v136, v136, v129, v130\n\t"
    "v_fma_f32 v137, v137, v129, v130\n\t"
    "v_fma_f32 v138, v138, v129, v130\n\t"
    "v_fma_f32 v139, v139, v129, v130\n\t"
    "v_cvt_pk_bf16_f32 v84, v52, v53\n\t"
    "v_cvt_pk_bf16_f32 v85, v54, v55\n\t"
    "v_cvt_pk_bf16_f32 v86, v136, v137\n\t"
    "v_cvt_pk_bf16_f32 v87, v138, v139\n\t"
    "v_fma_f32 v12, v12, v131, v225\n\t"
    "v_fma_f32 v13, v13, v131, v225\n\t"
    "v_mfma_f32_16x16x32_bf16 v[60:63], v[236:239], v[84:87], v[60:63]\n\t"
    "v_fma_f32 v14, v14, v131, v225\n\t"
    "v_fma_f32 v15, v15, v131, v225\n\t"
    "v_fma_f32 v132, v132, v131, v225\n\t"
    "v_fma_f32 v133, v133, v131, v225\n\t"
    "v_fma_f32 v134, v134, v131, v225\n\t"
    "v_fma_f32 v135, v135, v131, v225\n\t"
    "v_cvt_pk_bf16_f32 v80, v12, v13\n\t"
    "v_cvt_pk_bf16_f32 v81, v14, v15\n\t"
    "v_cvt_pk_bf16_f32 v82, v132, v133\n\t"
    "v_cvt_pk_bf16_f32 v83, v134, v135\n\t"
    "v_fma_f32 v160, v160, v226, v224\n\t"
    "v_fma_f32 v161, v161, v226, v224\n\t"
    "v_mfma_f32_16x16x32_bf16 v[56:59], v[236:239], v[80:83], v[56:59]\n\t"
    "ds_read_b128 v[120:123], v251 offset:6144\n\t"
    "v_fma_f32 v162, v162, v226, v224\n\t"
    "v_fma_f32 v163, v163, v226, v224\n\t"
    "v_fma_f32 v172, v172, v226, v224\n\t"
    "v_fma_f32 v173, v173, v226, v224\n\t"
    "v_fma_f32 v174, v174, v226, v224\n\t"
    "v_fma_f32 v175, v175, v226, v224\n\t"
    "v_cvt_pk_bf16_f32 v76, v160, v161\n\t"
    "v_cvt_pk_bf16_f32 v77, v162, v163\n\t"
    "v_cvt_pk_bf16_f32 v78, v172, v173\n\t"
    "v_cvt_pk_bf16_f32 v79, v174, v175\n\t"
    "v_fma_f32 v156, v156, v129, v130\n\t"
    "v_fma_f32 v157, v157, v129, v130\n\t"
    "v_mfma_f32_16x16x32_bf16 v[48:51], v[104:107], v[76:79], v[48:51]\n\t"
    "v_fma_f32 v158, v158, v129, v130\n\t"
    "v_fma_f32 v159, v159, v129, v130\n\t"
    "v_fma_f32 v168, v168, v129, v130\n\t"
    "v_fma_f32 v169, v169, v129, v130\n\t"
    "v_fma_f32 v170, v170, v129, v130\n\t"
    "v_fma_f32 v171, v171, v129, v130\n\t"
    "v_cvt_pk_bf16_f32 v72, v156, v157\n\t"
    "v_cvt_pk_bf16_f32 v73, v158, v159\n\t"
    "v_cvt_pk_bf16_f32 v74, v168, v169\n\t"
    "v_cvt_pk_bf16_f32 v75, v170, v171\n\t"
    "v_fma_f32 v148, v148, v131, v225\n\t"
    "v_fma_f32 v149, v149, v131, v225\n\t"
    "v_mfma_f32_16x16x32_bf16 v[60:63], v[104:107], v[72:75], v[60:63]\n\t"
    "v_fma_f32 v150, v150, v131, v225\n\t"
    "v_fma_f32 v151, v151, v131, v225\n\t"
    "v_fma_f32 v164, v164, v131, v225\n\t"
    "v_fma_f32 v165, v165, v131, v225\n\t"
    "v_fma_f32 v166, v166, v131, v225\n\t"
    "v_fma_f32 v167, v167, v131, v225\n\t"
    "v_cvt_pk_bf16_f32 v68, v148, v149\n\t"
    "v_cvt_pk_bf16_f32 v69, v150, v151\n\t"
    "v_cvt_pk_bf16_f32 v70, v164, v165\n\t"
    "v_cvt_pk_bf16_f32 v71, v166, v167\n\t"
    "v_cvt_pk_bf16_f32 v44, v48, v49\n\t"
    "v_cvt_pk_bf16_f32 v45, v50, v51\n\t"
    "v_mfma_f32_16x16x32_bf16 v[56:59], v[104:107], v[68:71], v[56:59]\n\t"
    "ds_read_b128 v[124:127], v251 offset:7168\n\t"
    "v_cvt_pk_bf16_f32 v48, v60, v61\n\t"
    "v_cvt_pk_bf16_f32 v49, v62, v63\n\t"
    "v_mfma_f32_16x16x32_bf16 v[108:111], v[240:243], v[100:103], 0\n\t"
    "v_mov_b32 v47, 0\n\t"
    "v_mov_b32 v51, 0\n\t"
    "v_mfma_f32_16x16x32_bf16 v[104:107], v[240:243], v[96:99], 0\n\t"
    "v_mov_b32 v43, 0\n\t"
    "v_cvt_pk_bf16_f32 v40, v56, v57\n\t"
    "v_mfma_f32_16x16x32_bf16 v[12:15], v[240:243], v[92:95], 0\n\t"
    "ds_read_b128 v[144:147], v251 offset:8192\n\t"
    "v_cvt_pk_bf16_f32 v41, v58, v59\n\t"
    "v_mov_b32 v59, 0\n\t"
    "s_waitcnt lgkmcnt(4)\n\t"
    "v_mfma_f32_16x16x32_bf16 v[108:111], v[232:235], v[88:91], v[108:111]\n\t"
    "v_mov_b32 v63, 0\n\t"
    "v_mov_b32 v55, 0\n\t"
    "v_mfma_f32_16x16x32_bf16 v[104:107], v[232:235], v[84:87], v[104:107]\n\t"
    "v_mov_b32 v67, 0\n\t"
    "v_mov_b32 v135, 0\n\t"
    "v_mfma_f32_16x16x32_bf16 v[12:15], v[232:235], v[80:83], v[12:15]\n\t"
    "ds_read_b128 v[148:151], v251 offset:9216\n\t"
    "v_mov_b32 v143, 0\n\t"
    "v_mov_b32 v139, 0\n\t"
    "s_waitcnt lgkmcnt(4)\n\t"
    "v_mfma_f32_16x16x32_bf16 v[108:111], v[228:231], v[76:79], v[108:111]\n\t"
    "v_mfma_f32_16x16x32_bf16 v[104:107], v[228:231], v[72:75], v[104:107]\n\t"
    "v_mfma_f32_16x16x32_bf16 v[12:15], v[228:231], v[68:71], v[12:15]\n\t"
    "ds_read_b128 v[152:155], v251 offset:10240\n\t"
    "s_waitcnt lgkmcnt(4)\n\t"
    "v_mfma_f32_16x16x32_bf16 v[112:115], v[120:123], v[100:103], 0\n\t"
    "s_nop 2\n\t"
    "v_cvt_pk_bf16_f32 v46, v110, v111\n\t"
    "v_mfma_f32_16x16x32_bf16 v[116:119], v[120:123], v[96:99], 0\n\t"
    "v_cvt_pk_bf16_f32 v58, v108, v109\n\t"
    "v_cvt_pk_bf16_f32 v50, v106, v107\n\t"
    "v_mfma_f32_16x16x32_bf16 v[108:111], v[120:123], v[92:95], 0\n\t"
    "ds_read_b128 v[156:159], v251 offset:11264\n\t"
    "v_cvt_pk_bf16_f32 v62, v104, v105\n\t"
    "v_cvt_pk_bf16_f32 v42, v14, v15\n\t"
    "s_waitcnt lgkmcnt(4)\n\t"
    "v_mfma_f32_16x16x32_bf16 v[112:115], v[124:127], v[88:91], v[112:115]\n\t"
    "v_cvt_pk_bf16_f32 v54, v12, v13\n\t"
    "v_mfma_f32_16x16x32_bf16 v[116:119], v[124:127], v[84:87], v[116:119]\n\t"
    "v_mfma_f32_16x16x32_bf16 v[108:111], v[124:127], v[80:83], v[108:111]\n\t"
    "ds_read_b128 v[240:243], v251 offset:49152\n\t"
    "s_waitcnt lgkmcnt(4)\n\t"
    "v_mfma_f32_16x16x32_bf16 v[112:115], v[144:147], v[76:79], v[112:115]\n\t"
    "v_mfma_f32_16x16x32_bf16 v[116:119], v[144:147], v[72:75], v[116:119]\n\t"
    "v_mfma_f32_16x16x32_bf16 v[108:111], v[144:147], v[68:71], v[108:111]\n\t"
    "ds_read_b128 v[124:127], v251 offset:50176\n\t"
    "s_waitcnt lgkmcnt(4)\n\t"
    "v_mfma_f32_16x16x32_bf16 v[12:15], v[100:103], v[148:151], 0\n\t"
    "s_nop 2\n\t"
    "v_cvt_pk_bf16_f32 v56, v112, v113\n\t"
    "v_mfma_f32_16x16x32_bf16 v[120:123], v[96:99], v[148:151], 0\n\t"
    "v_cvt_pk_bf16_f32 v57, v114, v115\n\t"
    "v_cvt_pk_bf16_f32 v60, v116, v117\n\t"
    "v_mfma_f32_16x16x32_bf16 v[112:115], v[92:95], v[148:151], 0\n\t"
    "ds_read_b128 v[144:147], v251 offset:51200\n\t"
    "v_cvt_pk_bf16_f32 v61, v118, v119\n\t"
    "v_cvt_pk_bf16_f32 v52, v108, v109\n\t"
    "s_waitcnt lgkmcnt(4)\n\t"
    "v_mfma_f32_16x16x32_bf16 v[12:15], v[88:91], v[152:155], v[12:15]\n\t"
    "v_cvt_pk_bf16_f32 v53, v110, v111\n\t"
    "v_mfma_f32_16x16x32_bf16 v[120:123], v[84:87], v[152:155], v[120:123]\n\t"
    "v_mfma_f32_16x16x32_bf16 v[112:115], v[80:83], v[152:155], v[112:115]\n\t"
    "s_waitcnt lgkmcnt(3)\n\t"
    "v_mfma_f32_16x16x32_bf16 v[12:15], v[76:79], v[156:159], v[12:15]\n\t"
    "v_mfma_f32_16x16x32_bf16 v[120:123], v[72:75], v[156:159], v[120:123]\n\t"
    "v_mfma_f32_16x16x32_bf16 v[112:115], v[68:71], v[156:159], v[112:115]\n\t"
    "s_waitcnt lgkmcnt(2)\n\t"
    "v_mfma_f32_16x16x32_bf16 v[104:107], v[100:103], v[240:243], 0\n\t"
    "v_mfma_f32_16x16x32_bf16 v[108:111], v[96:99], v[240:243], 0\n\t"
    "s_nop 2\n\t"
    "v_cvt_pk_bf16_f32 v12, v12, v13\n\t"
    "v_cvt_pk_bf16_f32 v13, v14, v15\n\t"
    "v_mfma_f32_16x16x32_bf16 v[116:119], v[92:95], v[240:243], 0\n\t"
    "v_cvt_pk_bf16_f32 v14, v120, v121\n\t"
    "v_cvt_pk_bf16_f32 v15, v122, v123\n\t"
    "s_waitcnt lgkmcnt(1)\n\t"
    "v_mfma_f32_16x16x32_bf16 v[104:107], v[88:91], v[124:127], v[104:107]\n\t"
    "v_cvt_pk_bf16_f32 v64, v112, v113\n\t"
    "v_cvt_pk_bf16_f32 v65, v114, v115\n\t"
    "v_mfma_f32_16x16x32_bf16 v[108:111], v[84:87], v[124:127], v[108:111]\n\t"
    "v_mfma_f32_16x16x32_bf16 v[116:119], v[80:83], v[124:127], v[116:119]\n\t"
    "s_waitcnt lgkmcnt(0)\n\t"
    "v_mfma_f32_16x16x32_bf16 v[104:107], v[76:79], v[144:147], v[104:107]\n\t"
    "v_mfma_f32_16x16x32_bf16 v[108:111], v[72:75], v[144:147], v[108:111]\n\t"
    "v_mfma_f32_16x16x32_bf16 v[116:119], v[68:71], v[144:147], v[116:119]\n\t"
    "s_nop 5\n\t"
    "v_cvt_pk_bf16_f32 v172, v104, v105\n\t"
    "v_cvt_pk_bf16_f32 v173, v106, v107\n\t"
    "v_cvt_pk_bf16_f32 v146, v108, v109\n\t"
    "v_cvt_pk_bf16_f32 v147, v110, v111\n\t"
    "v_cvt_pk_bf16_f32 v130, v116, v117\n\t"
    "v_cvt_pk_bf16_f32 v131, v118, v119\n\t"
    "s_mov_b32 s44, s45\n\t"
    "s_mov_b32 s45, s46\n\t"
    "s_mov_b32 s46, s47\n\t"
    "s_mov_b32 s48, s49\n\t"
    "s_mov_b32 s49, s50\n\t"
    "s_mov_b32 s50, 16\n\t"
    "s_sub_u32 s51, s51, 1\n\t"
    "s_add_u32 s54, s54, 1\n\t"
    "s_add_u32 s53, s53, 7680\n\t"
    "s_branch .Lw_r1_%=\n\t"
    ".Lw_spin_%=:\n\t"
    "s_min_u32 s66, s54, 1\n\t"
    "s_lshl_b32 s66, s66, 2\n\t"
    "s_add_u32 s66, s66, 160512\n\t"
    "v_mov_b32 v144, s66\n\t"
    ".Lw_spinloop_%=:\n\t"
    "ds_read_b32 v174, v144\n\t"
    "s_waitcnt lgkmcnt(0)\n\t"
    "v_readfirstlane_b32 s66, v174\n\t"
    "s_nop 3\n\t"
    "s_cmp_ge_u32 s66, 1\n\t"
    "s_cbranch_scc1 .Lw_spinback_%=\n\t"
    "s_sleep 2\n\t"
    "s_branch .Lw_spinloop_%=\n\t"
    ".Lw_stub1_%=:\n\t"
    "v_mov_b32 v98, 0x80000000\n\t"
    "s_cmp_eq_u32 s48, 16\n\t"
    "s_cbranch_scc1 .Lw_stuball1_%=\n\t"
    "v_lshrrev_b32 v191, 0, v218\n\t"
    "v_and_b32 v191, s48, v191\n\t"
    "v_cmp_ne_u32 vcc, 0, v191\n\t"
    "s_nop 1\n\t"
    "v_cndmask_b32 v212, v212, v98, vcc\n\t"
    "v_lshrrev_b32 v191, 4, v218\n\t"
    "v_and_b32 v191, s48, v191\n\t"
    "v_cmp_ne_u32 vcc, 0, v191\n\t"
    "s_nop 1\n\t"
    "v_cndmask_b32 v213, v213, v98, vcc\n\t"
    "v_lshrrev_b32 v191, 8, v218\n\t"
    "v_and_b32 v191, s48, v191\n\t"
    "v_cmp_ne_u32 vcc, 0, v191\n\t"
    "s_nop 1\n\t"
    "v_cndmask_b32 v214, v214, v98, vcc\n\t"
    "v_lshrrev_b32 v191, 12, v218\n\t"
    "v_and_b32 v191, s48, v191\n\t"
    "v_cmp_ne_u32 vcc, 0, v191\n\t"
    "s_nop 1\n\t"
    "v_cndmask_b32 v215, v215, v98, vcc\n\t"
    "v_lshrrev_b32 v191, 16, v218\n\t"
    "v_and_b32 v191, s48, v191\n\t"
    "v_cmp_ne_u32 vcc, 0, v191\n\t"
    "s_nop 1\n\t"
    "v_cndmask_b32 v216, v216, v98, vcc\n\t"
    "v_lshrrev_b32 v191, 20, v218\n\t"
    "v_and_b32 v191, s48, v191\n\t"
    "v_cmp_ne_u32 vcc, 0, v191\n\t"
    "s_nop 1\n\t"
    "v_cndmask_b32 v217, v217, v98, vcc\n\t"
    "s_branch .Lw_back1_%=\n\t"
    ".Lw_stuball1_%=:\n\t"
    "v_mov_b32 v212, v98\n\t"
    "v_mov_b32 v213, v98\n\t"
    "v_mov_b32 v214, v98\n\t"
    "v_mov_b32 v215, v98\n\t"
    "v_mov_b32 v216, v98\n\t"
    "v_mov_b32 v217, v98\n\t"
    "s_branch .Lw_back1_%=\n\t"
    ".Lw_stub2_%=:\n\t"
    "v_mov_b32 v156, 0x80000000\n\t"
    "s_cmp_eq_u32 s48, 16\n\t"
    "s_cbranch_scc1 .Lw_stuball2_%=\n\t"
    "v_lshrrev_b32 v157, 0, v218\n\t"
    "v_and_b32 v157, s48, v157\n\t"
    "v_cmp_ne_u32 vcc, 0, v157\n\t"
    "s_nop 1\n\t"
    "v_cndmask_b32 v220, v220, v156, vcc\n\t"
    "v_lshrrev_b32 v157, 4, v218\n\t"
    "v_and_b32 v157, s48, v157\n\t"
    "v_cmp_ne_u32 vcc, 0, v157\n\t"
    "s_nop 1\n\t"
    "v_cndmask_b32 v221, v221, v156, vcc\n\t"
    "v_lshrrev_b32 v157, 8, v218\n\t"
    "v_and_b32 v157, s48, v157\n\t"
    "v_cmp_ne_u32 vcc, 0, v157\n\t"
    "s_nop 1\n\t"
    "v_cndmask_b32 v222, v222, v156, vcc\n\t"
    "v_lshrrev_b32 v157, 12, v218\n\t"
    "v_and_b32 v157, s48, v157\n\t"
    "v_cmp_ne_u32 vcc, 0, v157\n\t"
    "s_nop 1\n\t"
    "v_cndmask_b32 v223, v223, v156, vcc\n\t"
    "v_lshrrev_b32 v157, 16, v218\n\t"
    "v_and_b32 v157, s48, v157\n\t"
    "v_cmp_ne_u32 vcc, 0, v157\n\t"
    "s_nop 1\n\t"
    "v_cndmask_b32 v216, v216, v156, vcc\n\t"
    "v_lshrrev_b32 v157, 20, v218\n\t"
    "v_and_b32 v157, s48, v157\n\t"
    "v_cmp_ne_u32 vcc, 0, v157\n\t"
    "s_nop 1\n\t"
    "v_cndmask_b32 v217, v217, v156, vcc\n\t"
    "s_branch .Lw_back2_%=\n\t"
    ".Lw_stuball2_%=:\n\t"
    "v_mov_b32 v220, v156\n\t"
    "v_mov_b32 v221, v156\n\t"
    "v_mov_b32 v222, v156\n\t"
    "v_mov_b32 v223, v156\n\t"
    "v_mov_b32 v216, v156\n\t"
    "v_mov_b32 v217, v156\n\t"
    "s_branch .Lw_back2_%=\n\t"
    ".Lw_stub3_%=:\n\t"
    "v_mov_b32 v177, 0x80000000\n\t"
    "s_cmp_eq_u32 s49, 16\n\t"
    "s_cbranch_scc1 .Lw_stuball3_%=\n\t"
    "v_lshrrev_b32 v176, 0, v234\n\t"
    "v_and_b32 v176, s49, v176\n\t"
    "v_cmp_ne_u32 vcc, 0, v176\n\t"
    "s_nop 1\n\t"
    "v_cndmask_b32 v192, v192, v177, vcc\n\t"
    "v_lshrrev_b32 v176, 4, v234\n\t"
    "v_and_b32 v176, s49, v176\n\t"
    "v_cmp_ne_u32 vcc, 0, v176\n\t"
    "s_nop 1\n\t"
    "v_cndmask_b32 v193, v193, v177, vcc\n\t"
    "v_lshrrev_b32 v176, 8, v234\n\t"
    "v_and_b32 v176, s49, v176\n\t"
    "v_cmp_ne_u32 vcc, 0, v176\n\t"
    "s_nop 1\n\t"
    "v_cndmask_b32 v194, v194, v177, vcc\n\t"
    "v_lshrrev_b32 v176, 12, v234\n\t"
    "v_and_b32 v176, s49, v176\n\t"
    "v_cmp_ne_u32 vcc, 0, v176\n\t"
    "s_nop 1\n\t"
    "v_cndmask_b32 v195, v195, v177, vcc\n\t"
    "v_lshrrev_b32 v176, 16, v234\n\t"
    "v_and_b32 v176, s49, v176\n\t"
    "v_cmp_ne_u32 vcc, 0, v176\n\t"
    "s_nop 1\n\t"
    "v_cndmask_b32 v232, v232, v177, vcc\n\t"
    "v_lshrrev_b32 v176, 20, v234\n\t"
    "v_and_b32 v176, s49, v176\n\t"
    "v_cmp_ne_u32 vcc, 0, v176\n\t"
    "s_nop 1\n\t"
    "v_cndmask_b32 v233, v233, v177, vcc\n\t"
    "v_mov_b32 v235, v177\n\t"
    "v_mov_b32 v72, v177\n\t"
    "s_branch .Lw_back3_%=\n\t"
    ".Lw_stuball3_%=:\n\t"
    "v_mov_b32 v192, v177\n\t"
    "v_mov_b32 v193, v177\n\t"
    "v_mov_b32 v194, v177\n\t"
    "v_mov_b32 v195, v177\n\t"
    "v_mov_b32 v232, v177\n\t"
    "v_mov_b32 v233, v177\n\t"
    "v_mov_b32 v235, v177\n\t"
    "v_mov_b32 v72, v177\n\t"
    "s_branch .Lw_back3_%=\n\t"
    ".Lw_sp4_%=:\n\t"
    "v_mov_b32 v9, 160536\n\t"
    ".Lw_spl4_%=:\n\t"
    "ds_read_b32 v8, v9\n\t"
    "s_waitcnt lgkmcnt(0)\n\t"
    "v_readfirstlane_b32 s66, v8\n\t"
    "s_nop 3\n\t"
    "s_cmp_ge_u32 s66, s61\n\t"
    "s_cbranch_scc1 .Lw_spb4_%=\n\t"
    "s_sleep 1\n\t"
    "s_branch .Lw_spl4_%=\n\t"
    ".Lw_stub5_%=:\n\t"
    "v_mov_b32 v9, 0x80000000\n\t"
    "s_cmp_eq_u32 s48, 16\n\t"
    "s_cbranch_scc1 .Lw_stuball5_%=\n\t"
    "v_lshrrev_b32 v8, 0, v16\n\t"
    "v_and_b32 v8, s48, v8\n\t"
    "v_cmp_ne_u32 vcc, 0, v8\n\t"
    "s_nop 1\n\t"
    "v_cndmask_b32 v10, v10, v9, vcc\n\t"
    "v_lshrrev_b32 v8, 4, v16\n\t"
    "v_and_b32 v8, s48, v8\n\t"
    "v_cmp_ne_u32 vcc, 0, v8\n\t"
    "s_nop 1\n\t"
    "v_cndmask_b32 v11, v11, v9, vcc\n\t"
    "v_lshrrev_b32 v8, 8, v16\n\t"
    "v_and_b32 v8, s48, v8\n\t"
    "v_cmp_ne_u32 vcc, 0, v8\n\t"
    "s_nop 1\n\t"
    "v_cndmask_b32 v12, v12, v9, vcc\n\t"
    "v_lshrrev_b32 v8, 12, v16\n\t"
    "v_and_b32 v8, s48, v8\n\t"
    "v_cmp_ne_u32 vcc, 0, v8\n\t"
    "s_nop 1\n\t"
    "v_cndmask_b32 v13, v13, v9, vcc\n\t"
    "v_lshrrev_b32 v8, 16, v16\n\t"
    "v_and_b32 v8, s48, v8\n\t"
    "v_cmp_ne_u32 vcc, 0, v8\n\t"
    "s_nop 1\n\t"
    "v_cndmask_b32 v14, v14, v9, vcc\n\t"
    "v_lshrrev_b32 v8, 20, v16\n\t"
    "v_and_b32 v8, s48, v8\n\t"
    "v_cmp_ne_u32 vcc, 0, v8\n\t"
    "s_nop 1\n\t"
    "v_cndmask_b32 v15, v15, v9, vcc\n\t"
    "s_branch .Lw_back5_%=\n\t"
    ".Lw_stuball5_%=:\n\t"
    "v_mov_b32 v10, v9\n\t"
    "v_mov_b32 v11, v9\n\t"
    "v_mov_b32 v12, v9\n\t"
    "v_mov_b32 v13, v9\n\t"
    "v_mov_b32 v14, v9\n\t"
    "v_mov_b32 v15, v9\n\t"
    "s_branch .Lw_back5_%=\n\t"
    ".Lw_stub6_%=:\n\t"
    "v_mov_b32 v3, 0x80000000\n\t"
    "s_cmp_eq_u32 s49, 16\n\t"
    "s_cbranch_scc1 .Lw_stuball6_%=\n\t"
    "v_lshrrev_b32 v2, 0, v222\n\t"
    "v_and_b32 v2, s49, v2\n\t"
    "v_cmp_ne_u32 vcc, 0, v2\n\t"
    "s_nop 1\n\t"
    "v_cndmask_b32 v104, v104, v3, vcc\n\t"
    "v_lshrrev_b32 v2, 4, v222\n\t"
    "v_and_b32 v2, s49, v2\n\t"
    "v_cmp_ne_u32 vcc, 0, v2\n\t"
    "s_nop 1\n\t"
    "v_cndmask_b32 v105, v105, v3, vcc\n\t"
    "v_lshrrev_b32 v2, 8, v222\n\t"
    "v_and_b32 v2, s49, v2\n\t"
    "v_cmp_ne_u32 vcc, 0, v2\n\t"
    "s_nop 1\n\t"
    "v_cndmask_b32 v106, v106, v3, vcc\n\t"
    "v_lshrrev_b32 v2, 12, v222\n\t"
    "v_and_b32 v2, s49, v2\n\t"
    "v_cmp_ne_u32 vcc, 0, v2\n\t"
    "s_nop 1\n\t"
    "v_cndmask_b32 v107, v107, v3, vcc\n\t"
    "v_lshrrev_b32 v2, 16, v222\n\t"
    "v_and_b32 v2, s49, v2\n\t"
    "v_cmp_ne_u32 vcc, 0, v2\n\t"
    "s_nop 1\n\t"
    "v_cndmask_b32 v220, v220, v3, vcc\n\t"
    "v_lshrrev_b32 v2, 20, v222\n\t"
    "v_and_b32 v2, s49, v2\n\t"
    "v_cmp_ne_u32 vcc, 0, v2\n\t"
    "s_nop 1\n\t"
    "v_cndmask_b32 v221, v221, v3, vcc\n\t"
    "v_mov_b32 v223, v3\n\t"
    "v_mov_b32 v72, v3\n\t"
    "s_branch .Lw_back6_%=\n\t"
    ".Lw_stuball6_%=:\n\t"
    "v_mov_b32 v104, v3\n\t"
    "v_mov_b32 v105, v3\n\t"
    "v_mov_b32 v106, v3\n\t"
    "v_mov_b32 v107, v3\n\t"
    "v_mov_b32 v220, v3\n\t"
    "v_mov_b32 v221, v3\n\t"
    "v_mov_b32 v223, v3\n\t"
    "v_mov_b32 v72, v3\n\t"
    "s_branch .Lw_back6_%=\n\t"
    ".Lw_sp7_%=:\n\t"
    "v_mov_b32 v3, 160532\n\t"
    ".Lw_spl7_%=:\n\t"
    "ds_read_b32 v2, v3\n\t"
    "s_waitcnt lgkmcnt(0)\n\t"
    "v_readfirstlane_b32 s66, v2\n\t"
    "s_nop 3\n\t"
    "s_cmp_ge_u32 s66, 3\n\t"
    "s_cbranch_scc1 .Lw_spb7_%=\n\t"
    "s_sleep 1\n\t"
    "s_branch .Lw_spl7_%=\n\t"
    ".Lw_sp8_%=:\n\t"
    "v_mov_b32 v18, 160536\n\t"
    ".Lw_spl8_%=:\n\t"
    "ds_read_b32 v9, v18\n\t"
    "s_waitcnt lgkmcnt(0)\n\t"
    "v_readfirstlane_b32 s66, v9\n\t"
    "s_nop 3\n\t"
    "s_cmp_ge_u32 s66, s61\n\t"
    "s_cbranch_scc1 .Lw_spb8_%=\n\t"
    "s_sleep 1\n\t"
    "s_branch .Lw_spl8_%=\n\t"
    ".Lw_sp9_%=:\n\t"
    "v_mov_b32 v27, 160532\n\t"
    ".Lw_spl9_%=:\n\t"
    "ds_read_b32 v9, v27\n\t"
    "s_waitcnt lgkmcnt(0)\n\t"
    "v_readfirstlane_b32 s66, v9\n\t"
    "s_nop 3\n\t"
    "s_cmp_ge_u32 s66, 3\n\t"
    "s_cbranch_scc1 .Lw_spb9_%=\n\t"
    "s_sleep 1\n\t"
    "s_branch .Lw_spl9_%=\n\t"
    ".Lw_lt_%=:\n\t"
    "s_bitcmp1_b32 s28, 3\n\t"
    "s_cbranch_scc1 .Lw_ltb_%=\n\t"
    "s_setprio 2\n\t"
    ".Lw_ltb_%=:\n\t"
    "s_bfe_u32 s61, s28, 0x30008\n\t"
    "s_cmp_lg_u32 s61, 0\n\t"
    "s_cbranch_scc1 .Lw_sp8_%=\n\t"
    ".Lw_spb8_%=:\n\t"
    "buffer_load_dwordx4 v[20:23], v0, s[36:39], 0 offen offset:0\n\t"
    "buffer_load_dwordx4 v[24:27], v0, s[36:39], 0 offen offset:32\n\t"
    "buffer_load_dwordx4 v[28:31], v0, s[36:39], 0 offen offset:64\n\t"
    "buffer_load_dwordx4 v[240:243], v0, s[36:39], 0 offen offset:96\n\t"
    "buffer_load_dwordx4 v[32:35], v0, s[36:39], 0 offen offset:128\n\t"
    "buffer_load_dwordx4 v[36:39], v0, s[36:39], 0 offen offset:160\n\t"
    "buffer_load_dwordx4 v[40:43], v0, s[36:39], 0 offen offset:192\n\t"
    "buffer_load_dwordx4 v[236:239], v0, s[36:39], 0 offen offset:224\n\t"
    "buffer_load_dwordx4 v[44:47], v0, s[36:39], 0 offen offset:256\n\t"
    "buffer_load_dwordx4 v[48:51], v0, s[36:39], 0 offen offset:288\n\t"
    "buffer_load_dwordx4 v[52:55], v0, s[36:39], 0 offen offset:320\n\t"
    "buffer_load_dwordx4 v[232:235], v0, s[36:39], 0 offen offset:352\n\t"
    "buffer_load_dwordx4 v[56:59], v0, s[36:39], 0 offen offset:384\n\t"
    "buffer_load_dwordx4 v[60:63], v0, s[36:39], 0 offen offset:416\n\t"
    "buffer_load_dwordx4 v[228:231], v0, s[36:39], 0 offen offset:448\n\t"
    "buffer_load_dwordx4 v[64:67], v0, s[36:39], 0 offen offset:480\n\t"
    "buffer_load_dwordx4 v[68:71], v0, s[36:39], 0 offen offset:512\n\t"
    "buffer_load_dwordx4 v[72:75], v0, s[36:39], 0 offen offset:544\n\t"
    "buffer_load_dwordx4 v[76:79], v1, s[36:39], 0 offen offset:0\n\t"
    "buffer_load_dwordx4 v[224:227], v1, s[36:39], 0 offen offset:32\n\t"
    "buffer_load_dwordx4 v[80:83], v1, s[36:39], 0 offen offset:64\n\t"
    "buffer_load_dwordx4 v[220:223], v1, s[36:39], 0 offen offset:96\n\t"
    "buffer_load_dwordx4 v[84:87], v1, s[36:39], 0 offen offset:128\n\t"
    "buffer_load_dwordx4 v[216:219], v1, s[36:39], 0 offen offset:160\n\t"
    "buffer_load_dwordx4 v[212:215], v2, s[36:39], 0 offen offset:0\n\t"
    "buffer_load_dwordx4 v[88:91], v2, s[36:39], 0 offen offset:32\n\t"
    "buffer_load_dwordx4 v[92:95], v2, s[36:39], 0 offen offset:64\n\t"
    "buffer_load_dwordx4 v[96:99], v2, s[36:39], 0 offen offset:96\n\t"
    "buffer_load_dwordx4 v[100:103], v2, s[36:39], 0 offen offset:128\n\t"
    "buffer_load_dwordx4 v[104:107], v2, s[36:39], 0 offen offset:160\n\t"
    "v_mov_b32 v9, 160536\n\t"
    "v_mov_b32 v18, 1\n\t"
    "s_mov_b64 exec, 1\n\t"
    "ds_add_u32 v9, v18\n\t"
    "s_mov_b64 exec, -1\n\t"
    "ds_read_b128 v[208:211], v250 offset:0\n\t"
    "ds_read_b128 v[108:111], v250 offset:2720\n\t"
    "ds_read_b128 v[112:115], v250 offset:5440\n\t"
    "ds_read_b128 v[204:207], v250 offset:8160\n\t"
    "ds_read_b128 v[116:119], v250 offset:10880\n\t"
    "ds_read_b128 v[200:203], v250 offset:13600\n\t"
    "ds_read_b128 v[196:199], v250 offset:544\n\t"
    "ds_read_b128 v[120:123], v250 offset:3264\n\t"
    "ds_read_b128 v[192:195], v250 offset:5984\n\t"
    "ds_read_b128 v[188:191], v250 offset:8704\n\t"
    "v_mov_b32 v187, 0\n\t"
    "v_mov_b32 v127, 0\n\t"
    "v_mov_b32 v135, 0\n\t"
    "v_mov_b32 v139, 0\n\t"
    "v_mov_b32 v143, 0\n\t"
    "v_mov_b32 v147, 0\n\t"
    "v_mov_b32 v151, 0\n\t"
    "v_mov_b32 v183, 0\n\t"
    "v_mov_b32 v128, 0x3f803f80\n\t"
    "v_mov_b32 v129, v128\n\t"
    "v_mov_b32 v130, v128\n\t"
    "v_mov_b32 v131, v128\n\t"
    "s_waitcnt lgkmcnt(0)\n\t"
    "s_waitcnt vmcnt(29)\n\t"
    "v_mfma_f32_16x16x32_bf16 v[176:179], v[208:211], v[20:23], 0\n\t"
    "ds_read_b128 v[20:23], v250 offset:11424\n\t"
    "s_waitcnt vmcnt(28)\n\t"
    "v_mfma_f32_16x16x32_bf16 v[208:211], v[108:111], v[24:27], 0\n\t"
    "ds_read_b128 v[24:27], v250 offset:14144\n\t"
    "s_waitcnt vmcnt(27)\n\t"
    "v_mfma_f32_16x16x32_bf16 v[172:175], v[112:115], v[28:31], 0\n\t"
    "ds_read_b128 v[28:31], v250 offset:1088\n\t"
    "s_waitcnt vmcnt(26)\n\t"
    "v_mfma_f32_16x16x32_bf16 v[108:111], v[204:207], v[240:243], 0\n\t"
    "ds_read_b128 v[112:115], v250 offset:3808\n\t"
    "s_waitcnt vmcnt(25)\n\t"
    "v_mfma_f32_16x16x32_bf16 v[152:155], v[116:119], v[32:35], 0\n\t"
    "ds_read_b128 v[32:35], v250 offset:6528\n\t"
    "s_waitcnt vmcnt(24)\n\t"
    "v_mfma_f32_16x16x32_bf16 v[240:243], v[200:203], v[36:39], 0\n\t"
    "ds_read_b128 v[36:39], v250 offset:9248\n\t"
    "s_waitcnt vmcnt(23)\n\t"
    "v_mfma_f32_16x16x32_bf16 v[176:179], v[196:199], v[40:43], v[176:179]\n\t"
    "ds_read_b128 v[204:207], v250 offset:11968\n\t"
    "s_waitcnt vmcnt(22)\n\t"
    "v_mfma_f32_16x16x32_bf16 v[208:211], v[120:123], v[236:239], v[208:211]\n\t"
    "ds_read_b128 v[40:43], v250 offset:14688\n\t"
    "s_waitcnt vmcnt(21)\n\t"
    "v_mfma_f32_16x16x32_bf16 v[172:175], v[192:195], v[44:47], v[172:175]\n\t"
    "ds_read_b128 v[44:47], v250 offset:1632\n\t"
    "s_waitcnt vmcnt(20)\n\t"
    "v_mfma_f32_16x16x32_bf16 v[108:111], v[188:191], v[48:51], v[108:111]\n\t"
    "ds_read_b128 v[48:51], v250 offset:4352\n\t"
    "s_waitcnt lgkmcnt(9)\n\t"
    "s_waitcnt vmcnt(19)\n\t"
    "v_mfma_f32_16x16x32_bf16 v[152:155], v[20:23], v[52:55], v[152:155]\n\t"
    "ds_read_b128 v[20:23], v250 offset:7072\n\t"
    "s_waitcnt lgkmcnt(9)\n\t"
    "s_waitcnt vmcnt(18)\n\t"
    "v_mfma_f32_16x16x32_bf16 v[240:243], v[24:27], v[232:235], v[240:243]\n\t"
    "ds_read_b128 v[24:27], v250 offset:9792\n\t"
    "s_waitcnt lgkmcnt(9)\n\t"
    "s_waitcnt vmcnt(17)\n\t"
    "v_mfma_f32_16x16x32_bf16 v[176:179], v[28:31], v[56:59], v[176:179]\n\t"
    "ds_read_b128 v[28:31], v250 offset:12512\n\t"
    "s_waitcnt lgkmcnt(9)\n\t"
    "s_waitcnt vmcnt(16)\n\t"
    "v_mfma_f32_16x16x32_bf16 v[208:211], v[112:115], v[60:63], v[208:211]\n\t"
    "ds_read_b128 v[52:55], v250 offset:15232\n\t"
    "s_waitcnt lgkmcnt(9)\n\t"
    "s_waitcnt vmcnt(15)\n\t"
    "v_mfma_f32_16x16x32_bf16 v[172:175], v[32:35], v[228:231], v[172:175]\n\t"
    "ds_read_b128 v[236:239], v250 offset:2176\n\t"
    "s_waitcnt lgkmcnt(9)\n\t"
    "s_waitcnt vmcnt(14)\n\t"
    "v_mfma_f32_16x16x32_bf16 v[108:111], v[36:39], v[64:67], v[108:111]\n\t"
    "ds_read_b128 v[32:35], v250 offset:4896\n\t"
    "s_waitcnt lgkmcnt(9)\n\t"
    "s_waitcnt vmcnt(13)\n\t"
    "v_mfma_f32_16x16x32_bf16 v[152:155], v[204:207], v[68:71], v[152:155]\n\t"
    "ds_read_b128 v[36:39], v250 offset:7616\n\t"
    "s_waitcnt lgkmcnt(9)\n\t"
    "s_waitcnt vmcnt(12)\n\t"
    "v_mfma_f32_16x16x32_bf16 v[240:243], v[40:43], v[72:75], v[240:243]\n\t"
    "ds_read_b128 v[232:235], v250 offset:10336\n\t"
    "s_waitcnt lgkmcnt(9)\n\t"
    "s_waitcnt vmcnt(11)\n\t"
    "v_mfma_f32_16x16x32_bf16 v[176:179], v[44:47], v[76:79], v[176:179]\n\t"
    "ds_read_b128 v[40:43], v250 offset:13056\n\t"
    "s_waitcnt lgkmcnt(9)\n\t"
    "s_waitcnt vmcnt(10)\n\t"
    "v_mfma_f32_16x16x32_bf16 v[208:211], v[48:51], v[224:227], v[208:211]\n\t"
    "ds_read_b128 v[44:47], v250 offset:15776\n\t"
    "s_waitcnt lgkmcnt(9)\n\t"
    "s_waitcnt vmcnt(9)\n\t"
    "v_mfma_f32_16x16x32_bf16 v[172:175], v[20:23], v[80:83], v[172:175]\n\t"
    "s_waitcnt lgkmcnt(8)\n\t"
    "s_waitcnt vmcnt(8)\n\t"
    "v_mfma_f32_16x16x32_bf16 v[108:111], v[24:27], v[220:223], v[108:111]\n\t"
    "s_waitcnt lgkmcnt(7)\n\t"
    "s_waitcnt vmcnt(7)\n\t"
    "v_mfma_f32_16x16x32_bf16 v[152:155], v[28:31], v[84:87], v[152:155]\n\t"
    "s_waitcnt lgkmcnt(6)\n\t"
    "s_waitcnt vmcnt(6)\n\t"
    "v_mfma_f32_16x16x32_bf16 v[240:243], v[52:55], v[216:219], v[240:243]\n\t"
    "s_waitcnt lgkmcnt(5)\n\t"
    "s_waitcnt vmcnt(5)\n\t"
    "v_mfma_f32_16x16x32_bf16 v[176:179], v[236:239], v[212:215], v[176:179]\n\t"
    "s_waitcnt lgkmcnt(4)\n\t"
    "s_waitcnt vmcnt(4)\n\t"
    "v_mfma_f32_16x16x32_bf16 v[208:211], v[32:35], v[88:91], v[208:211]\n\t"
    "s_waitcnt lgkmcnt(3)\n\t"
    "s_waitcnt vmcnt(3)\n\t"
    "v_mfma_f32_16x16x32_bf16 v[172:175], v[36:39], v[92:95], v[172:175]\n\t"
    "s_waitcnt lgkmcnt(2)\n\t"
    "s_waitcnt vmcnt(2)\n\t"
    "v_mfma_f32_16x16x32_bf16 v[108:111], v[232:235], v[96:99], v[108:111]\n\t"
    "s_waitcnt lgkmcnt(1)\n\t"
    "s_waitcnt vmcnt(1)\n\t"
    "v_mfma_f32_16x16x32_bf16 v[152:155], v[40:43], v[100:103], v[152:155]\n\t"
    "v_cvt_pk_bf16_f32 v20, v176, v177\n\t"
    "v_cvt_pk_bf16_f32 v21, v178, v179\n\t"
    "s_waitcnt lgkmcnt(0)\n\t"
    "s_waitcnt vmcnt(0)\n\t"
    "v_mfma_f32_16x16x32_bf16 v[240:243], v[44:47], v[104:107], v[240:243]\n\t"
    "v_cvt_pk_bf16_f32 v22, v208, v209\n\t"
    "v_cvt_pk_bf16_f32 v23, v210, v211\n\t"
    "v_cvt_pk_bf16_f32 v24, v172, v173\n\t"
    "v_cvt_pk_bf16_f32 v25, v174, v175\n\t"
    "v_mfma_f32_16x16x32_bf16 v[236:239], v[20:23], v[20:23], 0\n\t"
    "ds_write_b128 v4, v[20:23] offset:768\n\t"
    "v_cvt_pk_bf16_f32 v232, v152, v153\n\t"
    "v_cvt_pk_bf16_f32 v233, v154, v155\n\t"
    "v_mfma_f32_16x16x32_bf16 v[228:231], v[128:131], v[20:23], 0\n\t"
    "v_cvt_pk_bf16_f32 v234, v240, v241\n\t"
    "v_cvt_pk_bf16_f32 v235, v242, v243\n\t"
    "v_cvt_pk_bf16_f32 v26, v108, v109\n\t"
    "ds_write_b128 v4, v[232:235] offset:800\n\t"
    "s_branch .Lw_sp9_%=\n\t"
    ".Lw_spb9_%=:\n\t"
    "ds_read_b128 v[224:227], v251 offset:49152\n\t"
    "ds_read_b128 v[20:23], v251 offset:50176\n\t"
    "ds_read_b128 v[28:31], v251 offset:51200\n\t"
    "ds_read_b128 v[220:223], v251 offset:0\n\t"
    "ds_read_b128 v[216:219], v251 offset:1024\n\t"
    "ds_read_b128 v[212:215], v251 offset:2048\n\t"
    "v_cvt_pk_bf16_f32 v27, v110, v111\n\t"
    "ds_read_b128 v[32:35], v251 offset:3072\n\t"
    "ds_read_b128 v[36:39], v251 offset:4096\n\t"
    "v_mfma_f32_16x16x32_bf16 v[236:239], v[24:27], v[24:27], v[236:239]\n\t"
    "ds_write_b128 v4, v[24:27] offset:784\n\t"
    "ds_read_b128 v[40:43], v251 offset:5120\n\t"
    "ds_read_b128 v[204:207], v251 offset:6144\n\t"
    "v_mfma_f32_16x16x32_bf16 v[228:231], v[128:131], v[24:27], v[228:231]\n\t"
    "ds_read_b128 v[200:203], v251 offset:7168\n\t"
    "v_mfma_f32_16x16x32_bf16 v[236:239], v[232:235], v[232:235], v[236:239]\n\t"
    "ds_read_b128 v[24:27], v251 offset:8192\n\t"
    "v_mfma_f32_16x16x32_bf16 v[228:231], v[128:131], v[232:235], v[228:231]\n\t"
    "ds_read_b128 v[232:235], v251 offset:9216\n\t"
    "s_nop 4\n\t"
    "v_cndmask_b32_e64 v4, v239, v238, s[80:81]\n\t"
    "v_cndmask_b32_e64 v4, v4, v237, s[78:79]\n\t"
    "v_mul_f32 v9, 0x3c2aaaab, v228\n\t"
    "v_cndmask_b32_e64 v4, v4, v236, s[76:77]\n\t"
    "v_mul_f32_e64 v18, -v9, v9\n\t"
    "ds_bpermute_b32 v19, v253, v4\n\t"
    "s_waitcnt lgkmcnt(0)\n\t"
    "v_fmamk_f32 v4, v19, 0x3c2aaaab, v18\n\t"
    "v_max_f32 v4, 0, v4\n\t"
    "v_add_f32 v4, 0x3727c5ac, v4\n\t"
    "v_rsq_f32 v4, v4\n\t"
    "s_nop 0\n\t"
    "v_mul_f32 v4, 0x3f0aec44, v4\n\t"
    "v_mul_f32_e64 v9, -v9, v4\n\t"
    "v_fma_f32 v176, v176, v4, v9\n\t"
    "v_fma_f32 v177, v177, v4, v9\n\t"
    "v_fma_f32 v178, v178, v4, v9\n\t"
    "v_fma_f32 v179, v179, v4, v9\n\t"
    "v_fma_f32 v208, v208, v4, v9\n\t"
    "v_fma_f32 v209, v209, v4, v9\n\t"
    "v_fma_f32 v210, v210, v4, v9\n\t"
    "v_fma_f32 v211, v211, v4, v9\n\t"
    "v_cvt_pk_bf16_f32 v44, v176, v177\n\t"
    "v_cvt_pk_bf16_f32 v45, v178, v179\n\t"
    "v_cvt_pk_bf16_f32 v46, v208, v209\n\t"
    "v_cvt_pk_bf16_f32 v47, v210, v211\n\t"
    "v_fma_f32 v172, v172, v4, v9\n\t"
    "v_fma_f32 v173, v173, v4, v9\n\t"
    "v_mfma_f32_16x16x32_bf16 v[48:51], v[44:47], v[224:227], 0\n\t"
    "ds_read_b128 v[52:55], v251 offset:10240\n\t"
    "v_fma_f32 v174, v174, v4, v9\n\t"
    "v_fma_f32 v175, v175, v4, v9\n\t"
    "v_fma_f32 v108, v108, v4, v9\n\t"
    "v_fma_f32 v109, v109, v4, v9\n\t"
    "v_fma_f32 v110, v110, v4, v9\n\t"
    "v_fma_f32 v111, v111, v4, v9\n\t"
    "v_cvt_pk_bf16_f32 v56, v172, v173\n\t"
    "v_cvt_pk_bf16_f32 v57, v174, v175\n\t"
    "v_cvt_pk_bf16_f32 v58, v108, v109\n\t"
    "v_cvt_pk_bf16_f32 v59, v110, v111\n\t"
    "v_fma_f32 v152, v152, v4, v9\n\t"
    "v_fma_f32 v153, v153, v4, v9\n\t"
    "v_mfma_f32_16x16x32_bf16 v[48:51], v[56:59], v[20:23], v[48:51]\n\t"
    "ds_read_b128 v[20:23], v251 offset:11264\n\t"
    "v_fma_f32 v154, v154, v4, v9\n\t"
    "v_fma_f32 v155, v155, v4, v9\n\t"
    "v_fma_f32 v240, v240, v4, v9\n\t"
    "v_fma_f32 v241, v241, v4, v9\n\t"
    "v_fma_f32 v242, v242, v4, v9\n\t"
    "v_fma_f32 v243, v243, v4, v9\n\t"
    "v_cvt_pk_bf16_f32 v236, v152, v153\n\t"
    "v_cvt_pk_bf16_f32 v237, v154, v155\n\t"
    "v_cvt_pk_bf16_f32 v238, v240, v241\n\t"
    "v_cvt_pk_bf16_f32 v239, v242, v243\n\t"
    "s_nop 1\n\t"
    "v_mfma_f32_16x16x32_bf16 v[48:51], v[236:239], v[28:31], v[48:51]\n\t"
    "ds_read_b128 v[28:31], v251 offset:12288\n\t"
    "v_mfma_f32_16x16x32_bf16 v[60:63], v[220:223], v[44:47], 0\n\t"
    "ds_read_b128 v[64:67], v251 offset:13312\n\t"
    "v_mfma_f32_16x16x32_bf16 v[60:63], v[216:219], v[56:59], v[60:63]\n\t"
    "ds_read_b128 v[68:71], v251 offset:14336\n\t"
    "v_mfma_f32_16x16x32_bf16 v[60:63], v[212:215], v[236:239], v[60:63]\n\t"
    "ds_read_b128 v[72:75], v251 offset:15360\n\t"
    "s_nop 0\n\t"
    "v_cvt_pk_bf16_f32 v18, v48, v49\n\t"
    "v_cvt_pk_bf16_f32 v19, v50, v51\n\t"
    "v_mfma_f32_16x16x32_bf16 v[48:51], v[32:35], v[44:47], 0\n\t"
    "ds_read_b128 v[32:35], v251 offset:16384\n\t"
    "v_cndmask_b32_e64 v0, v128, v18, s[82:83]\n\t"
    "v_cndmask_b32_e64 v1, v128, v19, s[82:83]\n\t"
    "v_mfma_f32_16x16x32_bf16 v[48:51], v[36:39], v[56:59], v[48:51]\n\t"
    "ds_write_b16 v5, v0 offset:32\n\t"
    "v_cndmask_b32_e64 v18, v18, v128, s[82:83]\n\t"
    "v_cndmask_b32_e64 v19, v19, v128, s[82:83]\n\t"
    "v_mfma_f32_16x16x32_bf16 v[48:51], v[40:43], v[236:239], v[48:51]\n\t"
    "ds_write_b16_d16_hi v6, v0 offset:32\n\t"
    "ds_write_b16 v7, v1 offset:32\n\t"
    "ds_write_b16_d16_hi v8, v1 offset:32\n\t"
    "v_mfma_f32_16x16x32_bf16 v[36:39], v[204:207], v[44:47], 0\n\t"
    "ds_write_b16 v5, v18 offset:224\n\t"
    "ds_write_b16_d16_hi v6, v18 offset:224\n\t"
    "ds_write_b16 v7, v19 offset:224\n\t"
    "v_mfma_f32_16x16x32_bf16 v[36:39], v[200:203], v[56:59], v[36:39]\n\t"
    "ds_write_b16_d16_hi v8, v19 offset:224\n\t"
    "ds_read_b128 v[240:243], v251 offset:17408\n\t"
    "v_cvt_pk_bf16_f32 v184, v60, v61\n\t"
    "v_mfma_f32_16x16x32_bf16 v[36:39], v[24:27], v[236:239], v[36:39]\n\t"
    "ds_read_b128 v[24:27], v251 offset:18432\n\t"
    "v_cvt_pk_bf16_f32 v185, v62, v63\n\t"
    "v_cvt_pk_bf16_f32 v186, v50, v51\n\t"
    "v_mfma_f32_16x16x32_bf16 v[40:43], v[44:47], v[232:235], 0\n\t"
    "ds_read_b128 v[232:235], v251 offset:19456\n\t"
    "ds_write_b128 v3, v[184:187] offset:0\n\t"
    "v_cvt_pk_bf16_f32 v126, v48, v49\n\t"
    "s_waitcnt lgkmcnt(15)\n\t"
    "v_mfma_f32_16x16x32_bf16 v[40:43], v[56:59], v[52:55], v[40:43]\n\t"
    "ds_read_b128 v[48:51], v251 offset:20480\n\t"
    "v_cvt_pk_bf16_f32 v124, v36, v37\n\t"
    "v_cvt_pk_bf16_f32 v125, v38, v39\n\t"
    "v_mfma_f32_16x16x32_bf16 v[40:43], v[236:239], v[20:23], v[40:43]\n\t"
    "ds_read_b128 v[20:23], v251 offset:21504\n\t"
    "ds_read_b128 v[36:39], v251 offset:22528\n\t"
    "ds_write_b128 v3, v[124:127] offset:64\n\t"
    "v_mfma_f32_16x16x32_bf16 v[228:231], v[28:31], v[44:47], 0\n\t"
    "ds_read_b128 v[224:227], v251 offset:23552\n\t"
    "ds_read_b128 v[220:223], v251 offset:52224\n\t"
    "ds_read_b128 v[28:31], v251 offset:53248\n\t"
    "v_mfma_f32_16x16x32_bf16 v[228:231], v[64:67], v[56:59], v[228:231]\n\t"
    "ds_read_b128 v[52:55], v251 offset:54272\n\t"
    "v_cvt_pk_bf16_f32 v18, v40, v41\n\t"
    "v_cvt_pk_bf16_f32 v19, v42, v43\n\t"
    "s_waitcnt lgkmcnt(15)\n\t"
    "v_mfma_f32_16x16x32_bf16 v[228:231], v[68:71], v[236:239], v[228:231]\n\t"
    "ds_write_b16 v5, v18 offset:0\n\t"
    "ds_write_b16_d16_hi v6, v18 offset:0\n\t"
    "ds_write_b16 v7, v19 offset:0\n\t"
    "v_mfma_f32_16x16x32_bf16 v[40:43], v[72:75], v[44:47], 0\n\t"
    "ds_write_b16_d16_hi v8, v19 offset:0\n\t"
    "ds_read_b128 v[216:219], v251 offset:24576\n\t"
    "ds_read_b128 v[60:63], v251 offset:25600\n\t"
    "v_mfma_f32_16x16x32_bf16 v[40:43], v[32:35], v[56:59], v[40:43]\n\t"
    "ds_read_b128 v[32:35], v251 offset:26624\n\t"
    "v_cvt_pk_bf16_f32 v132, v228, v229\n\t"
    "v_cvt_pk_bf16_f32 v133, v230, v231\n\t"
    "s_waitcnt lgkmcnt(15)\n\t"
    "v_mfma_f32_16x16x32_bf16 v[40:43], v[240:243], v[236:239], v[40:43]\n\t"
    "ds_read_b128 v[64:67], v251 offset:27648\n\t"
    "v_mfma_f32_16x16x32_bf16 v[68:71], v[24:27], v[44:47], 0\n\t"
    "ds_read_b128 v[24:27], v251 offset:28672\n\t"
    "v_mfma_f32_16x16x32_bf16 v[68:71], v[232:235], v[56:59], v[68:71]\n\t"
    "ds_read_b128 v[72:75], v251 offset:29696\n\t"
    "s_waitcnt lgkmcnt(15)\n\t"
    "v_mfma_f32_16x16x32_bf16 v[68:71], v[48:51], v[236:239], v[68:71]\n\t"
    "ds_read_b128 v[48:51], v251 offset:30720\n\t"
    "v_cvt_pk_bf16_f32 v134, v42, v43\n\t"
    "v_cvt_pk_bf16_f32 v138, v40, v41\n\t"
    "v_mfma_f32_16x16x32_bf16 v[40:43], v[44:47], v[20:23], 0\n\t"
    "ds_write_b128 v3, v[132:135] offset:192\n\t"
    "ds_read_b128 v[20:23], v251 offset:31744\n\t"
    "v_mfma_f32_16x16x32_bf16 v[40:43], v[56:59], v[36:39], v[40:43]\n\t"
    "ds_read_b128 v[36:39], v251 offset:32768\n\t"
    "v_cvt_pk_bf16_f32 v136, v68, v69\n\t"
    "v_cvt_pk_bf16_f32 v137, v70, v71\n\t"
    "s_waitcnt lgkmcnt(15)\n\t"
    "v_mfma_f32_16x16x32_bf16 v[40:43], v[236:239], v[224:227], v[40:43]\n\t"
    "ds_write_b128 v3, v[136:139] offset:256\n\t"
    "ds_read_b128 v[68:71], v251 offset:33792\n\t"
    "v_mfma_f32_16x16x32_bf16 v[76:79], v[44:47], v[220:223], 0\n\t"
    "ds_read_b128 v[80:83], v251 offset:34816\n\t"
    "v_mfma_f32_16x16x32_bf16 v[76:79], v[56:59], v[28:31], v[76:79]\n\t"
    "ds_read_b128 v[28:31], v251 offset:35840\n\t"
    "s_waitcnt lgkmcnt(15)\n\t"
    "v_mfma_f32_16x16x32_bf16 v[76:79], v[236:239], v[52:55], v[76:79]\n\t"
    "ds_read_b128 v[240:243], v251 offset:36864\n\t"
    "v_cvt_pk_bf16_f32 v18, v40, v41\n\t"
    "v_cvt_pk_bf16_f32 v19, v42, v43\n\t"
    "s_waitcnt lgkmcnt(14)\n\t"
    "v_mfma_f32_16x16x32_bf16 v[40:43], v[216:219], v[44:47], 0\n\t"
    "ds_write_b16 v5, v18 offset:192\n\t"
    "ds_write_b16_d16_hi v6, v18 offset:192\n\t"
    "ds_write_b16 v7, v19 offset:192\n\t"
    "s_waitcnt lgkmcnt(15)\n\t"
    "v_mfma_f32_16x16x32_bf16 v[40:43], v[60:63], v[56:59], v[40:43]\n\t"
    "ds_write_b16_d16_hi v8, v19 offset:192\n\t"
    "v_cvt_pk_bf16_f32 v18, v76, v77\n\t"
    "v_cvt_pk_bf16_f32 v19, v78, v79\n\t"
    "v_mfma_f32_16x16x32_bf16 v[40:43], v[32:35], v[236:239], v[40:43]\n\t"
    "ds_read_b128 v[232:235], v251 offset:37888\n\t"
    "v_cndmask_b32_e64 v0, v128, v18, s[82:83]\n\t"
    "v_cndmask_b32_e64 v1, v128, v19, s[82:83]\n\t"
    "s_waitcnt lgkmcnt(15)\n\t"
    "v_mfma_f32_16x16x32_bf16 v[32:35], v[64:67], v[44:47], 0\n\t"
    "ds_read_b128 v[52:55], v251 offset:38912\n\t"
    "v_cndmask_b32_e64 v18, v18, v128, s[82:83]\n\t"
    "v_cndmask_b32_e64 v19, v19, v128, s[82:83]\n\t"
    "v_mfma_f32_16x16x32_bf16 v[32:35], v[24:27], v[56:59], v[32:35]\n\t"
    "ds_read_b128 v[24:27], v251 offset:39936\n\t"
    "v_cvt_pk_bf16_f32 v140, v40, v41\n\t"
    "v_cvt_pk_bf16_f32 v141, v42, v43\n\t"
    "s_waitcnt lgkmcnt(15)\n\t"
    "v_mfma_f32_16x16x32_bf16 v[32:35], v[72:75], v[236:239], v[32:35]\n\t"
    "ds_write_b16 v5, v0 offset:416\n\t"
    "ds_write_b16_d16_hi v6, v0 offset:416\n\t"
    "ds_write_b16 v7, v1 offset:416\n\t"
    "v_mfma_f32_16x16x32_bf16 v[40:43], v[48:51], v[44:47], 0\n\t"
    "ds_write_b16_d16_hi v8, v1 offset:416\n\t"
    "ds_write_b16 v5, v18 offset:608\n\t"
    "ds_write_b16_d16_hi v6, v18 offset:608\n\t"
    "s_waitcnt lgkmcnt(15)\n\t"
    "v_mfma_f32_16x16x32_bf16 v[40:43], v[20:23], v[56:59], v[40:43]\n\t"
    "ds_write_b16 v7, v19 offset:608\n\t"
    "ds_write_b16_d16_hi v8, v19 offset:608\n\t"
    "ds_read_b128 v[20:23], v251 offset:40960\n\t"
    "v_mfma_f32_16x16x32_bf16 v[40:43], v[36:39], v[236:239], v[40:43]\n\t"
    "ds_read_b128 v[228:231], v251 offset:41984\n\t"
    "ds_read_b128 v[224:227], v251 offset:43008\n\t"
    "v_cvt_pk_bf16_f32 v142, v34, v35\n\t"
    "v_mfma_f32_16x16x32_bf16 v[36:39], v[44:47], v[68:71], 0\n\t"
    "ds_read_b128 v[48:51], v251 offset:44032\n\t"
    "ds_write_b128 v3, v[140:143] offset:384\n\t"
    "v_cvt_pk_bf16_f32 v146, v32, v33\n\t"
    "v_mfma_f32_16x16x32_bf16 v[36:39], v[56:59], v[80:83], v[36:39]\n\t"
    "ds_read_b128 v[32:35], v251 offset:45056\n\t"
    "v_cvt_pk_bf16_f32 v144, v40, v41\n\t"
    "v_cvt_pk_bf16_f32 v145, v42, v43\n\t"
    "s_waitcnt lgkmcnt(15)\n\t"
    "v_mfma_f32_16x16x32_bf16 v[36:39], v[236:239], v[28:31], v[36:39]\n\t"
    "ds_read_b128 v[28:31], v251 offset:46080\n\t"
    "ds_read_b128 v[40:43], v251 offset:47104\n\t"
    "ds_write_b128 v3, v[144:147] offset:448\n\t"
    "v_mfma_f32_16x16x32_bf16 v[60:63], v[240:243], v[44:47], 0\n\t"
    "ds_read_b128 v[64:67], v251 offset:48128\n\t"
    "v_mfma_f32_16x16x32_bf16 v[60:63], v[232:235], v[56:59], v[60:63]\n\t"
    "s_nop 1\n\t"
    "v_cvt_pk_bf16_f32 v18, v36, v37\n\t"
    "v_mfma_f32_16x16x32_bf16 v[60:63], v[52:55], v[236:239], v[60:63]\n\t"
    "ds_write_b16 v5, v18 offset:384\n\t"
    "v_cvt_pk_bf16_f32 v19, v38, v39\n\t"
    "ds_write_b16_d16_hi v6, v18 offset:384\n\t"
    "s_waitcnt lgkmcnt(15)\n\t"
    "v_mfma_f32_16x16x32_bf16 v[36:39], v[24:27], v[44:47], 0\n\t"
    "ds_write_b16 v7, v19 offset:384\n\t"
    "ds_write_b16_d16_hi v8, v19 offset:384\n\t"
    "s_nop 0\n\t"
    "v_cvt_pk_bf16_f32 v148, v60, v61\n\t"
    "v_cvt_pk_bf16_f32 v149, v62, v63\n\t"
    "s_waitcnt lgkmcnt(13)\n\t"
    "v_mfma_f32_16x16x32_bf16 v[36:39], v[20:23], v[56:59], v[36:39]\n\t"
    "s_waitcnt lgkmcnt(12)\n\t"
    "v_mfma_f32_16x16x32_bf16 v[36:39], v[228:231], v[236:239], v[36:39]\n\t"
    "s_waitcnt lgkmcnt(11)\n\t"
    "v_mfma_f32_16x16x32_bf16 v[20:23], v[224:227], v[44:47], 0\n\t"
    "s_waitcnt lgkmcnt(10)\n\t"
    "v_mfma_f32_16x16x32_bf16 v[20:23], v[48:51], v[56:59], v[20:23]\n\t"
    "s_waitcnt lgkmcnt(8)\n\t"
    "v_mfma_f32_16x16x32_bf16 v[20:23], v[32:35], v[236:239], v[20:23]\n\t"
    "s_waitcnt lgkmcnt(7)\n\t"
    "v_mfma_f32_16x16x32_bf16 v[24:27], v[44:47], v[28:31], 0\n\t"
    "v_cvt_pk_bf16_f32 v150, v38, v39\n\t"
    "v_cvt_pk_bf16_f32 v182, v36, v37\n\t"
    "s_waitcnt lgkmcnt(6)\n\t"
    "v_mfma_f32_16x16x32_bf16 v[24:27], v[56:59], v[40:43], v[24:27]\n\t"
    "ds_write_b128 v3, v[148:151] offset:576\n\t"
    "s_nop 0\n\t"
    "v_cvt_pk_bf16_f32 v180, v20, v21\n\t"
    "v_cvt_pk_bf16_f32 v181, v22, v23\n\t"
    "s_waitcnt lgkmcnt(5)\n\t"
    "v_mfma_f32_16x16x32_bf16 v[24:27], v[236:239], v[64:67], v[24:27]\n\t"
    "ds_write_b128 v3, v[180:183] offset:640\n\t"
    "s_nop 6\n\t"
    "v_cvt_pk_bf16_f32 v18, v24, v25\n\t"
    "v_cvt_pk_bf16_f32 v19, v26, v27\n\t"
    "ds_write_b16 v5, v18 offset:576\n\t"
    "ds_write_b16_d16_hi v6, v18 offset:576\n\t"
    "ds_write_b16 v7, v19 offset:576\n\t"
    "ds_write_b16_d16_hi v8, v19 offset:576\n\t"
    "s_waitcnt lgkmcnt(0)\n\t"
    "s_bfe_u32 s66, s28, 0x10003\n\t"
    "s_lshl_b32 s66, s66, 2\n\t"
    "s_add_u32 s66, s66, 160512\n\t"
    "v_mov_b32 v9, s66\n\t"
    "v_mov_b32 v8, 1\n\t"
    "s_mov_b64 exec, 1\n\t"
    "ds_write_b32 v9, v8\n\t"
    "s_mov_b64 exec, -1\n\t"
    "s_setprio 0\n\t"
    "s_branch .Lw_pre_%=\n\t"
    ".Lw_r2b_%=:\n\t"
    "v_mfma_f32_16x16x32_bf16 v[152:155], v[44:47], v[56:59], 0\n\t"
    "ds_read_b128 v[164:167], v170 offset:640\n\t"
    "ds_read_b128 v[160:163], v169 offset:640\n\t"
    "ds_read_b128 v[156:159], v168 offset:640\n\t"
    "v_mfma_f32_16x16x32_bf16 v[168:171], v[48:51], v[56:59], 0\n\t"
    "ds_read_b128 v[88:91], v175 offset:576\n\t"
    "ds_read_b128 v[80:83], v174 offset:640\n\t"
    "v_lshrrev_b32 v69, 2, v251\n\t"
    "v_mfma_f32_16x16x32_bf16 v[100:103], v[40:43], v[56:59], 0\n\t"
    "ds_read_b128 v[72:75], v252 offset:20480\n\t"
    "v_and_b32 v69, 48, v69\n\t"
    "v_bfe_u32 v192, v251, 3, 3\n\t"
    "v_mfma_f32_16x16x32_bf16 v[96:99], v[44:47], v[60:63], 0\n\t"
    "v_or_b32 v69, v69, v192\n\t"
    "v_cndmask_b32_e64 v148, v172, v128, s[82:83]\n\t"
    "v_mfma_f32_16x16x32_bf16 v[92:95], v[48:51], v[60:63], 0\n\t"
    "v_and_or_b32 v192, v69, 50, 8\n\t"
    "v_cndmask_b32_e64 v149, v173, v128, s[82:83]\n\t"
    "v_mfma_f32_16x16x32_bf16 v[84:87], v[40:43], v[60:63], 0\n\t"
    "ds_read_u16 v66, v145 offset:576\n\t"
    "v_exp_f32 v152, v152\n\t"
    "v_exp_f32 v153, v153\n\t"
    "v_exp_f32 v154, v154\n\t"
    "v_exp_f32 v155, v155\n\t"
    "v_exp_f32 v168, v168\n\t"
    "v_exp_f32 v169, v169\n\t"
    "v_exp_f32 v170, v170\n\t"
    "v_exp_f32 v171, v171\n\t"
    "v_cvt_pk_bf16_f32 v152, v152, v153\n\t"
    "v_cvt_pk_bf16_f32 v153, v154, v155\n\t"
    "v_cvt_pk_bf16_f32 v154, v168, v169\n\t"
    "v_cvt_pk_bf16_f32 v155, v170, v171\n\t"
    "v_cndmask_b32_e64 v150, v146, v128, s[82:83]\n\t"
    "v_cndmask_b32_e64 v151, v147, v128, s[82:83]\n\t"
    "v_exp_f32 v100, v100\n\t"
    "s_waitcnt lgkmcnt(6)\n\t"
    "v_mfma_f32_16x16x32_bf16 v[168:171], v[164:167], v[44:47], 0\n\t"
    "ds_read_u16 v134, v145 offset:608\n\t"
    "v_exp_f32 v101, v101\n\t"
    "s_waitcnt lgkmcnt(6)\n\t"
    "v_mfma_f32_16x16x32_bf16 v[168:171], v[160:163], v[48:51], v[168:171]\n\t"
    "v_exp_f32 v102, v102\n\t"
    "s_waitcnt lgkmcnt(5)\n\t"
    "v_mfma_f32_16x16x32_bf16 v[168:171], v[156:159], v[40:43], v[168:171]\n\t"
    "v_exp_f32 v103, v103\n\t"
    "s_waitcnt lgkmcnt(4)\n\t"
    "v_mfma_f32_16x16x32_bf16 v[156:159], v[88:91], v[56:59], 0\n\t"
    "v_cvt_pk_bf16_f32 v136, v100, v101\n\t"
    "v_cvt_pk_bf16_f32 v137, v102, v103\n\t"
    "v_mfma_f32_16x16x32_bf16 v[184:187], v[88:91], v[60:63], 0\n\t"
    "v_cndmask_b32_e64 v132, v130, v128, s[82:83]\n\t"
    "v_cndmask_b32_e64 v133, v131, v128, s[82:83]\n\t"
    "s_waitcnt lgkmcnt(3)\n\t"
    "v_mfma_f32_16x16x32_bf16 v[168:171], v[80:83], v[88:91], v[168:171]\n\t"
    "v_exp_f32 v96, v96\n\t"
    "v_mfma_f32_16x16x32_bf16 v[176:179], v[44:47], v[52:55], 0\n\t"
    "v_exp_f32 v156, v156\n\t"
    "v_mfma_f32_16x16x32_bf16 v[172:175], v[48:51], v[52:55], 0\n\t"
    "v_cvt_pk_bf16_f32 v138, v156, v156\n\t"
    "v_exp_f32 v97, v97\n\t"
    "v_mfma_f32_16x16x32_bf16 v[100:103], v[40:43], v[52:55], 0\n\t"
    "s_nop 0\n\t"
    "v_exp_f32 v168, v168\n\t"
    "v_mfma_f32_16x16x32_bf16 v[180:183], v[88:91], v[52:55], 0\n\t"
    "v_cvt_pk_bf16_f32 v70, v168, v168\n\t"
    "v_cvt_pk_bf16_f32 v156, v96, v97\n\t"
    "v_mfma_f32_16x16x32_bf16 v[52:55], v[12:15], v[152:155], 0\n\t"
    "ds_write_b16 v254, v70\n\t"
    "ds_read_b128 v[168:171], v255 offset:0\n\t"
    "ds_read_b128 v[164:167], v255 offset:64\n\t"
    "v_mfma_f32_16x16x32_bf16 v[160:163], v[148:151], v[152:155], 0\n\t"
    "v_exp_f32 v98, v98\n\t"
    "s_waitcnt lgkmcnt(4)\n\t"
    "v_mfma_f32_16x16x32_bf16 v[52:55], v[64:67], v[136:139], v[52:55]\n\t"
    "v_exp_f32 v99, v99\n\t"
    "s_waitcnt lgkmcnt(3)\n\t"
    "v_mfma_f32_16x16x32_bf16 v[160:163], v[132:135], v[136:139], v[160:163]\n\t"
    "ds_read_b128 v[40:43], v252 offset:21504\n\t"
    "v_exp_f32 v92, v92\n\t"
    "v_exp_f32 v93, v93\n\t"
    "v_exp_f32 v94, v94\n\t"
    "v_exp_f32 v95, v95\n\t"
    "v_cvt_pk_bf16_f32 v157, v98, v99\n\t"
    "v_cvt_pk_bf16_f32 v158, v92, v93\n\t"
    "v_cvt_pk_bf16_f32 v159, v94, v95\n\t"
    "v_exp_f32 v84, v84\n\t"
    "v_exp_f32 v85, v85\n\t"
    "v_mfma_f32_16x16x32_bf16 v[88:91], v[12:15], v[156:159], 0\n\t"
    "v_exp_f32 v86, v86\n\t"
    "v_mfma_f32_16x16x32_bf16 v[92:95], v[148:151], v[156:159], 0\n\t"
    "v_exp_f32 v87, v87\n\t"
    "v_exp_f32 v184, v184\n\t"
    "v_cvt_pk_bf16_f32 v140, v84, v85\n\t"
    "v_cvt_pk_bf16_f32 v141, v86, v87\n\t"
    "v_cvt_pk_bf16_f32 v142, v184, v184\n\t"
    "v_exp_f32 v176, v176\n\t"
    "v_exp_f32 v177, v177\n\t"
    "v_mfma_f32_16x16x32_bf16 v[88:91], v[64:67], v[140:143], v[88:91]\n\t"
    "ds_read_b128 v[240:243], v252 offset:0\n\t"
    "v_exp_f32 v178, v178\n\t"
    "v_mfma_f32_16x16x32_bf16 v[92:95], v[132:135], v[140:143], v[92:95]\n\t"
    "v_exp_f32 v179, v179\n\t"
    "s_waitcnt lgkmcnt(3)\n\t"
    "v_mfma_f32_16x16x32_bf16 v[96:99], v[168:171], v[12:15], 0\n\t"
    "v_exp_f32 v172, v172\n\t"
    "v_mfma_f32_16x16x32_bf16 v[140:143], v[168:171], v[148:151], 0\n\t"
    "ds_read_b128 v[44:47], v252 offset:1024\n\t"
    "v_exp_f32 v173, v173\n\t"
    "s_waitcnt lgkmcnt(3)\n\t"
    "v_mfma_f32_16x16x32_bf16 v[96:99], v[164:167], v[64:67], v[96:99]\n\t"
    "v_exp_f32 v174, v174\n\t"
    "v_mfma_f32_16x16x32_bf16 v[140:143], v[164:167], v[132:135], v[140:143]\n\t"
    "v_exp_f32 v175, v175\n\t"
    "v_cvt_pk_bf16_f32 v152, v176, v177\n\t"
    "v_cvt_pk_bf16_f32 v153, v178, v179\n\t"
    "v_cvt_pk_bf16_f32 v154, v172, v173\n\t"
    "v_cvt_pk_bf16_f32 v155, v174, v175\n\t"
    "v_exp_f32 v100, v100\n\t"
    "v_exp_f32 v101, v101\n\t"
    "v_mfma_f32_16x16x32_bf16 v[84:87], v[12:15], v[152:155], 0\n\t"
    "ds_read_b128 v[48:51], v252 offset:2048\n\t"
    "v_exp_f32 v102, v102\n\t"
    "v_mfma_f32_16x16x32_bf16 v[12:15], v[148:151], v[152:155], 0\n\t"
    "v_exp_f32 v103, v103\n\t"
    "v_exp_f32 v180, v180\n\t"
    "v_cvt_pk_bf16_f32 v136, v100, v101\n\t"
    "v_cvt_pk_bf16_f32 v137, v102, v103\n\t"
    "v_cvt_pk_bf16_f32 v138, v180, v180\n\t"
    "v_min3_u32 v129, v129, v160, v92\n\t"
    "v_max3_u32 v191, v191, v160, v92\n\t"
    "v_mfma_f32_16x16x32_bf16 v[84:87], v[64:67], v[136:139], v[84:87]\n\t"
    "v_mov_b32_dpp v93, v140 quad_perm:[0,0,0,0] row_mask:0xf bank_mask:0xf bound_ctrl:1\n\t"
    "v_rcp_f32 v70, v160\n\t"
    "v_mfma_f32_16x16x32_bf16 v[12:15], v[132:135], v[136:139], v[12:15]\n\t"
    "ds_read_b128 v[56:59], v252 offset:22528\n\t"
    "v_rcp_f32 v160, v93\n\t"
    "s_waitcnt lgkmcnt(4)\n\t"
    "v_mfma_f32_16x16x32_bf16 v[236:239], v[40:43], v[36:39], 0\n\t"
    "v_mul_f32 v70, 0x3febdf2f, v70\n\t"
    "v_mul_f32 v160, 0x3febdf2f, v160\n\t"
    "v_mfma_f32_16x16x32_bf16 v[60:63], v[40:43], v[32:35], 0\n\t"
    "v_rcp_f32 v71, v92\n\t"
    "v_mfma_f32_16x16x32_bf16 v[64:67], v[40:43], v[28:31], 0\n\t"
    "ds_read_b128 v[40:43], v252 offset:3072\n\t"
    "v_min3_u32 v129, v129, v12, v93\n\t"
    "v_max3_u32 v191, v191, v12, v93\n\t"
    "s_waitcnt lgkmcnt(4)\n\t"
    "v_mfma_f32_16x16x32_bf16 v[236:239], v[240:243], v[124:127], v[236:239]\n\t"
    "v_cmp_le_u32 vcc, s67, v191\n\t"
    "v_cmp_ge_u32_e64 s[62:63], s68, v129\n\t"
    "s_nop 1\n\t"
    "s_or_b64 s[62:63], s[62:63], vcc\n\t"
    "s_cselect_b32 s64, 160768, s53\n\t"
    "s_cselect_b32 s65, 0, 1\n\t"
    "s_cselect_b32 s66, 1, 0\n\t"
    "s_lshl_b32 s66, s66, s54\n\t"
    "s_or_b32 s55, s55, s66\n\t"
    "v_mfma_f32_16x16x32_bf16 v[60:63], v[240:243], v[120:123], v[60:63]\n\t"
    "v_mul_f32 v52, v52, v70\n\t"
    "v_mul_f32 v53, v53, v70\n\t"
    "v_mfma_f32_16x16x32_bf16 v[64:67], v[240:243], v[116:119], v[64:67]\n\t"
    "ds_read_b128 v[80:83], v252 offset:4096\n\t"
    "v_mul_f32 v54, v54, v70\n\t"
    "v_mul_f32 v55, v55, v70\n\t"
    "s_waitcnt lgkmcnt(4)\n\t"
    "v_mfma_f32_16x16x32_bf16 v[236:239], v[44:47], v[112:115], v[236:239]\n\t"
    "v_mul_f32 v162, v162, v70\n\t"
    "v_mul_f32 v163, v163, v70\n\t"
    "v_mfma_f32_16x16x32_bf16 v[60:63], v[44:47], v[108:111], v[60:63]\n\t"
    "v_cvt_pk_bf16_f32 v145, v52, v53\n\t"
    "v_cvt_pk_bf16_f32 v146, v54, v55\n\t"
    "v_mfma_f32_16x16x32_bf16 v[64:67], v[44:47], v[104:107], v[64:67]\n\t"
    "ds_read_b128 v[44:47], v252 offset:5120\n\t"
    "v_cvt_pk_bf16_f32 v147, v162, v163\n\t"
    "v_mul_f32 v71, 0x3febdf2f, v71\n\t"
    "v_rcp_f32 v70, v12\n\t"
    "s_waitcnt lgkmcnt(4)\n\t"
    "v_mfma_f32_16x16x32_bf16 v[236:239], v[48:51], v[144:147], v[236:239]\n\t"
    "v_mul_f32 v88, v88, v71\n\t"
    "v_mul_f32 v89, v89, v71\n\t"
    "v_mul_f32 v90, v90, v71\n\t"
    "v_mul_f32 v91, v91, v71\n\t"
    "v_mul_f32 v94, v94, v71\n\t"
    "v_mul_f32 v95, v95, v71\n\t"
    "v_cvt_pk_bf16_f32 v77, v88, v89\n\t"
    "v_cvt_pk_bf16_f32 v78, v90, v91\n\t"
    "v_cvt_pk_bf16_f32 v240, v236, v237\n\t"
    "v_cvt_pk_bf16_f32 v241, v238, v239\n\t"
    "v_cvt_pk_bf16_f32 v79, v94, v95\n\t"
    "v_mul_f32 v70, 0x3febdf2f, v70\n\t"
    "v_add_u32 v12, s64, v69\n\t"
    "v_mfma_f32_16x16x32_bf16 v[60:63], v[48:51], v[76:79], v[60:63]\n\t"
    "ds_write_b16 v12, v190 offset:64\n\t"
    "v_mul_f32 v84, v84, v70\n\t"
    "v_mul_f32 v85, v85, v70\n\t"
    "v_mul_f32 v86, v86, v70\n\t"
    "v_mul_f32 v87, v87, v70\n\t"
    "v_mul_f32 v14, v14, v70\n\t"
    "v_mul_f32 v15, v15, v70\n\t"
    "v_cvt_pk_bf16_f32 v69, v84, v85\n\t"
    "v_cvt_pk_bf16_f32 v70, v86, v87\n\t"
    "v_cvt_pk_bf16_f32 v52, v60, v61\n\t"
    "v_cvt_pk_bf16_f32 v53, v62, v63\n\t"
    "v_cvt_pk_bf16_f32 v71, v14, v15\n\t"
    "v_add_u32 v54, s64, v192\n\t"
    "v_mul_f32 v96, v96, v160\n\t"
    "v_mfma_f32_16x16x32_bf16 v[64:67], v[48:51], v[68:71], v[64:67]\n\t"
    "ds_read_b128 v[236:239], v252 offset:21504\n\t"
    "v_mul_f32 v140, v140, v160\n\t"
    "ds_write_b16 v12, v189 offset:256\n\t"
    "s_waitcnt lgkmcnt(6)\n\t"
    "v_mfma_f32_16x16x32_bf16 v[232:235], v[56:59], v[36:39], 0\n\t"
    "ds_write_b16 v12, v188 offset:448\n\t"
    "v_cvt_pk_bf16_f32 v191, v96, v140\n\t"
    "v_mfma_f32_16x16x32_bf16 v[228:231], v[56:59], v[32:35], 0\n\t"
    "ds_write_b16 v12, v191 offset:640\n\t"
    "v_cvt_pk_bf16_f32 v12, v64, v65\n\t"
    "v_cvt_pk_bf16_f32 v13, v66, v67\n\t"
    "v_mfma_f32_16x16x32_bf16 v[32:35], v[56:59], v[28:31], 0\n\t"
    "ds_read_b128 v[28:31], v252 offset:6144\n\t"
    "s_mov_b64 exec, s[82:83]\n\t"
    "ds_write_b16_d16_hi v54, v190 offset:64\n\t"
    "ds_write_b16_d16_hi v54, v188 offset:448\n\t"
    "s_not_b64 exec, s[82:83]\n\t"
    "ds_write_b16_d16_hi v54, v189 offset:256\n\t"
    "ds_write_b16_d16_hi v54, v191 offset:640\n\t"
    "s_mov_b64 exec, -1\n\t"
    "s_waitcnt lgkmcnt(12)\n\t"
    "v_mfma_f32_16x16x32_bf16 v[232:235], v[40:43], v[124:127], v[232:235]\n\t"
    "s_min_u32 s66, s54, 1\n\t"
    "s_lshl_b32 s66, s66, 2\n\t"
    "s_add_u32 s66, s66, 160520\n\t"
    "v_mov_b32 v15, s66\n\t"
    "v_mov_b32 v14, s65\n\t"
    "s_mov_b64 exec, 1\n\t"
    "ds_add_u32 v15, v14\n\t"
    "s_mov_b64 exec, -1\n\t"
    "v_mfma_f32_16x16x32_bf16 v[228:231], v[40:43], v[120:123], v[228:231]\n\t"
    "v_mfma_f32_16x16x32_bf16 v[32:35], v[40:43], v[116:119], v[32:35]\n\t"
    "ds_read_b128 v[36:39], v252 offset:7168\n\t"
    "s_waitcnt lgkmcnt(13)\n\t"
    "v_mfma_f32_16x16x32_bf16 v[232:235], v[80:83], v[112:115], v[232:235]\n\t"
    "v_mfma_f32_16x16x32_bf16 v[228:231], v[80:83], v[108:111], v[228:231]\n\t"
    "v_mfma_f32_16x16x32_bf16 v[32:35], v[80:83], v[104:107], v[32:35]\n\t"
    "ds_read_b128 v[40:43], v252 offset:8192\n\t"
    "s_waitcnt lgkmcnt(13)\n\t"
    "v_mfma_f32_16x16x32_bf16 v[232:235], v[44:47], v[144:147], v[232:235]\n\t"
    "v_mfma_f32_16x16x32_bf16 v[228:231], v[44:47], v[76:79], v[228:231]\n\t"
    "v_mfma_f32_16x16x32_bf16 v[32:35], v[44:47], v[68:71], v[32:35]\n\t"
    "ds_read_b128 v[44:47], v252 offset:22528\n\t"
    "s_waitcnt lgkmcnt(12)\n\t"
    "v_mfma_f32_16x16x32_bf16 v[48:51], v[236:239], v[24:27], 0\n\t"
    "s_nop 2\n\t"
    "v_cvt_pk_bf16_f32 v242, v232, v233\n\t"
    "v_mfma_f32_16x16x32_bf16 v[56:59], v[236:239], v[20:23], 0\n\t"
    "v_cvt_pk_bf16_f32 v243, v234, v235\n\t"
    "v_cvt_pk_bf16_f32 v54, v228, v229\n\t"
    "v_mfma_f32_16x16x32_bf16 v[60:63], v[236:239], v[16:19], 0\n\t"
    "buffer_store_dwordx4 v[240:243], v73, s[40:43], s44 offen offset:0 sc1\n\t"
    "v_cvt_pk_bf16_f32 v55, v230, v231\n\t"
    "v_cvt_pk_bf16_f32 v14, v32, v33\n\t"
    "s_waitcnt lgkmcnt(8)\n\t"
    "v_mfma_f32_16x16x32_bf16 v[48:51], v[28:31], v[124:127], v[48:51]\n\t"
    "buffer_store_dwordx4 v[52:55], v74, s[40:43], s44 offen offset:0 sc1\n\t"
    "v_cvt_pk_bf16_f32 v15, v34, v35\n\t"
    "ds_read_b128 v[240:243], v252 offset:9216\n\t"
    "v_mfma_f32_16x16x32_bf16 v[56:59], v[28:31], v[120:123], v[56:59]\n\t"
    "buffer_store_dwordx4 v[12:15], v75, s[40:43], s44 offen offset:0 sc1\n\t"
    "v_mfma_f32_16x16x32_bf16 v[60:63], v[28:31], v[116:119], v[60:63]\n\t"
    "ds_read_b128 v[28:31], v252 offset:10240\n\t"
    "s_waitcnt lgkmcnt(4)\n\t"
    "v_mfma_f32_16x16x32_bf16 v[48:51], v[36:39], v[112:115], v[48:51]\n\t"
    "v_mfma_f32_16x16x32_bf16 v[56:59], v[36:39], v[108:111], v[56:59]\n\t"
    "v_mfma_f32_16x16x32_bf16 v[60:63], v[36:39], v[104:107], v[60:63]\n\t"
    "ds_read_b128 v[236:239], v252 offset:11264\n\t"
    "s_waitcnt lgkmcnt(4)\n\t"
    "v_mfma_f32_16x16x32_bf16 v[48:51], v[40:43], v[144:147], v[48:51]\n\t"
    "v_mfma_f32_16x16x32_bf16 v[56:59], v[40:43], v[76:79], v[56:59]\n\t"
    "v_mfma_f32_16x16x32_bf16 v[60:63], v[40:43], v[68:71], v[60:63]\n\t"
    "ds_read_b128 v[12:15], v252 offset:21504\n\t"
    "s_waitcnt lgkmcnt(4)\n\t"
    "v_mfma_f32_16x16x32_bf16 v[32:35], v[44:47], v[24:27], 0\n\t"
    "s_nop 2\n\t"
    "v_cvt_pk_bf16_f32 v232, v48, v49\n\t"
    "v_mfma_f32_16x16x32_bf16 v[228:231], v[44:47], v[20:23], 0\n\t"
    "v_cvt_pk_bf16_f32 v233, v50, v51\n\t"
    "v_cvt_pk_bf16_f32 v20, v56, v57\n\t"
    "v_mfma_f32_16x16x32_bf16 v[24:27], v[44:47], v[16:19], 0\n\t"
    "ds_read_b128 v[16:19], v252 offset:12288\n\t"
    "v_cvt_pk_bf16_f32 v21, v58, v59\n\t"
    "v_cvt_pk_bf16_f32 v36, v60, v61\n\t"
    "s_waitcnt lgkmcnt(4)\n\t"
    "v_mfma_f32_16x16x32_bf16 v[32:35], v[240:243], v[124:127], v[32:35]\n\t"
    "v_cvt_pk_bf16_f32 v37, v62, v63\n\t"
    "v_mfma_f32_16x16x32_bf16 v[228:231], v[240:243], v[120:123], v[228:231]\n\t"
    "v_mfma_f32_16x16x32_bf16 v[24:27], v[240:243], v[116:119], v[24:27]\n\t"
    "ds_read_b128 v[240:243], v252 offset:13312\n\t"
    "s_waitcnt lgkmcnt(4)\n\t"
    "v_mfma_f32_16x16x32_bf16 v[32:35], v[28:31], v[112:115], v[32:35]\n\t"
    "v_mfma_f32_16x16x32_bf16 v[228:231], v[28:31], v[108:111], v[228:231]\n\t"
    "v_mfma_f32_16x16x32_bf16 v[24:27], v[28:31], v[104:107], v[24:27]\n\t"
    "ds_read_b128 v[28:31], v252 offset:14336\n\t"
    "s_waitcnt lgkmcnt(4)\n\t"
    "v_mfma_f32_16x16x32_bf16 v[32:35], v[236:239], v[144:147], v[32:35]\n\t"
    "v_mfma_f32_16x16x32_bf16 v[228:231], v[236:239], v[76:79], v[228:231]\n\t"
    "v_mfma_f32_16x16x32_bf16 v[24:27], v[236:239], v[68:71], v[24:27]\n\t"
    "ds_read_b128 v[40:43], v252 offset:22528\n\t"
    "s_waitcnt lgkmcnt(4)\n\t"
    "v_mfma_f32_16x16x32_bf16 v[44:47], v[12:15], v[8:11], 0\n\t"
    "s_nop 2\n\t"
    "v_cvt_pk_bf16_f32 v234, v32, v33\n\t"
    "v_mfma_f32_16x16x32_bf16 v[48:51], v[12:15], v[4:7], 0\n\t"
    "v_cvt_pk_bf16_f32 v235, v34, v35\n\t"
    "v_cvt_pk_bf16_f32 v22, v228, v229\n\t"
    "v_mfma_f32_16x16x32_bf16 v[236:239], v[12:15], v[0:3], 0\n\t"
    "buffer_store_dwordx4 v[232:235], v73, s[40:43], s44 offen offset:64 sc1\n\t"
    "v_cvt_pk_bf16_f32 v23, v230, v231\n\t"
    "v_cvt_pk_bf16_f32 v38, v24, v25\n\t"
    "s_waitcnt lgkmcnt(3)\n\t"
    "v_mfma_f32_16x16x32_bf16 v[44:47], v[16:19], v[124:127], v[44:47]\n\t"
    "buffer_store_dwordx4 v[20:23], v74, s[40:43], s44 offen offset:64 sc1\n\t"
    "v_cvt_pk_bf16_f32 v39, v26, v27\n\t"
    "ds_read_b128 v[12:15], v252 offset:15360\n\t"
    "v_mfma_f32_16x16x32_bf16 v[48:51], v[16:19], v[120:123], v[48:51]\n\t"
    "buffer_store_dwordx4 v[36:39], v75, s[40:43], s44 offen offset:64 sc1\n\t"
    "v_mfma_f32_16x16x32_bf16 v[236:239], v[16:19], v[116:119], v[236:239]\n\t"
    "ds_read_b128 v[16:19], v252 offset:16384\n\t"
    "s_waitcnt lgkmcnt(4)\n\t"
    "v_mfma_f32_16x16x32_bf16 v[44:47], v[240:243], v[112:115], v[44:47]\n\t"
    "v_mfma_f32_16x16x32_bf16 v[48:51], v[240:243], v[108:111], v[48:51]\n\t"
    "v_mfma_f32_16x16x32_bf16 v[236:239], v[240:243], v[104:107], v[236:239]\n\t"
    "ds_read_b128 v[20:23], v252 offset:17408\n\t"
    "s_waitcnt lgkmcnt(4)\n\t"
    "v_mfma_f32_16x16x32_bf16 v[44:47], v[28:31], v[144:147], v[44:47]\n\t"
    "v_mfma_f32_16x16x32_bf16 v[48:51], v[28:31], v[76:79], v[48:51]\n\t"
    "v_mfma_f32_16x16x32_bf16 v[236:239], v[28:31], v[68:71], v[236:239]\n\t"
    "s_waitcnt lgkmcnt(3)\n\t"
    "v_mfma_f32_16x16x32_bf16 v[24:27], v[40:43], v[8:11], 0\n\t"
    "v_mfma_f32_16x16x32_bf16 v[8:11], v[40:43], v[4:7], 0\n\t"
    "s_nop 2\n\t"
    "v_cvt_pk_bf16_f32 v4, v44, v45\n\t"
    "v_cvt_pk_bf16_f32 v5, v46, v47\n\t"
    "v_mfma_f32_16x16x32_bf16 v[28:31], v[40:43], v[0:3], 0\n\t"
    "v_cvt_pk_bf16_f32 v0, v48, v49\n\t"
    "v_cvt_pk_bf16_f32 v1, v50, v51\n\t"
    "s_waitcnt lgkmcnt(2)\n\t"
    "v_mfma_f32_16x16x32_bf16 v[24:27], v[12:15], v[124:127], v[24:27]\n\t"
    "v_cvt_pk_bf16_f32 v32, v236, v237\n\t"
    "v_cvt_pk_bf16_f32 v33, v238, v239\n\t"
    "v_mfma_f32_16x16x32_bf16 v[8:11], v[12:15], v[120:123], v[8:11]\n\t"
    "v_mfma_f32_16x16x32_bf16 v[28:31], v[12:15], v[116:119], v[28:31]\n\t"
    "s_waitcnt lgkmcnt(1)\n\t"
    "v_mfma_f32_16x16x32_bf16 v[24:27], v[16:19], v[112:115], v[24:27]\n\t"
    "v_mfma_f32_16x16x32_bf16 v[8:11], v[16:19], v[108:111], v[8:11]\n\t"
    "v_mfma_f32_16x16x32_bf16 v[28:31], v[16:19], v[104:107], v[28:31]\n\t"
    "s_waitcnt lgkmcnt(0)\n\t"
    "v_mfma_f32_16x16x32_bf16 v[24:27], v[20:23], v[144:147], v[24:27]\n\t"
    "v_mfma_f32_16x16x32_bf16 v[8:11], v[20:23], v[76:79], v[8:11]\n\t"
    "v_mfma_f32_16x16x32_bf16 v[28:31], v[20:23], v[68:71], v[28:31]\n\t"
    "s_nop 5\n\t"
    "v_cvt_pk_bf16_f32 v6, v24, v25\n\t"
    "v_cvt_pk_bf16_f32 v7, v26, v27\n\t"
    "v_cvt_pk_bf16_f32 v2, v8, v9\n\t"
    "v_cvt_pk_bf16_f32 v3, v10, v11\n\t"
    "v_cvt_pk_bf16_f32 v34, v28, v29\n\t"
    "v_cvt_pk_bf16_f32 v35, v30, v31\n\t"
    "buffer_store_dwordx4 v[4:7], v73, s[40:43], s44 offen offset:128 sc1\n\t"
    "buffer_store_dwordx4 v[0:3], v74, s[40:43], s44 offen offset:128 sc1\n\t"
    "buffer_store_dwordx4 v[32:35], v75, s[40:43], s44 offen offset:128 sc1\n\t"
        : "={s55}"(badmask), "+{v0}"(lp0), "+{v1}"(lp3), "+{v2}"(lp4), "+{v3}"(lrec), "+{v4}"(lrecx), "+{v5}"(lrv0), "+{v6}"(lrv1), "+{v7}"(lrv2), "+{v8}"(lrv3),
          "+{v[10:13]}"(ft0), "+{v[14:17]}"(ft1), "+{s28}"(flags),
          "+{v244}"(c.cA[0]), "+{v245}"(c.cA[1]), "+{v246}"(c.cA[2]), "+{v247}"(c.cB[0]), "+{v248}"(c.cB[1]), "+{v249}"(c.cB[2]), "+{v250}"(c.cW), "+{v251}"(c.l16),
          "+{v252}"(c.l16o), "+{v253}"(c.bperm), "+{v254}"(c.pw), "+{v255}"(c.pr)
        : "{s[36:39]}"(rx), "{s[40:43]}"(rout), "{s[20:23]}"(sa), "{s[24:27]}"(sb)
        : CLOBBERS
          "vcc", "scc", "m0", "memory");
  }
  OPQ(c.l16);
  unsigned wv2 = wv, bid2 = blockIdx.x;
  asm volatile("" : "+s"(wv2), "+s"(bid2));
  const unsigned jw2 = bid2 >> 3, base2 = (bid2 & 7) * 666 + ((bid2 & 7) ? 1 : 0);
  const unsigned nW2 = 20 + ((640 + jw2 < 666 + ((bid2 & 7) == 0 ? 1 : 0)) ? 1 : 0);
  {
    const unsigned n3 = (c.l16 >> 4) & 3;
    c.q0 = n3 == 0; c.q1 = n3 == 1; c.q2 = n3 == 2; c.lo2 = n3 < 2;
  }
  if (__builtin_expect(badmask != 0, 0)) {
#pragma unroll 1
    for (unsigned rho = 0; rho < 3; ++rho) {
      if (!((badmask >> rho) & 1)) continue;
#pragma unroll
      for (int t = 0; t < 3; ++t) { OPQ(c.cA[t]); OPQ(c.cB[t]); }
      OPQ(c.cW); OPQ(c.l16); OPQ(c.l16o); OPQ(c.pw); OPQ(c.pr);
      const unsigned wi = win_index(base2, jw2, rho, wv2), r = wi / NW1, cc = wi % NW1;
      const unsigned wbs = (7 * r * IMG + 7 * cc) * PIXB;
      const unsigned bms = (r == 0 ? 1u : 0u) | (r == NW1 - 1 ? 2u : 0u) | (cc == 0 ? 4u : 0u) | (cc == NW1 - 1 ? 8u : 0u);
      const unsigned m0s = lds0 + L_SLAB + wv2 * SLABB;
      f32x4 xa[3][6];
      asm volatile("s_waitcnt vmcnt(0)" ::: "memory");
      dma_slab_tab(c, m0s, rx, wbs, bms);
      asm volatile("s_waitcnt vmcnt(0)" ::: "memory");
      conv_slab<0>(c, xa);
      dma_slab_tab(c, m0s, rx, wbs + 64, bms);
      asm volatile("s_waitcnt vmcnt(0)" ::: "memory");
      conv_slab<1>(c, xa);
      dma_slab_tab(c, m0s, rx, wbs + 128, bms);
      asm volatile("s_waitcnt vmcnt(0)" ::: "memory");
      conv_slab<2>(c, xa);
      u32x4 hp[3][3], Op[3][3], x0p[3][3];
      layer_norm<3>(c, xa, x0p, hp);
      const unsigned rec = L_REC + (rho * 8 + wv2) * RECB;
      const unsigned lnn = (c.l16 >> 4) & 15, lgg = c.l16 >> 8;
      Rec rc;
      rc.k3 = rec + lgg * 16;
      rc.v3 = lgg == 0 ? rec + 128 + lnn * 2 : L_ZERO;
      unsigned oL[4];
      head_safe<0>(c, rc, rc.k3 + 64, hp, Op, oL);
      head_safe<1>(c, rc, rc.k3 + 64, hp, Op, oL);
      head_safe<2>(c, rc, rc.k3 + 64, hp, Op, oL);
      head_safe<3>(c, rc, rc.k3 + 64, hp, Op, oL);
      const unsigned oa = rec + 64 + (lnn >> 2) * 16 + (lnn & 3) * 2;
      const unsigned ob = rec + 64 + (lnn >> 2) * 16 + 8 + (lnn & 1) * 2;
      const unsigned obe = c.lo2 ? ob : L_JUNK, obo = c.lo2 ? L_JUNK : ob;
#pragma unroll
      for (int h = 0; h < 4; ++h) {
        ldw16(oa + h * 192, oL[h]);
        ldw16(((h & 1) ? obo : obe) + h * 192, oL[h] >> 16);
      }
      CBAR();
      if (c.l16 == 0) __hip_atomic_fetch_add((AS3 unsigned*)ldsp(F_EPA + 4 * (rho ? 1 : 0)), 1u, __ATOMIC_RELAXED, __HIP_MEMORY_SCOPE_WORKGROUP);
      CBAR();
      const u32x4 t2 = ldr128(c.l16o + TABO + 2048);
#pragma unroll
      for (int p = 0; p < 3; ++p) {
        u32x4 o[3];
        proj_pair<3>(c, p, x0p, Op, o);
#pragma unroll
        for (int t = 0; t < 3; ++t) __builtin_amdgcn_raw_buffer_store_b128(o[t], rout, t2[1 + t] + p * 64, wbs, 0);
      }
    }
  }

  if (wv2 >= 6) {
    const unsigned lt = 7 - wv2;
    OPQ(c.l16);
    const unsigned n = (c.l16 >> 4) & 15, g = c.l16 >> 8;
    const unsigned lwl = 8 * lt + n;
    const bool lvalid = lwl < (lt ? nW2 : 8);
    const unsigned wi = win_index(base2, jw2, lwl >> 3, lwl & 7);
    const unsigned lpix = ((7 * (wi / NW1) + LEFT_I) * IMG + 7 * (wi % NW1) + LEFT_J) * PIXB;
    spin_until(F_EPA + 4 * lt, lt == 0 ? 8u : nW2 - 8);
    const unsigned rec = L_REC + (lvalid ? lwl : 8 * lt) * RECB;
    u32x4 oh[4], xp[1][3];
#pragma unroll
    for (int hd = 0; hd < 4; ++hd) oh[hd] = ldr128(rec + hd * 192 + 64 + g * 16);
#pragma unroll
    for (int s = 0; s < 3; ++s) xp[0][s] = ldr128(rec + 768 + g * 48 + s * 16);
    const u32x4 O3[1][3] = {{u32x4{oh[0][0], oh[0][1], oh[0][2], oh[1][0]}, u32x4{oh[1][1], oh[1][2], oh[2][0], oh[2][1]}, u32x4{oh[2][2], oh[3][0], oh[3][1], oh[3][2]}}};
#pragma unroll
    for (int p = 0; p < 3; ++p) {
      u32x4 o[1];
      proj_pair<1>(c, p, xp, O3, o);
      __builtin_amdgcn_raw_buffer_store_b128(o[0], rout, lvalid ? lpix + g * 16 + p * 64 : 0x80000000u, 0, 0);
    }
  }
  if (((WBL2 >> (4 * wv2)) & 15) == jw2 + 1) asm volatile("buffer_wbl2 sc1\n\ts_waitcnt vmcnt(0)" ::: "memory");
}

extern "C" void kernel_launch(void* const* d_in, const int* in_sizes, int n_in, void* d_out, int out_size, void* d_ws, size_t ws_size, hipStream_t stream) {
  (void)in_sizes; (void)n_in; (void)out_size; (void)d_ws; (void)ws_size;
  wattn_kernel<<<256, 512, 0, stream>>>((const uint16_t*)d_in[0], (const uint16_t*)d_in[1], (const uint16_t*)d_in[2], (const uint16_t*)d_in[3], (uint16_t*)d_out
  );
}
```
